```python
import torch

def _softmax(x, axis=-1):
    return torch.softmax(x, dim=axis)

def _gelu(x):
    return torch.nn.functional.gelu(x, approximate='tanh')

def _silu(x):
    return torch.nn.functional.silu(x)

def _sigmoid(x):
    return torch.sigmoid(x)

def _layernorm(x, axis=-1, eps=1e-05):
    m = x.mean(dim=axis, keepdim=True)
    v = x.var(dim=axis, keepdim=True, unbiased=False)
    return (x - m) / torch.sqrt(v + eps)

def _scatter_add(tgt, idx, src, axis=0):
    return tgt.clone().index_add_(axis, idx, src)

def _segment_sum(src, seg, n):
    out = torch.zeros((n,) + src.shape[1:], dtype=src.dtype)
    return out.index_add_(0, seg, src)

def _pad(x, pw):
    flat = tuple((v for pair in reversed(pw) for v in pair))
    return torch.nn.functional.pad(x, flat)
kernel_name = 'adaLN_cond__mha_block_cached__glu_gated_swiglu__prompt'

def setup_inputs():

    _w = lambda a: (lambda t: t.to(torch.bfloat16) if t.is_floating_point() else t)(torch.as_tensor(a))

    def _A_setup():
        B, Sq, D = (1, 2048, 512)
        H, Sk, Dh = (32, 2048, 128)
        return {'x': _w((lambda *s: torch.randn(*s, dtype=torch.bfloat16) * 0.1)(B, Sq, D)), 'k_cache': _w((lambda *s: torch.randn(*s, dtype=torch.bfloat16) * 0.8)(B, H, Sk, Dh)), 'v_cache': _w((lambda *s: torch.randn(*s, dtype=torch.bfloat16) * 3.2)(B, H, Sk, Dh)), 'wq': _w((lambda *s: torch.randn(*s, dtype=torch.bfloat16) * 0.8)(D, H * Dh) * D ** (-0.5)), 'wo': _w((lambda *s: torch.randn(*s, dtype=torch.bfloat16) * 3.2)(H * Dh, D) * (H * Dh) ** (-0.5))}
    _A_in = _A_setup()

    def _B_setup():
        B, S, D, F = (1, 2048, 512, 1792)
        return {'x': _w((lambda *s: torch.randn(*s, dtype=torch.bfloat16) * 0.1)(B, S, D)), 'w1': _w((lambda *s: torch.randn(*s, dtype=torch.bfloat16) * 3.2)(D, F) * (2.0 / D) ** 0.5), 'w3': _w((lambda *s: torch.randn(*s, dtype=torch.bfloat16) * 3.2)(D, F) * (2.0 / D) ** 0.5), 'w2': _w((lambda *s: torch.randn(*s, dtype=torch.bfloat16) * 3.2)(F, D) * (2.0 / F) ** 0.5)}
    _B_in = _B_setup()
    return {'A_x': _A_in['x'], 'A_k_cache': _A_in['k_cache'], 'A_v_cache': _A_in['v_cache'], 'A_wq': _A_in['wq'], 'A_wo': _A_in['wo'], 'B_x': _B_in['x'], 'B_w1': _B_in['w1'], 'B_w3': _B_in['w3'], 'B_w2': _B_in['w2'], 'c': _w((lambda *s: torch.randn(*s, dtype=torch.bfloat16) * 0.8)(1, 512) * 8.0), 'w_mod': _w((lambda *s: torch.randn(*s, dtype=torch.bfloat16) * 0.8)(512, 6 * 512) * 512 ** (-0.5) * 8.0)}

def reference(A_x, A_k_cache, A_v_cache, A_wq, A_wo, B_x, B_w1, B_w3, B_w2, c, w_mod):

    def _A_ref(x, k_cache, v_cache, wq, wo):
        B, Sq, D = x.shape
        H, Dh = (32, 128)
        q = (lambda x, *d: x.permute(*d))((x @ wq).reshape(B, Sq, H, Dh), 0, 2, 1, 3)
        scores = torch.einsum('bhqd,bhkd->bhqk', q, k_cache) * Dh ** (-0.5)
        p = _softmax(scores, axis=-1)
        o = torch.einsum('bhqk,bhkd->bhqd', p, v_cache)
        return (lambda x, *d: x.permute(*d))(o, 0, 2, 1, 3).reshape(B, Sq, H * Dh) @ wo

    def _B_ref(x, w1, w3, w2):
        gate = torch.einsum('bsd,df->bsf', x, w1)
        up = torch.einsum('bsd,df->bsf', x, w3)
        h = _silu(gate) * up
        out = torch.einsum('bsf,fd->bsd', h, w2)
        return out
    x = A_x
    mod = (_silu(c) @ w_mod).reshape(c.shape[0], 6, 1, -1)
    sa, ca, ga, sb, cb, gb = (mod[:, 0].clone(), mod[:, 1].clone(), mod[:, 2].clone(), mod[:, 3].clone(), mod[:, 4].clone(), mod[:, 5].clone())
    h = _layernorm(x, axis=-1) * (1 + ca) + sa
    x = x + ga * _A_ref(h, A_k_cache, A_v_cache, A_wq, A_wo)
    h = _layernorm(x, axis=-1) * (1 + cb) + sb
    x = x + gb * _B_ref(h, B_w1, B_w3, B_w2)
    return x
```

```cpp
#include <hip/hip_runtime.h>
#include <hip/hip_bf16.h>
typedef unsigned short u16;
__device__ __forceinline__ float bf2f(u16 b){ return __uint_as_float(((unsigned)b)<<16); }
__device__ __forceinline__ u16 f2bf(float f){ unsigned u=__float_as_uint(f); u += 0x7FFFu + ((u>>16)&1u); return (u16)(u>>16); }

constexpr int S=2048, D=512, H=32, DH=128, F=1792;

__global__ void k_mod(const u16* c, const u16* w, float* mod){
  int n = blockIdx.x*blockDim.x+threadIdx.x; if(n>=3072) return;
  float acc=0; for(int k=0;k<512;k++){ float x=bf2f(c[k]); float s=x/(1.f+__expf(-x)); s=bf2f(f2bf(s)); acc+=s*bf2f(w[k*3072+n]); }
  mod[n]=bf2f(f2bf(acc));
}
__global__ void k_ln(const u16* x, const float* shift, const float* scale, u16* h){
  int r=blockIdx.x; int t=threadIdx.x; __shared__ float red[256];
  float a=bf2f(x[r*D+t]), b=bf2f(x[r*D+256+t]);
  red[t]=a+b; __syncthreads(); for(int s=128;s>0;s>>=1){ if(t<s) red[t]+=red[t+s]; __syncthreads(); }
  float m=red[0]/D; __syncthreads();
  red[t]=(a-m)*(a-m)+(b-m)*(b-m); __syncthreads(); for(int s=128;s>0;s>>=1){ if(t<s) red[t]+=red[t+s]; __syncthreads(); }
  float v=red[0]/D; float rs=rsqrtf(v+1e-5f);
  h[r*D+t]=f2bf((a-m)*rs*(1.f+scale[t])+shift[t]);
  h[r*D+256+t]=f2bf((b-m)*rs*(1.f+scale[256+t])+shift[256+t]);
}
template<int MODE>
__global__ void k_gemm(const u16* A, const u16* B, u16* C, int M,int N,int K, const u16* res, const float* gate, const u16* B2){
  __shared__ float sa[16][17], sb[16][17], sb2[16][17];
  int tx=threadIdx.x, ty=threadIdx.y; int row=blockIdx.y*16+ty, col=blockIdx.x*16+tx; float acc=0, acc2=0;
  for(int k0=0;k0<K;k0+=16){ sa[ty][tx]=bf2f(A[(size_t)row*K+k0+tx]); sb[ty][tx]=bf2f(B[(size_t)(k0+ty)*N+col]); if(MODE==2) sb2[ty][tx]=bf2f(B2[(size_t)(k0+ty)*N+col]); __syncthreads();
    for(int k=0;k<16;k++){ acc+=sa[ty][k]*sb[k][tx]; if(MODE==2) acc2+=sa[ty][k]*sb2[k][tx]; } __syncthreads(); }
  if(MODE==0) C[(size_t)row*N+col]=f2bf(acc);
  if(MODE==1) C[(size_t)row*N+col]=f2bf(bf2f(res[(size_t)row*N+col])+gate[col]*acc);
  if(MODE==2){ float s=acc/(1.f+__expf(-acc)); C[(size_t)row*N+col]=f2bf(s*acc2); }
}
__global__ void k_attn(const u16* Q, const u16* K, const u16* V, u16* O){
  int q=blockIdx.x, h=blockIdx.y, t=threadIdx.x; __shared__ float qs[DH]; __shared__ float sc[S]; __shared__ float red[128];
  qs[t]=bf2f(Q[(size_t)q*H*DH+h*DH+t]); __syncthreads();
  float mx=-1e30f;
  for(int k=t;k<S;k+=128){ const u16* kp=K+((size_t)h*S+k)*DH; float a=0; for(int d=0;d<DH;d++) a+=qs[d]*bf2f(kp[d]); a*=0.08838834764f; sc[k]=a; mx=fmaxf(mx,a);}
  red[t]=mx; __syncthreads(); for(int s=64;s>0;s>>=1){ if(t<s) red[t]=fmaxf(red[t],red[t+s]); __syncthreads(); } mx=red[0]; __syncthreads();
  float sum=0; for(int k=t;k<S;k+=128){ float e=__expf(sc[k]-mx); sc[k]=e; sum+=e; }
  red[t]=sum; __syncthreads(); for(int s=64;s>0;s>>=1){ if(t<s) red[t]+=red[t+s]; __syncthreads(); } sum=red[0];
  float o=0; for(int k=0;k<S;k++) o+=sc[k]*bf2f(V[((size_t)h*S+k)*DH+t]);
  O[(size_t)q*H*DH+h*DH+t]=f2bf(o/sum);
}
extern "C" void kernel_launch(void* const* d_in, const int* in_sizes, int n_in, void* d_out, int out_size, void* d_ws, size_t ws_size, hipStream_t stream){
  const u16 *x=(const u16*)d_in[0], *kc=(const u16*)d_in[1], *vc=(const u16*)d_in[2], *wq=(const u16*)d_in[3], *wo=(const u16*)d_in[4];
  const u16 *w1=(const u16*)d_in[6], *w3=(const u16*)d_in[7], *w2=(const u16*)d_in[8], *c=(const u16*)d_in[9], *wm=(const u16*)d_in[10];
  char* ws=(char*)d_ws; float* mod=(float*)ws; u16* h1=(u16*)(ws+(1<<20)); u16* q=(u16*)(ws+(4<<20)); u16* o=(u16*)(ws+(24<<20)); u16* x1=(u16*)(ws+(44<<20)); u16* h2=(u16*)(ws+(48<<20)); u16* g=(u16*)(ws+(52<<20));
  k_mod<<<12,256,0,stream>>>(c,wm,mod);
  k_ln<<<S,256,0,stream>>>(x,mod,mod+512,h1);
  k_gemm<0><<<dim3(4096/16,S/16),dim3(16,16),0,stream>>>(h1,wq,q,S,4096,512,nullptr,nullptr,nullptr);
  k_attn<<<dim3(S,H),128,0,stream>>>(q,kc,vc,o);
  k_gemm<1><<<dim3(512/16,S/16),dim3(16,16),0,stream>>>(o,wo,x1,S,512,4096,x,mod+1024,nullptr);
  k_ln<<<S,256,0,stream>>>(x1,mod+1536,mod+2048,h2);
  k_gemm<2><<<dim3(F/16,S/16),dim3(16,16),0,stream>>>(h2,w1,g,S,F,512,nullptr,nullptr,w3);
  k_gemm<1><<<dim3(512/16,S/16),dim3(16,16),0,stream>>>(g,w2,(u16*)d_out,S,512,F,x1,mod+2560,nullptr);
}
```

```cpp
#include <hip/hip_runtime.h>

extern "C" __global__ __launch_bounds__(256, 1) void k_mega(void* ws, const void* wq, const void* kc, const void* vc, const void* wo, const void* w1, const void* w3, const void* w2, const void* x, void* out, const void* cvec, const void* wmod) {
  unsigned tid = threadIdx.x; unsigned bid = blockIdx.x;
  asm volatile(
    "s_mov_b64 s[86:87], %0\n"
    "s_add_u32 s86, s86, 0x4000\n"
    "s_addc_u32 s87, s87, 0\n"
    "s_load_dword s88, s[86:87], 0x0\n"
    "s_mov_b64 s[52:53], %0\n"
    "s_add_u32 s52, s52, 0x10000\n"
    "s_addc_u32 s53, s53, 0\n"
    "s_mov_b64 s[60:61], %0\n"
    "s_add_u32 s60, s60, 0x0\n"
    "s_addc_u32 s61, s61, 0\n"
    "s_mov_b64 s[56:57], %0\n"
    "s_add_u32 s56, s56, 0x220000\n"
    "s_addc_u32 s57, s57, 0\n"
    "s_mov_b64 s[80:81], %0\n"
    "s_add_u32 s80, s80, 0x8000000\n"
    "s_addc_u32 s81, s81, 0\n"
    "s_mov_b64 s[48:49], %1\n"
    "s_mov_b64 s[40:41], %2\n"
    "s_mov_b64 s[44:45], %3\n"
    "s_mov_b32 s73, %10\n"
    "v_mov_b32 v0, %11\n"
    "s_mov_b32 s98, %10\n"
    "v_and_b32 v1, 63, v0\n"
    "v_lshrrev_b32 v20, 6, v0\n"
    "s_nop 0\n"
    "v_readfirstlane_b32 s64, v20\n"
    "s_and_b32 s74, s73, 7\n"
    "s_lshr_b32 s75, s73, 3\n"
    "s_and_b32 s66, s75, 7\n"
    "s_lshr_b32 s75, s75, 3\n"
    "s_lshl_b32 s74, s74, 2\n"
    "s_add_u32 s65, s74, s75\n"
    "s_nop 1\n"
    "s_and_b32 s73, s98, 7\n"
    "s_lshl_b32 s73, s73, 2\n"
    "s_lshr_b32 s74, s98, 6\n"
    "s_add_u32 s91, s73, s74\n"
    "s_lshl_b32 s73, s91, 3\n"
    "s_lshl_b32 s74, s66, 8\n"
    "s_add_u32 s90, s73, s74\n"
    "s_lshl_b32 s74, s64, 1\n"
    "s_add_u32 s90, s90, s74\n"
    "s_mov_b64 s[92:93], %8\n"
    "s_and_b32 s93, s93, 0xffff\n"
    "s_mov_b32 s94, 0x200000\n"
    "s_mov_b32 s95, 0x20000\n"
    "v_lshlrev_b32 v124, 4, v1\n"
    "s_lshl_b32 s73, s90, 10\n"
    "buffer_load_dwordx4 v[128:131], v124, s[92:95], s73 offen offset:0\n"
    "buffer_load_dwordx4 v[132:135], v124, s[92:95], s73 offen offset:1024\n"
    "s_and_b32 s73, s98, 7\n"
    "s_lshr_b32 s74, s98, 5\n"
    "s_lshl_b32 s89, s73, 1\n"
    "s_add_u32 s89, s89, s74\n"
    "s_cmp_lt_u32 s74, 2\n"
    "s_cbranch_scc1 .Lgv1\n"
    "s_lshl_b32 s89, s73, 2\n"
    "s_add_u32 s89, s89, s74\n"
    "s_add_u32 s89, s89, 14\n"
    "s_cmp_lt_u32 s74, 6\n"
    "s_cbranch_scc1 .Lgv1\n"
    "s_mov_b32 s89, 48\n"
    ".Lgv1:\n"
    "s_mov_b64 s[92:93], %13\n"
    "s_and_b32 s93, s93, 0xffff\n"
    "s_mov_b32 s94, 0x300000\n"
    "s_mov_b32 s95, 0x20000\n"
    "s_lshr_b32 s73, s98, 3\n"
    "s_and_b32 s73, s73, 3\n"
    "s_lshl_b32 s73, s73, 5\n"
    "s_lshl_b32 s74, s89, 7\n"
    "s_add_u32 s73, s73, s74\n"
    "v_and_b32 v125, 1, v0\n"
    "v_lshlrev_b32 v125, 4, v125\n"
    "v_lshrrev_b32 v126, 1, v0\n"
    "v_mul_u32_u24 v127, 0x1800, v126\n"
    "v_add_u32 v125, v125, v127\n"
    "s_cmp_lt_u32 s89, 48\n"
    "s_cselect_b32 s73, s73, 0x7ff00000\n"
    "buffer_load_dwordx4 v[160:163], v125, s[92:95], s73 offen offset:0\n"
    "s_add_u32 s73, s73, 0xc0000\n"
    "buffer_load_dwordx4 v[164:167], v125, s[92:95], s73 offen offset:0\n"
    "s_add_u32 s73, s73, 0xc0000\n"
    "buffer_load_dwordx4 v[168:171], v125, s[92:95], s73 offen offset:0\n"
    "s_add_u32 s73, s73, 0xc0000\n"
    "buffer_load_dwordx4 v[172:175], v125, s[92:95], s73 offen offset:0\n"
    "s_add_u32 s73, s73, 0xc0000\n"
    "s_mov_b64 s[92:93], %12\n"
    "s_and_b32 s93, s93, 0xffff\n"
    "s_mov_b32 s94, 0x400\n"
    "s_mov_b32 s95, 0x20000\n"
    "v_lshlrev_b32 v126, 1, v126\n"
    "buffer_load_ushort v176, v126, s[92:95], 0 offen offset:0\n"
    "buffer_load_ushort v177, v126, s[92:95], 0 offen offset:256\n"
    "buffer_load_ushort v178, v126, s[92:95], 0 offen offset:512\n"
    "buffer_load_ushort v179, v126, s[92:95], 0 offen offset:768\n"
    "s_and_b32 s73, s98, 7\n"
    "s_lshr_b32 s74, s98, 3\n"
    "s_lshr_b32 s70, s73, 2\n"
    "s_lshl_b32 s70, s70, 3\n"
    "s_and_b32 s75, s74, 7\n"
    "s_add_u32 s70, s70, s75\n"
    "s_and_b32 s79, s73, 3\n"
    "s_lshl_b32 s79, s79, 2\n"
    "s_lshr_b32 s75, s74, 3\n"
    "s_add_u32 s79, s79, s75\n"
    "s_getpc_b64 s[92:93]\n"
    ".Lmega_pc:\n"
    "s_sub_u32 s92, s92, .Lmega_pc-k_mega\n"
    "s_subb_u32 s93, s93, 0\n"
    "s_and_b32 s93, s93, 0xffff\n"
    "s_mov_b32 s94, .Lmega_code_end-k_mega\n"
    "s_mov_b32 s95, 0x20000\n"
    "s_lshr_b32 s73, s98, 3\n"
    "s_lshl_b32 s73, s73, 2\n"
    "s_add_u32 s73, s73, s64\n"
    "s_lshl_b32 s73, s73, 10\n"
    "buffer_load_dwordx4 v[248:251], v124, s[92:95], s73 offen offset:0\n"
    "s_lshl_b32 s74, s65, 19\n"
    "s_add_u32 s40, s40, s74\n"
    "s_addc_u32 s41, s41, 0\n"
    "s_add_u32 s44, s44, s74\n"
    "s_addc_u32 s45, s45, 0\n"
    "s_and_b32 s41, s41, 0xffff\n"
    "s_mov_b32 s42, 0x80000\n"
    "s_mov_b32 s43, 0x20000\n"
    "s_and_b32 s45, s45, 0xffff\n"
    "s_mov_b32 s46, 0x80000\n"
    "s_mov_b32 s47, 0x20000\n"
    "s_lshl_b32 s74, s65, 8\n"
    "s_add_u32 s48, s48, s74\n"
    "s_addc_u32 s49, s49, 0\n"
    "s_and_b32 s49, s49, 0xffff\n"
    "s_sub_u32 s50, 0x400000, s74\n"
    "s_mov_b32 s51, 0x20000\n"
    "s_lshl_b32 s74, s66, 3\n"
    "s_lshl_b32 s75, s64, 1\n"
    "s_add_u32 s74, s74, s75\n"
    "s_lshl_b32 s75, s74, 15\n"
    "s_add_u32 s52, s52, s75\n"
    "s_addc_u32 s53, s53, 0\n"
    "s_and_b32 s53, s53, 0xffff\n"
    "s_mov_b32 s54, 0x10000\n"
    "s_mov_b32 s55, 0x20000\n"
    "s_lshl_b32 s75, s74, 18\n"
    "s_add_u32 s56, s56, s75\n"
    "s_addc_u32 s57, s57, 0\n"
    "s_lshl_b32 s75, s65, 13\n"
    "s_add_u32 s56, s56, s75\n"
    "s_addc_u32 s57, s57, 0\n"
    "s_and_b32 s57, s57, 0xffff\n"
    "s_mov_b32 s58, 0x42000\n"
    "s_mov_b32 s59, 0x20000\n"
    "s_and_b32 s61, s61, 0xffff\n"
    "s_mov_b32 s62, 0x1000\n"
    "s_mov_b32 s63, 0x20000\n"
    "s_lshl_b32 s69, s64, 12\n"
    "s_mov_b32 s76, 0x1000\n"
    "s_mov_b32 s72, 0x5d800000\n"
    "v_lshlrev_b32 v9, 4, v1\n"
    "v_add_u32 v10, 0x8000, v9\n"
    "v_lshrrev_b32 v20, 4, v1\n"
    "v_and_b32 v22, 3, v1\n"
    "v_bfe_u32 v21, v1, 2, 2\n"
    "v_lshlrev_b32 v21, 13, v21\n"
    "v_lshl_add_u32 v21, v20, 6, v21\n"
    "v_lshl_add_u32 v2, v22, 4, v21\n"
    "v_add_u32 v3, 0x7c00, v2\n"
    "v_add_u32 v4, 0xf800, v2\n"
    "v_add_u32 v5, 0x17400, v2\n"
    "v_bfe_u32 v21, v1, 2, 2\n"
    "v_lshlrev_b32 v21, 8, v21\n"
    "v_lshl_add_u32 v21, v20, 6, v21\n"
    "v_lshl_add_u32 v15, v22, 4, v21\n"
    "v_and_b32 v21, 15, v1\n"
    "v_xor_b32 v21, v21, v20\n"
    "v_xor_b32 v22, 0, v21\n"
    "v_lshlrev_b32 v22, 4, v22\n"
    "v_lshl_add_u32 v11, v20, 8, v22\n"
    "v_xor_b32 v22, 4, v21\n"
    "v_lshlrev_b32 v22, 4, v22\n"
    "v_lshl_add_u32 v12, v20, 8, v22\n"
    "v_xor_b32 v22, 8, v21\n"
    "v_lshlrev_b32 v22, 4, v22\n"
    "v_lshl_add_u32 v13, v20, 8, v22\n"
    "v_xor_b32 v22, 12, v21\n"
    "v_lshlrev_b32 v22, 4, v22\n"
    "v_lshl_add_u32 v14, v20, 8, v22\n"
    "v_lshrrev_b32 v20, 5, v1\n"
    "v_lshlrev_b32 v20, 11, v20\n"
    "v_bfe_u32 v21, v1, 2, 2\n"
    "v_lshl_add_u32 v20, v21, 6, v20\n"
    "v_bfe_u32 v21, v1, 4, 1\n"
    "v_lshl_add_u32 v20, v21, 5, v20\n"
    "v_and_b32 v21, 1, v1\n"
    "v_lshl_add_u32 v20, v21, 4, v20\n"
    "v_bfe_u32 v21, v1, 1, 1\n"
    "v_lshl_add_u32 v6, v21, 3, v20\n"
    "v_lshrrev_b32 v20, 5, v1\n"
    "v_lshlrev_b32 v20, 5, v20\n"
    "v_add_u32 v8, 0x24000, v20\n"
    "s_waitcnt lgkmcnt(0)\n"
    "s_add_u32 s88, s88, 1\n"
    "s_waitcnt vmcnt(9)\n"
    "v_mov_b32 v132, v132\n"
    "v_lshlrev_b32 v64, 16, v128\n"
    "v_and_b32 v65, 0xffff0000, v128\n"
    "v_lshlrev_b32 v66, 16, v129\n"
    "v_and_b32 v67, 0xffff0000, v129\n"
    "v_lshlrev_b32 v68, 16, v130\n"
    "v_and_b32 v69, 0xffff0000, v130\n"
    "v_lshlrev_b32 v70, 16, v131\n"
    "v_and_b32 v71, 0xffff0000, v131\n"
    "v_add_f32 v80, v64, v65\n"
    "v_add_f32 v80, v80, v66\n"
    "v_add_f32 v80, v80, v67\n"
    "v_add_f32 v80, v80, v68\n"
    "v_add_f32 v80, v80, v69\n"
    "v_add_f32 v80, v80, v70\n"
    "v_add_f32 v80, v80, v71\n"
    "v_lshlrev_b32 v72, 16, v132\n"
    "v_and_b32 v73, 0xffff0000, v132\n"
    "v_lshlrev_b32 v74, 16, v133\n"
    "v_and_b32 v75, 0xffff0000, v133\n"
    "v_lshlrev_b32 v76, 16, v134\n"
    "v_and_b32 v77, 0xffff0000, v134\n"
    "v_lshlrev_b32 v78, 16, v135\n"
    "v_and_b32 v79, 0xffff0000, v135\n"
    "v_add_f32 v81, v72, v73\n"
    "v_add_f32 v81, v81, v74\n"
    "v_add_f32 v81, v81, v75\n"
    "v_add_f32 v81, v81, v76\n"
    "v_add_f32 v81, v81, v77\n"
    "v_add_f32 v81, v81, v78\n"
    "v_add_f32 v81, v81, v79\n"
    "s_nop 1\n"
    "s_nop 1\n"
    "v_add_f32_dpp v80, v80, v80 row_ror:8 row_mask:0xf bank_mask:0xf\n"
    "v_add_f32_dpp v81, v81, v81 row_ror:8 row_mask:0xf bank_mask:0xf\n"
    "s_nop 1\n"
    "s_nop 1\n"
    "v_add_f32_dpp v80, v80, v80 row_ror:4 row_mask:0xf bank_mask:0xf\n"
    "v_add_f32_dpp v81, v81, v81 row_ror:4 row_mask:0xf bank_mask:0xf\n"
    "s_nop 1\n"
    "s_nop 1\n"
    "v_add_f32_dpp v80, v80, v80 row_ror:2 row_mask:0xf bank_mask:0xf\n"
    "v_add_f32_dpp v81, v81, v81 row_ror:2 row_mask:0xf bank_mask:0xf\n"
    "s_nop 1\n"
    "s_nop 1\n"
    "v_add_f32_dpp v80, v80, v80 row_ror:1 row_mask:0xf bank_mask:0xf\n"
    "v_add_f32_dpp v81, v81, v81 row_ror:1 row_mask:0xf bank_mask:0xf\n"
    "v_mov_b32 v82, v80\n"
    "v_mov_b32 v83, v81\n"
    "s_nop 1\n"
    "s_nop 1\n"
    "v_permlane16_swap_b32 v80, v82\n"
    "v_permlane16_swap_b32 v81, v83\n"
    "v_add_f32 v80, v80, v82\n"
    "v_add_f32 v81, v81, v83\n"
    "v_mov_b32 v82, v80\n"
    "v_mov_b32 v83, v81\n"
    "s_nop 1\n"
    "s_nop 1\n"
    "v_permlane32_swap_b32 v80, v82\n"
    "v_permlane32_swap_b32 v81, v83\n"
    "v_add_f32 v80, v80, v82\n"
    "v_add_f32 v81, v81, v83\n"
    "v_mul_f32 v80, 0x3b000000, v80\n"
    "v_mul_f32 v81, 0x3b000000, v81\n"
    "v_sub_f32 v64, v64, v80\n"
    "v_sub_f32 v65, v65, v80\n"
    "v_sub_f32 v66, v66, v80\n"
    "v_sub_f32 v67, v67, v80\n"
    "v_sub_f32 v68, v68, v80\n"
    "v_sub_f32 v69, v69, v80\n"
    "v_sub_f32 v70, v70, v80\n"
    "v_sub_f32 v71, v71, v80\n"
    "v_mul_f32 v80, v64, v64\n"
    "v_fmac_f32 v80, v65, v65\n"
    "v_fmac_f32 v80, v66, v66\n"
    "v_fmac_f32 v80, v67, v67\n"
    "v_fmac_f32 v80, v68, v68\n"
    "v_fmac_f32 v80, v69, v69\n"
    "v_fmac_f32 v80, v70, v70\n"
    "v_fmac_f32 v80, v71, v71\n"
    "v_sub_f32 v72, v72, v81\n"
    "v_sub_f32 v73, v73, v81\n"
    "v_sub_f32 v74, v74, v81\n"
    "v_sub_f32 v75, v75, v81\n"
    "v_sub_f32 v76, v76, v81\n"
    "v_sub_f32 v77, v77, v81\n"
    "v_sub_f32 v78, v78, v81\n"
    "v_sub_f32 v79, v79, v81\n"
    "v_mul_f32 v81, v72, v72\n"
    "v_fmac_f32 v81, v73, v73\n"
    "v_fmac_f32 v81, v74, v74\n"
    "v_fmac_f32 v81, v75, v75\n"
    "v_fmac_f32 v81, v76, v76\n"
    "v_fmac_f32 v81, v77, v77\n"
    "v_fmac_f32 v81, v78, v78\n"
    "v_fmac_f32 v81, v79, v79\n"
    "s_mov_b64 s[92:93], %0\n"
    "s_add_u32 s92, s92, 0x10000\n"
    "s_addc_u32 s93, s93, 0\n"
    "s_and_b32 s93, s93, 0xffff\n"
    "s_mov_b32 s94, 0x200000\n"
    "s_mov_b32 s95, 0x20000\n"
    "v_lshrrev_b32 v84, 1, v1\n"
    "v_lshlrev_b32 v84, 10, v84\n"
    "v_and_b32 v85, 1, v1\n"
    "v_lshl_add_u32 v84, v85, 9, v84\n"
    "s_nop 1\n"
    "s_nop 1\n"
    "v_add_f32_dpp v80, v80, v80 row_ror:8 row_mask:0xf bank_mask:0xf\n"
    "v_add_f32_dpp v81, v81, v81 row_ror:8 row_mask:0xf bank_mask:0xf\n"
    "s_nop 1\n"
    "s_nop 1\n"
    "v_add_f32_dpp v80, v80, v80 row_ror:4 row_mask:0xf bank_mask:0xf\n"
    "v_add_f32_dpp v81, v81, v81 row_ror:4 row_mask:0xf bank_mask:0xf\n"
    "s_nop 1\n"
    "s_nop 1\n"
    "v_add_f32_dpp v80, v80, v80 row_ror:2 row_mask:0xf bank_mask:0xf\n"
    "v_add_f32_dpp v81, v81, v81 row_ror:2 row_mask:0xf bank_mask:0xf\n"
    "s_nop 1\n"
    "s_nop 1\n"
    "v_add_f32_dpp v80, v80, v80 row_ror:1 row_mask:0xf bank_mask:0xf\n"
    "v_add_f32_dpp v81, v81, v81 row_ror:1 row_mask:0xf bank_mask:0xf\n"
    "v_mov_b32 v82, v80\n"
    "v_mov_b32 v83, v81\n"
    "s_nop 1\n"
    "s_nop 1\n"
    "v_permlane16_swap_b32 v80, v82\n"
    "v_permlane16_swap_b32 v81, v83\n"
    "v_add_f32 v80, v80, v82\n"
    "v_add_f32 v81, v81, v83\n"
    "v_mov_b32 v82, v80\n"
    "v_mov_b32 v83, v81\n"
    "s_nop 1\n"
    "s_nop 1\n"
    "v_permlane32_swap_b32 v80, v82\n"
    "v_permlane32_swap_b32 v81, v83\n"
    "v_add_f32 v80, v80, v82\n"
    "v_add_f32 v81, v81, v83\n"
    "v_mov_b32 v82, 0x3727c5ac\n"
    "v_fmac_f32 v82, 0x3b000000, v80\n"
    "v_rsq_f32 v80, v82\n"
    "s_nop 0\n"
    "v_mul_f32 v64, v64, v80\n"
    "v_mul_f32 v65, v65, v80\n"
    "v_mul_f32 v66, v66, v80\n"
    "v_mul_f32 v67, v67, v80\n"
    "v_mul_f32 v68, v68, v80\n"
    "v_mul_f32 v69, v69, v80\n"
    "v_mul_f32 v70, v70, v80\n"
    "v_mul_f32 v71, v71, v80\n"
    "v_cvt_pk_f16_f32 v88, v64, v65\n"
    "v_cvt_pk_f16_f32 v89, v66, v67\n"
    "v_cvt_pk_f16_f32 v90, v68, v69\n"
    "v_cvt_pk_f16_f32 v91, v70, v71\n"
    "s_add_u32 s73, s90, 0\n"
    "s_lshr_b32 s74, s73, 5\n"
    "s_lshl_b32 s74, s74, 15\n"
    "s_and_b32 s73, s73, 31\n"
    "s_lshl_b32 s73, s73, 4\n"
    "s_add_u32 s73, s73, s74\n"
    "buffer_store_dwordx4 v[88:91], v84, s[92:95], s73 offen offset:0 sc1\n"
    "v_mov_b32 v83, 0x3727c5ac\n"
    "v_fmac_f32 v83, 0x3b000000, v81\n"
    "v_rsq_f32 v81, v83\n"
    "s_nop 0\n"
    "v_mul_f32 v72, v72, v81\n"
    "v_mul_f32 v73, v73, v81\n"
    "v_mul_f32 v74, v74, v81\n"
    "v_mul_f32 v75, v75, v81\n"
    "v_mul_f32 v76, v76, v81\n"
    "v_mul_f32 v77, v77, v81\n"
    "v_mul_f32 v78, v78, v81\n"
    "v_mul_f32 v79, v79, v81\n"
    "v_cvt_pk_f16_f32 v92, v72, v73\n"
    "v_cvt_pk_f16_f32 v93, v74, v75\n"
    "v_cvt_pk_f16_f32 v94, v76, v77\n"
    "v_cvt_pk_f16_f32 v95, v78, v79\n"
    "s_add_u32 s73, s90, 1\n"
    "s_lshr_b32 s74, s73, 5\n"
    "s_lshl_b32 s74, s74, 15\n"
    "s_and_b32 s73, s73, 31\n"
    "s_lshl_b32 s73, s73, 4\n"
    "s_add_u32 s73, s73, s74\n"
    "buffer_store_dwordx4 v[92:95], v84, s[92:95], s73 offen offset:0 sc1\n"
    "s_waitcnt vmcnt(6)\n"
    "v_lshlrev_b32 v176, 16, v176\n"
    "v_mul_f32 v112, 0xbfb8aa3b, v176\n"
    "v_exp_f32 v112, v112\n"
    "s_nop 0\n"
    "v_add_f32 v112, 0x3f800000, v112\n"
    "v_rcp_f32 v112, v112\n"
    "s_nop 0\n"
    "v_mul_f32 v176, v176, v112\n"
    "v_cvt_pk_bf16_f32 v176, v176, v176\n"
    "v_lshlrev_b32 v176, 16, v176\n"
    "s_waitcnt vmcnt(5)\n"
    "v_lshlrev_b32 v177, 16, v177\n"
    "v_mul_f32 v113, 0xbfb8aa3b, v177\n"
    "v_exp_f32 v113, v113\n"
    "s_nop 0\n"
    "v_add_f32 v113, 0x3f800000, v113\n"
    "v_rcp_f32 v113, v113\n"
    "s_nop 0\n"
    "v_mul_f32 v177, v177, v113\n"
    "v_cvt_pk_bf16_f32 v177, v177, v177\n"
    "v_lshlrev_b32 v177, 16, v177\n"
    "s_waitcnt vmcnt(4)\n"
    "v_lshlrev_b32 v178, 16, v178\n"
    "v_mul_f32 v114, 0xbfb8aa3b, v178\n"
    "v_exp_f32 v114, v114\n"
    "s_nop 0\n"
    "v_add_f32 v114, 0x3f800000, v114\n"
    "v_rcp_f32 v114, v114\n"
    "s_nop 0\n"
    "v_mul_f32 v178, v178, v114\n"
    "v_cvt_pk_bf16_f32 v178, v178, v178\n"
    "v_lshlrev_b32 v178, 16, v178\n"
    "s_waitcnt vmcnt(3)\n"
    "v_lshlrev_b32 v179, 16, v179\n"
    "v_mul_f32 v115, 0xbfb8aa3b, v179\n"
    "v_exp_f32 v115, v115\n"
    "s_nop 0\n"
    "v_add_f32 v115, 0x3f800000, v115\n"
    "v_rcp_f32 v115, v115\n"
    "s_nop 0\n"
    "v_mul_f32 v179, v179, v115\n"
    "v_cvt_pk_bf16_f32 v179, v179, v179\n"
    "v_lshlrev_b32 v179, 16, v179\n"
    "v_lshlrev_b32 v112, 16, v160\n"
    "v_and_b32 v113, 0xffff0000, v160\n"
    "v_mul_f32 v240, v112, v176\n"
    "v_mul_f32 v241, v113, v176\n"
    "v_lshlrev_b32 v112, 16, v161\n"
    "v_and_b32 v113, 0xffff0000, v161\n"
    "v_mul_f32 v242, v112, v176\n"
    "v_mul_f32 v243, v113, v176\n"
    "v_lshlrev_b32 v112, 16, v162\n"
    "v_and_b32 v113, 0xffff0000, v162\n"
    "v_mul_f32 v244, v112, v176\n"
    "v_mul_f32 v245, v113, v176\n"
    "v_lshlrev_b32 v112, 16, v163\n"
    "v_and_b32 v113, 0xffff0000, v163\n"
    "v_mul_f32 v246, v112, v176\n"
    "v_mul_f32 v247, v113, v176\n"
    "v_lshlrev_b32 v112, 16, v164\n"
    "v_and_b32 v113, 0xffff0000, v164\n"
    "v_fmac_f32 v240, v112, v177\n"
    "v_fmac_f32 v241, v113, v177\n"
    "v_lshlrev_b32 v112, 16, v165\n"
    "v_and_b32 v113, 0xffff0000, v165\n"
    "v_fmac_f32 v242, v112, v177\n"
    "v_fmac_f32 v243, v113, v177\n"
    "v_lshlrev_b32 v112, 16, v166\n"
    "v_and_b32 v113, 0xffff0000, v166\n"
    "v_fmac_f32 v244, v112, v177\n"
    "v_fmac_f32 v245, v113, v177\n"
    "v_lshlrev_b32 v112, 16, v167\n"
    "v_and_b32 v113, 0xffff0000, v167\n"
    "v_fmac_f32 v246, v112, v177\n"
    "v_fmac_f32 v247, v113, v177\n"
    "v_lshlrev_b32 v112, 16, v168\n"
    "v_and_b32 v113, 0xffff0000, v168\n"
    "v_fmac_f32 v240, v112, v178\n"
    "v_fmac_f32 v241, v113, v178\n"
    "v_lshlrev_b32 v112, 16, v169\n"
    "v_and_b32 v113, 0xffff0000, v169\n"
    "v_fmac_f32 v242, v112, v178\n"
    "v_fmac_f32 v243, v113, v178\n"
    "v_lshlrev_b32 v112, 16, v170\n"
    "v_and_b32 v113, 0xffff0000, v170\n"
    "v_fmac_f32 v244, v112, v178\n"
    "v_fmac_f32 v245, v113, v178\n"
    "v_lshlrev_b32 v112, 16, v171\n"
    "v_and_b32 v113, 0xffff0000, v171\n"
    "v_fmac_f32 v246, v112, v178\n"
    "v_fmac_f32 v247, v113, v178\n"
    "v_lshlrev_b32 v112, 16, v172\n"
    "v_and_b32 v113, 0xffff0000, v172\n"
    "v_fmac_f32 v240, v112, v179\n"
    "v_fmac_f32 v241, v113, v179\n"
    "v_lshlrev_b32 v112, 16, v173\n"
    "v_and_b32 v113, 0xffff0000, v173\n"
    "v_fmac_f32 v242, v112, v179\n"
    "v_fmac_f32 v243, v113, v179\n"
    "v_lshlrev_b32 v112, 16, v174\n"
    "v_and_b32 v113, 0xffff0000, v174\n"
    "v_fmac_f32 v244, v112, v179\n"
    "v_fmac_f32 v245, v113, v179\n"
    "v_lshlrev_b32 v112, 16, v175\n"
    "v_and_b32 v113, 0xffff0000, v175\n"
    "v_fmac_f32 v246, v112, v179\n"
    "v_fmac_f32 v247, v113, v179\n"
    "s_nop 1\n"
    "v_add_f32_dpp v240, v240, v240 quad_perm:[2,3,0,1] row_mask:0xf bank_mask:0xf\n"
    "v_add_f32_dpp v241, v241, v241 quad_perm:[2,3,0,1] row_mask:0xf bank_mask:0xf\n"
    "v_add_f32_dpp v242, v242, v242 quad_perm:[2,3,0,1] row_mask:0xf bank_mask:0xf\n"
    "v_add_f32_dpp v243, v243, v243 quad_perm:[2,3,0,1] row_mask:0xf bank_mask:0xf\n"
    "v_add_f32_dpp v244, v244, v244 quad_perm:[2,3,0,1] row_mask:0xf bank_mask:0xf\n"
    "v_add_f32_dpp v245, v245, v245 quad_perm:[2,3,0,1] row_mask:0xf bank_mask:0xf\n"
    "v_add_f32_dpp v246, v246, v246 quad_perm:[2,3,0,1] row_mask:0xf bank_mask:0xf\n"
    "v_add_f32_dpp v247, v247, v247 quad_perm:[2,3,0,1] row_mask:0xf bank_mask:0xf\n"
    "s_nop 1\n"
    "v_add_f32_dpp v240, v240, v240 row_ror:4 row_mask:0xf bank_mask:0xf\n"
    "v_add_f32_dpp v241, v241, v241 row_ror:4 row_mask:0xf bank_mask:0xf\n"
    "v_add_f32_dpp v242, v242, v242 row_ror:4 row_mask:0xf bank_mask:0xf\n"
    "v_add_f32_dpp v243, v243, v243 row_ror:4 row_mask:0xf bank_mask:0xf\n"
    "v_add_f32_dpp v244, v244, v244 row_ror:4 row_mask:0xf bank_mask:0xf\n"
    "v_add_f32_dpp v245, v245, v245 row_ror:4 row_mask:0xf bank_mask:0xf\n"
    "v_add_f32_dpp v246, v246, v246 row_ror:4 row_mask:0xf bank_mask:0xf\n"
    "v_add_f32_dpp v247, v247, v247 row_ror:4 row_mask:0xf bank_mask:0xf\n"
    "s_nop 1\n"
    "v_add_f32_dpp v240, v240, v240 row_ror:8 row_mask:0xf bank_mask:0xf\n"
    "v_add_f32_dpp v241, v241, v241 row_ror:8 row_mask:0xf bank_mask:0xf\n"
    "v_add_f32_dpp v242, v242, v242 row_ror:8 row_mask:0xf bank_mask:0xf\n"
    "v_add_f32_dpp v243, v243, v243 row_ror:8 row_mask:0xf bank_mask:0xf\n"
    "v_add_f32_dpp v244, v244, v244 row_ror:8 row_mask:0xf bank_mask:0xf\n"
    "v_add_f32_dpp v245, v245, v245 row_ror:8 row_mask:0xf bank_mask:0xf\n"
    "v_add_f32_dpp v246, v246, v246 row_ror:8 row_mask:0xf bank_mask:0xf\n"
    "v_add_f32_dpp v247, v247, v247 row_ror:8 row_mask:0xf bank_mask:0xf\n"
    "v_mov_b32 v100, v240\n"
    "v_mov_b32 v101, v241\n"
    "v_mov_b32 v102, v242\n"
    "v_mov_b32 v103, v243\n"
    "v_mov_b32 v104, v244\n"
    "v_mov_b32 v105, v245\n"
    "v_mov_b32 v106, v246\n"
    "v_mov_b32 v107, v247\n"
    "s_nop 1\n"
    "v_permlane16_swap_b32 v240, v100\n"
    "v_permlane16_swap_b32 v241, v101\n"
    "v_permlane16_swap_b32 v242, v102\n"
    "v_permlane16_swap_b32 v243, v103\n"
    "v_permlane16_swap_b32 v244, v104\n"
    "v_permlane16_swap_b32 v245, v105\n"
    "v_permlane16_swap_b32 v246, v106\n"
    "v_permlane16_swap_b32 v247, v107\n"
    "v_add_f32 v240, v240, v100\n"
    "v_add_f32 v241, v241, v101\n"
    "v_add_f32 v242, v242, v102\n"
    "v_add_f32 v243, v243, v103\n"
    "v_add_f32 v244, v244, v104\n"
    "v_add_f32 v245, v245, v105\n"
    "v_add_f32 v246, v246, v106\n"
    "v_add_f32 v247, v247, v107\n"
    "v_mov_b32 v100, v240\n"
    "v_mov_b32 v101, v241\n"
    "v_mov_b32 v102, v242\n"
    "v_mov_b32 v103, v243\n"
    "v_mov_b32 v104, v244\n"
    "v_mov_b32 v105, v245\n"
    "v_mov_b32 v106, v246\n"
    "v_mov_b32 v107, v247\n"
    "s_nop 1\n"
    "v_permlane32_swap_b32 v240, v100\n"
    "v_permlane32_swap_b32 v241, v101\n"
    "v_permlane32_swap_b32 v242, v102\n"
    "v_permlane32_swap_b32 v243, v103\n"
    "v_permlane32_swap_b32 v244, v104\n"
    "v_permlane32_swap_b32 v245, v105\n"
    "v_permlane32_swap_b32 v246, v106\n"
    "v_permlane32_swap_b32 v247, v107\n"
    "v_add_f32 v240, v240, v100\n"
    "v_add_f32 v241, v241, v101\n"
    "v_add_f32 v242, v242, v102\n"
    "v_add_f32 v243, v243, v103\n"
    "v_add_f32 v244, v244, v104\n"
    "v_add_f32 v245, v245, v105\n"
    "v_add_f32 v246, v246, v106\n"
    "v_add_f32 v247, v247, v107\n"
    "v_and_b32 v120, 1, v1\n"
    "v_lshlrev_b32 v120, 5, v120\n"
    "s_lshl_b32 s73, s64, 6\n"
    "s_add_u32 s73, s73, 0x20000\n"
    "v_add_u32 v120, s73, v120\n"
    "ds_write_b128 v120, v[240:243] offset:0\n"
    "ds_write_b128 v120, v[244:247] offset:16\n"
    "s_waitcnt lgkmcnt(0)\n"
    "s_barrier\n"
    "v_and_b32 v120, 15, v1\n"
    "v_lshlrev_b32 v120, 2, v120\n"
    "v_add_u32 v121, 0x20000, v120\n"
    "ds_read_b32 v240, v121 offset:0\n"
    "ds_read_b32 v241, v121 offset:64\n"
    "ds_read_b32 v242, v121 offset:128\n"
    "ds_read_b32 v243, v121 offset:192\n"
    "s_waitcnt lgkmcnt(2)\n"
    "v_add_f32 v240, v240, v241\n"
    "s_waitcnt lgkmcnt(0)\n"
    "v_add_f32 v242, v242, v243\n"
    "v_add_f32 v240, v240, v242\n"
    "v_cvt_pk_bf16_f32 v240, v240, v240\n"
    "v_lshlrev_b32 v240, 16, v240\n"
    "s_lshr_b32 s73, s89, 3\n"
    "s_cmp_eq_u32 s73, 1\n"
    "s_cbranch_scc1 .Lgx2a\n"
    "s_cmp_eq_u32 s73, 4\n"
    "s_cbranch_scc0 .Lgx2b\n"
    ".Lgx2a:\n"
    "v_add_f32 v240, 0x3f800000, v240\n"
    "v_cvt_pk_bf16_f32 v240, v240, v240\n"
    "v_lshlrev_b32 v240, 16, v240\n"
    ".Lgx2b:\n"
    "s_nop 0\n"
    "s_cmp_gt_u32 s73, 1\n"
    "s_cbranch_scc1 .Lgx2c\n"
    "v_mul_f32 v240, 0x3e0293ee, v240\n"
    ".Lgx2c:\n"
    "s_mov_b64 s[92:93], %0\n"
    "s_add_u32 s92, s92, 0x0\n"
    "s_addc_u32 s93, s93, 0\n"
    "s_and_b32 s93, s93, 0xffff\n"
    "s_mov_b32 s94, 0x3000\n"
    "s_mov_b32 s95, 0x20000\n"
    "s_lshr_b32 s73, s98, 3\n"
    "s_and_b32 s73, s73, 3\n"
    "s_lshl_b32 s73, s73, 6\n"
    "s_lshl_b32 s74, s89, 8\n"
    "s_add_u32 s73, s73, s74\n"
    "buffer_store_dword v240, v120, s[92:95], s73 offen offset:0 sc1\n"
    "s_cmp_ge_u32 s98, 0x50\n"
    "s_cbranch_scc1 .Lz3\n"
    "s_cmp_lg_u32 s64, 0\n"
    "s_cbranch_scc1 .Lz3\n"
    "s_add_u32 s92, s86, 0xb00\n"
    "s_addc_u32 s93, s87, 0\n"
    "s_lshl_b32 s99, s98, 8\n"
    "s_add_u32 s92, s92, s99\n"
    "s_addc_u32 s93, s93, 0\n"
    "v_mov_b32 v120, 0\n"
    "s_mov_b64 exec, 1\n"
    "global_store_dword v120, v120, s[92:93] sc1\n"
    "s_mov_b64 exec, -1\n"
    ".Lz3:\n"
    "s_add_u32 s92, s86, 0x100\n"
    "s_addc_u32 s93, s87, 0\n"
    "s_lshl_b32 s99, s66, 8\n"
    "s_add_u32 s92, s92, s99\n"
    "s_addc_u32 s93, s93, 0\n"
    "s_lshl_b32 s99, s91, 2\n"
    "s_add_u32 s92, s92, s99\n"
    "s_addc_u32 s93, s93, 0\n"
    "s_waitcnt vmcnt(0)\n"
    "s_barrier\n"
    "s_cmp_lg_u32 s64, 0\n"
    "s_cbranch_scc1 .Lfl4\n"
    "s_lshr_b32 s73, s98, 3\n"
    "s_and_b32 s73, s73, 3\n"
    "s_lshl_b32 s74, s89, 2\n"
    "s_add_u32 s73, s73, s74\n"
    "s_lshl_b32 s73, s73, 2\n"
    "s_add_u32 s73, s73, 0x5c00\n"
    "s_lshl_b32 s74, s66, 8\n"
    "s_lshl_b32 s75, s91, 2\n"
    "s_add_u32 s74, s74, s75\n"
    "s_add_u32 s74, s74, 0x100\n"
    "v_lshlrev_b32 v120, 8, v1\n"
    "v_add_u32 v120, s73, v120\n"
    "v_mov_b32 v121, s74\n"
    "v_cmp_gt_u32 vcc, 8, v1\n"
    "s_nop 2\n"
    "s_cmp_lt_u32 s89, 16\n"
    "s_cselect_b64 vcc, vcc, 0\n"
    "s_nop 1\n"
    "v_cndmask_b32 v120, v121, v120, vcc\n"
    "v_mov_b32 v121, s88\n"
    "s_mov_b64 exec, 0x1ff\n"
    "global_store_dword v120, v121, s[86:87] sc1\n"
    "s_mov_b64 exec, -1\n"
    ".Lfl4:\n"
    "s_cmp_eq_u32 s64, 0\n"
    "s_cbranch_scc1 .Lpoll5w0\n"
    "s_add_u32 m0, s69, 0x4000\n"
    "s_lshl_b32 s78, s64, 17\n"
    "s_add_u32 s78, s78, 0\n"
    "buffer_load_dwordx4 v2, s[48:51], s78 offen offset:0 lds\n"
    "buffer_load_dwordx4 v3, s[48:51], s78 offen offset:1024 lds\n"
    "buffer_load_dwordx4 v4, s[48:51], s78 offen offset:2048 lds\n"
    "buffer_load_dwordx4 v5, s[48:51], s78 offen offset:3072 lds\n"
    "s_add_u32 m0, s69, 0x8000\n"
    "s_lshl_b32 s78, s64, 17\n"
    "s_add_u32 s78, s78, 0x80000\n"
    "buffer_load_dwordx4 v2, s[48:51], s78 offen offset:0 lds\n"
    "buffer_load_dwordx4 v3, s[48:51], s78 offen offset:1024 lds\n"
    "buffer_load_dwordx4 v4, s[48:51], s78 offen offset:2048 lds\n"
    "buffer_load_dwordx4 v5, s[48:51], s78 offen offset:3072 lds\n"
    "s_add_u32 m0, s69, 0x10000\n"
    "s_lshl_b32 s78, s64, 17\n"
    "s_add_u32 s78, s78, 0x100000\n"
    "buffer_load_dwordx4 v2, s[48:51], s78 offen offset:0 lds\n"
    "buffer_load_dwordx4 v3, s[48:51], s78 offen offset:1024 lds\n"
    "buffer_load_dwordx4 v4, s[48:51], s78 offen offset:2048 lds\n"
    "buffer_load_dwordx4 v5, s[48:51], s78 offen offset:3072 lds\n"
    "s_add_u32 m0, s69, 0x14000\n"
    "s_lshl_b32 s78, s64, 17\n"
    "s_add_u32 s78, s78, 0x180000\n"
    "buffer_load_dwordx4 v2, s[48:51], s78 offen offset:0 lds\n"
    "buffer_load_dwordx4 v3, s[48:51], s78 offen offset:1024 lds\n"
    "buffer_load_dwordx4 v4, s[48:51], s78 offen offset:2048 lds\n"
    "buffer_load_dwordx4 v5, s[48:51], s78 offen offset:3072 lds\n"
    "s_add_u32 s67, s69, 0\n"
    "s_add_u32 m0, s69, 0\n"
    "s_nop 0\n"
    "buffer_load_dwordx4 v11, s[40:43], s67 offen offset:0 lds\n"
    "buffer_load_dwordx4 v12, s[40:43], s67 offen offset:1024 lds\n"
    "buffer_load_dwordx4 v13, s[40:43], s67 offen offset:2048 lds\n"
    "buffer_load_dwordx4 v14, s[40:43], s67 offen offset:3072 lds\n"
    "s_barrier\n"
    "s_branch .Lpoll5x\n"
    ".Lpoll5w0:\n"
    "s_and_b32 s89, s98, 7\n"
    "v_min_u32 v116, 31, v1\n"
    "v_lshlrev_b32 v116, 2, v116\n"
    "s_lshl_b32 s99, s66, 8\n"
    "s_add_u32 s99, s99, 0x100\n"
    "v_add_u32 v116, s99, v116\n"
    "v_min_u32 v117, 63, v1\n"
    "v_lshlrev_b32 v117, 2, v117\n"
    "s_lshl_b32 s99, s89, 8\n"
    "s_add_u32 s99, s99, 0x5c00\n"
    "v_add_u32 v117, s99, v117\n"
    "s_mov_b32 s94, 0\n"
    ".Lpoll5:\n"
    "global_load_dword v120, v116, s[86:87] sc1\n"
    "global_load_dword v121, v117, s[86:87] sc1\n"
    "s_waitcnt vmcnt(0)\n"
    "v_cmp_eq_u32 vcc, s88, v120\n"
    "v_cmp_eq_u32 s[84:85], s88, v121\n"
    "s_nop 1\n"
    "s_and_b64 vcc, vcc, s[84:85]\n"
    "s_nop 3\n"
    "s_cmp_eq_u64 vcc, -1\n"
    "s_cbranch_scc1 .Lpoll5d\n"
    "s_add_u32 s94, s94, 1\n"
    "s_cmp_gt_u32 s94, 400000\n"
    "s_cbranch_scc1 .Lpoll5d\n"
    "s_sleep 1\n"
    "s_branch .Lpoll5\n"
    ".Lpoll5d:\n"
    "s_barrier\n"
    "s_add_u32 m0, s69, 0x4000\n"
    "s_lshl_b32 s78, s64, 17\n"
    "s_add_u32 s78, s78, 0\n"
    "buffer_load_dwordx4 v2, s[48:51], s78 offen offset:0 lds\n"
    "buffer_load_dwordx4 v3, s[48:51], s78 offen offset:1024 lds\n"
    "buffer_load_dwordx4 v4, s[48:51], s78 offen offset:2048 lds\n"
    "buffer_load_dwordx4 v5, s[48:51], s78 offen offset:3072 lds\n"
    "s_add_u32 m0, s69, 0x8000\n"
    "s_lshl_b32 s78, s64, 17\n"
    "s_add_u32 s78, s78, 0x80000\n"
    "buffer_load_dwordx4 v2, s[48:51], s78 offen offset:0 lds\n"
    "buffer_load_dwordx4 v3, s[48:51], s78 offen offset:1024 lds\n"
    "buffer_load_dwordx4 v4, s[48:51], s78 offen offset:2048 lds\n"
    "buffer_load_dwordx4 v5, s[48:51], s78 offen offset:3072 lds\n"
    "s_add_u32 m0, s69, 0x10000\n"
    "s_lshl_b32 s78, s64, 17\n"
    "s_add_u32 s78, s78, 0x100000\n"
    "buffer_load_dwordx4 v2, s[48:51], s78 offen offset:0 lds\n"
    "buffer_load_dwordx4 v3, s[48:51], s78 offen offset:1024 lds\n"
    "buffer_load_dwordx4 v4, s[48:51], s78 offen offset:2048 lds\n"
    "buffer_load_dwordx4 v5, s[48:51], s78 offen offset:3072 lds\n"
    "s_add_u32 m0, s69, 0x14000\n"
    "s_lshl_b32 s78, s64, 17\n"
    "s_add_u32 s78, s78, 0x180000\n"
    "buffer_load_dwordx4 v2, s[48:51], s78 offen offset:0 lds\n"
    "buffer_load_dwordx4 v3, s[48:51], s78 offen offset:1024 lds\n"
    "buffer_load_dwordx4 v4, s[48:51], s78 offen offset:2048 lds\n"
    "buffer_load_dwordx4 v5, s[48:51], s78 offen offset:3072 lds\n"
    "s_add_u32 s67, s69, 0\n"
    "s_add_u32 m0, s69, 0\n"
    "s_nop 0\n"
    "buffer_load_dwordx4 v11, s[40:43], s67 offen offset:0 lds\n"
    "buffer_load_dwordx4 v12, s[40:43], s67 offen offset:1024 lds\n"
    "buffer_load_dwordx4 v13, s[40:43], s67 offen offset:2048 lds\n"
    "buffer_load_dwordx4 v14, s[40:43], s67 offen offset:3072 lds\n"
    ".Lpoll5x:\n"
    "s_lshl_b32 s74, s64, 10\n"
    "buffer_load_dwordx4 v[48:51], v9, s[60:63], s74 offen offset:0 sc1\n"
    "s_mov_b32 s77, 0\n"
    "buffer_load_dwordx4 v[64:67], v9, s[52:55], s77 offen offset:0 sc1\n"
    "buffer_load_dwordx4 v[68:71], v10, s[52:55], s77 offen offset:0 sc1\n"
    "buffer_load_dwordx4 v[72:75], v9, s[52:55], s77 offen offset:1024 sc1\n"
    "buffer_load_dwordx4 v[76:79], v10, s[52:55], s77 offen offset:1024 sc1\n"
    "buffer_load_dwordx4 v[80:83], v9, s[52:55], s77 offen offset:2048 sc1\n"
    "buffer_load_dwordx4 v[84:87], v10, s[52:55], s77 offen offset:2048 sc1\n"
    "buffer_load_dwordx4 v[88:91], v9, s[52:55], s77 offen offset:3072 sc1\n"
    "buffer_load_dwordx4 v[92:95], v10, s[52:55], s77 offen offset:3072 sc1\n"
    "s_mov_b32 s77, 0x1000\n"
    "buffer_load_dwordx4 v[96:99], v9, s[52:55], s77 offen offset:0 sc1\n"
    "buffer_load_dwordx4 v[100:103], v10, s[52:55], s77 offen offset:0 sc1\n"
    "buffer_load_dwordx4 v[104:107], v9, s[52:55], s77 offen offset:1024 sc1\n"
    "buffer_load_dwordx4 v[108:111], v10, s[52:55], s77 offen offset:1024 sc1\n"
    "buffer_load_dwordx4 v[112:115], v9, s[52:55], s77 offen offset:2048 sc1\n"
    "buffer_load_dwordx4 v[116:119], v10, s[52:55], s77 offen offset:2048 sc1\n"
    "buffer_load_dwordx4 v[120:123], v9, s[52:55], s77 offen offset:3072 sc1\n"
    "buffer_load_dwordx4 v[124:127], v10, s[52:55], s77 offen offset:3072 sc1\n"
    "s_lshl_b32 s74, s64, 10\n"
    "s_add_u32 s74, s74, 0x24000\n"
    "v_add_u32 v52, s74, v9\n"
    "s_waitcnt vmcnt(16)\n"
    "ds_write_b128 v52, v[48:51] offset:0\n"
    "s_waitcnt lgkmcnt(0)\n"
    "s_barrier\n"
    "v_add_u32 v7, 0x4000, v6\n"
    "ds_read_b128 v[16:19], v8 offset:0\n"
    "ds_read_b128 v[20:23], v8 offset:16\n"
    "ds_read_b128 v[24:27], v8 offset:2048\n"
    "ds_read_b128 v[28:31], v8 offset:2064\n"
    "ds_read_b128 v[32:35], v8 offset:64\n"
    "ds_read_b128 v[36:39], v8 offset:80\n"
    "ds_read_b128 v[40:43], v8 offset:2112\n"
    "ds_read_b128 v[44:47], v8 offset:2128\n"
    "ds_read_b64_tr_b16 a[192:193], v7 offset:0\n"
    "ds_read_b64_tr_b16 a[194:195], v7 offset:1024\n"
    "ds_read_b64_tr_b16 a[196:197], v7 offset:256\n"
    "ds_read_b64_tr_b16 a[198:199], v7 offset:1280\n"
    "ds_read_b64_tr_b16 a[200:201], v7 offset:512\n"
    "ds_read_b64_tr_b16 a[202:203], v7 offset:1536\n"
    "ds_read_b64_tr_b16 a[204:205], v7 offset:768\n"
    "ds_read_b64_tr_b16 a[206:207], v7 offset:1792\n"
    "s_waitcnt lgkmcnt(13)\n"
    "s_waitcnt vmcnt(15)\n"
    "v_fma_mix_f32 v48, v64, v24, v16 op_sel:[0,0,0] op_sel_hi:[1,0,0]\n"
    "v_fma_mix_f32 v49, v64, v25, v17 op_sel:[1,0,0] op_sel_hi:[1,0,0]\n"
    "v_fma_mix_f32 v50, v65, v26, v18 op_sel:[0,0,0] op_sel_hi:[1,0,0]\n"
    "v_fma_mix_f32 v51, v65, v27, v19 op_sel:[1,0,0] op_sel_hi:[1,0,0]\n"
    "v_cvt_pk_bf16_f32 v64, v48, v49\n"
    "s_waitcnt lgkmcnt(12)\n"
    "v_fma_mix_f32 v52, v66, v28, v20 op_sel:[0,0,0] op_sel_hi:[1,0,0]\n"
    "v_fma_mix_f32 v53, v66, v29, v21 op_sel:[1,0,0] op_sel_hi:[1,0,0]\n"
    "v_cvt_pk_bf16_f32 v65, v50, v51\n"
    "v_fma_mix_f32 v54, v67, v30, v22 op_sel:[0,0,0] op_sel_hi:[1,0,0]\n"
    "v_fma_mix_f32 v55, v67, v31, v23 op_sel:[1,0,0] op_sel_hi:[1,0,0]\n"
    "v_cvt_pk_bf16_f32 v66, v52, v53\n"
    "s_waitcnt vmcnt(14)\n"
    "v_fma_mix_f32 v56, v68, v24, v16 op_sel:[0,0,0] op_sel_hi:[1,0,0]\n"
    "v_fma_mix_f32 v57, v68, v25, v17 op_sel:[1,0,0] op_sel_hi:[1,0,0]\n"
    "v_cvt_pk_bf16_f32 v67, v54, v55\n"
    "v_fma_mix_f32 v58, v69, v26, v18 op_sel:[0,0,0] op_sel_hi:[1,0,0]\n"
    "v_fma_mix_f32 v59, v69, v27, v19 op_sel:[1,0,0] op_sel_hi:[1,0,0]\n"
    "v_cvt_pk_bf16_f32 v68, v56, v57\n"
    "v_fma_mix_f32 v60, v70, v28, v20 op_sel:[0,0,0] op_sel_hi:[1,0,0]\n"
    "v_fma_mix_f32 v61, v70, v29, v21 op_sel:[1,0,0] op_sel_hi:[1,0,0]\n"
    "v_cvt_pk_bf16_f32 v69, v58, v59\n"
    "v_fma_mix_f32 v62, v71, v30, v22 op_sel:[0,0,0] op_sel_hi:[1,0,0]\n"
    "v_fma_mix_f32 v63, v71, v31, v23 op_sel:[1,0,0] op_sel_hi:[1,0,0]\n"
    "v_cvt_pk_bf16_f32 v70, v60, v61\n"
    "v_cvt_pk_bf16_f32 v71, v62, v63\n"
    "s_waitcnt lgkmcnt(6)\n"
    "v_mfma_f32_32x32x16_bf16 v[128:143], a[192:195], v[64:67], 0\n"
    "ds_read_b64_tr_b16 a[208:209], v7 offset:4096\n"
    "s_waitcnt vmcnt(13)\n"
    "v_fma_mix_f32 v48, v72, v40, v32 op_sel:[0,0,0] op_sel_hi:[1,0,0]\n"
    "s_add_u32 m0, s69, 0xc000\n"
    "s_lshl_b32 s78, s64, 17\n"
    "s_add_u32 s78, s78, 0x200000\n"
    "v_mfma_f32_32x32x16_bf16 v[144:159], a[192:195], v[68:71], 0\n"
    "ds_read_b64_tr_b16 a[210:211], v7 offset:5120\n"
    "ds_read_b128 v[16:19], v8 offset:128\n"
    "buffer_load_dwordx4 v2, s[48:51], s78 offen offset:0 lds\n"
    "v_fma_mix_f32 v49, v72, v41, v33 op_sel:[1,0,0] op_sel_hi:[1,0,0]\n"
    "v_fma_mix_f32 v50, v73, v42, v34 op_sel:[0,0,0] op_sel_hi:[1,0,0]\n"
    "v_fma_mix_f32 v51, v73, v43, v35 op_sel:[1,0,0] op_sel_hi:[1,0,0]\n"
    "s_waitcnt lgkmcnt(7)\n"
    "v_mfma_f32_32x32x16_bf16 v[160:175], a[196:199], v[64:67], 0\n"
    "ds_read_b64_tr_b16 a[212:213], v7 offset:4352\n"
    "v_cvt_pk_bf16_f32 v72, v48, v49\n"
    "v_fma_mix_f32 v52, v74, v44, v36 op_sel:[0,0,0] op_sel_hi:[1,0,0]\n"
    "v_fma_mix_f32 v53, v74, v45, v37 op_sel:[1,0,0] op_sel_hi:[1,0,0]\n"
    "v_cvt_pk_bf16_f32 v73, v50, v51\n"
    "v_fma_mix_f32 v54, v75, v46, v38 op_sel:[0,0,0] op_sel_hi:[1,0,0]\n"
    "v_mfma_f32_32x32x16_bf16 v[176:191], a[196:199], v[68:71], 0\n"
    "ds_read_b64_tr_b16 a[214:215], v7 offset:5376\n"
    "ds_read_b128 v[20:23], v8 offset:144\n"
    "buffer_load_dwordx4 v3, s[48:51], s78 offen offset:1024 lds\n"
    "v_fma_mix_f32 v55, v75, v47, v39 op_sel:[1,0,0] op_sel_hi:[1,0,0]\n"
    "v_cvt_pk_bf16_f32 v74, v52, v53\n"
    "s_waitcnt vmcnt(14)\n"
    "v_fma_mix_f32 v56, v76, v40, v32 op_sel:[0,0,0] op_sel_hi:[1,0,0]\n"
    "s_waitcnt lgkmcnt(8)\n"
    "v_mfma_f32_32x32x16_bf16 v[192:207], a[200:203], v[64:67], 0\n"
    "ds_read_b64_tr_b16 a[216:217], v7 offset:4608\n"
    "v_fma_mix_f32 v57, v76, v41, v33 op_sel:[1,0,0] op_sel_hi:[1,0,0]\n"
    "v_cvt_pk_bf16_f32 v75, v54, v55\n"
    "v_fma_mix_f32 v58, v77, v42, v34 op_sel:[0,0,0] op_sel_hi:[1,0,0]\n"
    "v_fma_mix_f32 v59, v77, v43, v35 op_sel:[1,0,0] op_sel_hi:[1,0,0]\n"
    "v_mfma_f32_32x32x16_bf16 v[208:223], a[200:203], v[68:71], 0\n"
    "ds_read_b64_tr_b16 a[218:219], v7 offset:5632\n"
    "ds_read_b128 v[24:27], v8 offset:2176\n"
    "buffer_load_dwordx4 v4, s[48:51], s78 offen offset:2048 lds\n"
    "v_cvt_pk_bf16_f32 v76, v56, v57\n"
    "v_fma_mix_f32 v60, v78, v44, v36 op_sel:[0,0,0] op_sel_hi:[1,0,0]\n"
    "v_fma_mix_f32 v61, v78, v45, v37 op_sel:[1,0,0] op_sel_hi:[1,0,0]\n"
    "s_waitcnt lgkmcnt(9)\n"
    "v_mfma_f32_32x32x16_bf16 v[224:239], a[204:207], v[64:67], 0\n"
    "ds_read_b64_tr_b16 a[220:221], v7 offset:4864\n"
    "buffer_load_dwordx4 v5, s[48:51], s78 offen offset:3072 lds\n"
    "v_cvt_pk_bf16_f32 v77, v58, v59\n"
    "v_fma_mix_f32 v62, v79, v46, v38 op_sel:[0,0,0] op_sel_hi:[1,0,0]\n"
    "v_fma_mix_f32 v63, v79, v47, v39 op_sel:[1,0,0] op_sel_hi:[1,0,0]\n"
    "v_cvt_pk_bf16_f32 v78, v60, v61\n"
    "v_mfma_f32_32x32x16_bf16 v[240:255], a[204:207], v[68:71], 0\n"
    "ds_read_b64_tr_b16 a[222:223], v7 offset:5888\n"
    "ds_read_b128 v[28:31], v8 offset:2192\n"
    "v_cvt_pk_bf16_f32 v79, v62, v63\n"
    "s_waitcnt lgkmcnt(10)\n"
    "v_mfma_f32_32x32x16_bf16 v[128:143], a[208:211], v[72:75], v[128:143]\n"
    "ds_read_b64_tr_b16 a[192:193], v7 offset:8192\n"
    "s_waitcnt lgkmcnt(4)\n"
    "s_waitcnt vmcnt(15)\n"
    "v_fma_mix_f32 v48, v80, v24, v16 op_sel:[0,0,0] op_sel_hi:[1,0,0]\n"
    "v_fma_mix_f32 v49, v80, v25, v17 op_sel:[1,0,0] op_sel_hi:[1,0,0]\n"
    "v_fma_mix_f32 v50, v81, v26, v18 op_sel:[0,0,0] op_sel_hi:[1,0,0]\n"
    "v_fma_mix_f32 v51, v81, v27, v19 op_sel:[1,0,0] op_sel_hi:[1,0,0]\n"
    "v_mfma_f32_32x32x16_bf16 v[144:159], a[208:211], v[76:79], v[144:159]\n"
    "ds_read_b64_tr_b16 a[194:195], v7 offset:9216\n"
    "ds_read_b128 v[32:35], v8 offset:192\n"
    "v_cvt_pk_bf16_f32 v80, v48, v49\n"
    "s_waitcnt lgkmcnt(3)\n"
    "v_fma_mix_f32 v52, v82, v28, v20 op_sel:[0,0,0] op_sel_hi:[1,0,0]\n"
    "v_fma_mix_f32 v53, v82, v29, v21 op_sel:[1,0,0] op_sel_hi:[1,0,0]\n"
    "v_mfma_f32_32x32x16_bf16 v[160:175], a[212:215], v[72:75], v[160:175]\n"
    "ds_read_b64_tr_b16 a[196:197], v7 offset:8448\n"
    "s_mov_b32 s77, 0x2000\n"
    "buffer_load_dwordx4 v[64:67], v9, s[52:55], s77 offen offset:0 sc1\n"
    "v_cvt_pk_bf16_f32 v81, v50, v51\n"
    "v_fma_mix_f32 v54, v83, v30, v22 op_sel:[0,0,0] op_sel_hi:[1,0,0]\n"
    "v_mfma_f32_32x32x16_bf16 v[176:191], a[212:215], v[76:79], v[176:191]\n"
    "ds_read_b64_tr_b16 a[198:199], v7 offset:9472\n"
    "ds_read_b128 v[36:39], v8 offset:208\n"
    "v_fma_mix_f32 v55, v83, v31, v23 op_sel:[1,0,0] op_sel_hi:[1,0,0]\n"
    "v_cvt_pk_bf16_f32 v82, v52, v53\n"
    "s_waitcnt vmcnt(15)\n"
    "v_fma_mix_f32 v56, v84, v24, v16 op_sel:[0,0,0] op_sel_hi:[1,0,0]\n"
    "v_fma_mix_f32 v57, v84, v25, v17 op_sel:[1,0,0] op_sel_hi:[1,0,0]\n"
    "v_mfma_f32_32x32x16_bf16 v[192:207], a[216:219], v[72:75], v[192:207]\n"
    "ds_read_b64_tr_b16 a[200:201], v7 offset:8704\n"
    "buffer_load_dwordx4 v[68:71], v10, s[52:55], s77 offen offset:0 sc1\n"
    "v_cvt_pk_bf16_f32 v83, v54, v55\n"
    "v_fma_mix_f32 v58, v85, v26, v18 op_sel:[0,0,0] op_sel_hi:[1,0,0]\n"
    "v_fma_mix_f32 v59, v85, v27, v19 op_sel:[1,0,0] op_sel_hi:[1,0,0]\n"
    "v_mfma_f32_32x32x16_bf16 v[208:223], a[216:219], v[76:79], v[208:223]\n"
    "ds_read_b64_tr_b16 a[202:203], v7 offset:9728\n"
    "ds_read_b128 v[40:43], v8 offset:2240\n"
    "v_cvt_pk_bf16_f32 v84, v56, v57\n"
    "v_fma_mix_f32 v60, v86, v28, v20 op_sel:[0,0,0] op_sel_hi:[1,0,0]\n"
    "v_fma_mix_f32 v61, v86, v29, v21 op_sel:[1,0,0] op_sel_hi:[1,0,0]\n"
    "v_mfma_f32_32x32x16_bf16 v[224:239], a[220:223], v[72:75], v[224:239]\n"
    "ds_read_b64_tr_b16 a[204:205], v7 offset:8960\n"
    "v_cvt_pk_bf16_f32 v85, v58, v59\n"
    "v_fma_mix_f32 v62, v87, v30, v22 op_sel:[0,0,0] op_sel_hi:[1,0,0]\n"
    "v_fma_mix_f32 v63, v87, v31, v23 op_sel:[1,0,0] op_sel_hi:[1,0,0]\n"
    "v_cvt_pk_bf16_f32 v86, v60, v61\n"
    "v_mfma_f32_32x32x16_bf16 v[240:255], a[220:223], v[76:79], v[240:255]\n"
    "ds_read_b64_tr_b16 a[206:207], v7 offset:9984\n"
    "ds_read_b128 v[44:47], v8 offset:2256\n"
    "v_cvt_pk_bf16_f32 v87, v62, v63\n"
    "s_waitcnt lgkmcnt(10)\n"
    "v_mfma_f32_32x32x16_bf16 v[128:143], a[192:195], v[80:83], v[128:143]\n"
    "ds_read_b64_tr_b16 a[208:209], v7 offset:12288\n"
    "s_waitcnt lgkmcnt(4)\n"
    "s_waitcnt vmcnt(15)\n"
    "v_fma_mix_f32 v48, v88, v40, v32 op_sel:[0,0,0] op_sel_hi:[1,0,0]\n"
    "v_fma_mix_f32 v49, v88, v41, v33 op_sel:[1,0,0] op_sel_hi:[1,0,0]\n"
    "s_add_u32 m0, s69, 0x18000\n"
    "s_lshl_b32 s78, s64, 17\n"
    "s_add_u32 s78, s78, 0x280000\n"
    "v_mfma_f32_32x32x16_bf16 v[144:159], a[192:195], v[84:87], v[144:159]\n"
    "ds_read_b64_tr_b16 a[210:211], v7 offset:13312\n"
    "ds_read_b128 v[16:19], v8 offset:256\n"
    "buffer_load_dwordx4 v2, s[48:51], s78 offen offset:0 lds\n"
    "v_fma_mix_f32 v50, v89, v42, v34 op_sel:[0,0,0] op_sel_hi:[1,0,0]\n"
    "v_fma_mix_f32 v51, v89, v43, v35 op_sel:[1,0,0] op_sel_hi:[1,0,0]\n"
    "v_cvt_pk_bf16_f32 v88, v48, v49\n"
    "v_mfma_f32_32x32x16_bf16 v[160:175], a[196:199], v[80:83], v[160:175]\n"
    "ds_read_b64_tr_b16 a[212:213], v7 offset:12544\n"
    "s_mov_b32 s77, 0x2000\n"
    "buffer_load_dwordx4 v[72:75], v9, s[52:55], s77 offen offset:1024 sc1\n"
    "s_waitcnt lgkmcnt(4)\n"
    "v_fma_mix_f32 v52, v90, v44, v36 op_sel:[0,0,0] op_sel_hi:[1,0,0]\n"
    "v_fma_mix_f32 v53, v90, v45, v37 op_sel:[1,0,0] op_sel_hi:[1,0,0]\n"
    "v_cvt_pk_bf16_f32 v89, v50, v51\n"
    "v_mfma_f32_32x32x16_bf16 v[176:191], a[196:199], v[84:87], v[176:191]\n"
    "ds_read_b64_tr_b16 a[214:215], v7 offset:13568\n"
    "ds_read_b128 v[20:23], v8 offset:272\n"
    "buffer_load_dwordx4 v3, s[48:51], s78 offen offset:1024 lds\n"
    "v_fma_mix_f32 v54, v91, v46, v38 op_sel:[0,0,0] op_sel_hi:[1,0,0]\n"
    "v_fma_mix_f32 v55, v91, v47, v39 op_sel:[1,0,0] op_sel_hi:[1,0,0]\n"
    "v_cvt_pk_bf16_f32 v90, v52, v53\n"
    "v_mfma_f32_32x32x16_bf16 v[192:207], a[200:203], v[80:83], v[192:207]\n"
    "ds_read_b64_tr_b16 a[216:217], v7 offset:12800\n"
    "buffer_load_dwordx4 v[76:79], v10, s[52:55], s77 offen offset:1024 sc1\n"
    "s_waitcnt vmcnt(18)\n"
    "v_fma_mix_f32 v56, v92, v40, v32 op_sel:[0,0,0] op_sel_hi:[1,0,0]\n"
    "v_fma_mix_f32 v57, v92, v41, v33 op_sel:[1,0,0] op_sel_hi:[1,0,0]\n"
    "v_cvt_pk_bf16_f32 v91, v54, v55\n"
    "v_fma_mix_f32 v58, v93, v42, v34 op_sel:[0,0,0] op_sel_hi:[1,0,0]\n"
    "v_mfma_f32_32x32x16_bf16 v[208:223], a[200:203], v[84:87], v[208:223]\n"
    "ds_read_b64_tr_b16 a[218:219], v7 offset:13824\n"
    "ds_read_b128 v[24:27], v8 offset:2304\n"
    "buffer_load_dwordx4 v4, s[48:51], s78 offen offset:2048 lds\n"
    "v_fma_mix_f32 v59, v93, v43, v35 op_sel:[1,0,0] op_sel_hi:[1,0,0]\n"
    "v_cvt_pk_bf16_f32 v92, v56, v57\n"
    "v_fma_mix_f32 v60, v94, v44, v36 op_sel:[0,0,0] op_sel_hi:[1,0,0]\n"
    "v_mfma_f32_32x32x16_bf16 v[224:239], a[204:207], v[80:83], v[224:239]\n"
    "ds_read_b64_tr_b16 a[220:221], v7 offset:13056\n"
    "buffer_load_dwordx4 v5, s[48:51], s78 offen offset:3072 lds\n"
    "v_fma_mix_f32 v61, v94, v45, v37 op_sel:[1,0,0] op_sel_hi:[1,0,0]\n"
    "v_cvt_pk_bf16_f32 v93, v58, v59\n"
    "v_fma_mix_f32 v62, v95, v46, v38 op_sel:[0,0,0] op_sel_hi:[1,0,0]\n"
    "v_fma_mix_f32 v63, v95, v47, v39 op_sel:[1,0,0] op_sel_hi:[1,0,0]\n"
    "v_mfma_f32_32x32x16_bf16 v[240:255], a[204:207], v[84:87], v[240:255]\n"
    "ds_read_b64_tr_b16 a[222:223], v7 offset:14080\n"
    "ds_read_b128 v[28:31], v8 offset:2320\n"
    "v_cvt_pk_bf16_f32 v94, v60, v61\n"
    "v_cvt_pk_bf16_f32 v95, v62, v63\n"
    "s_waitcnt lgkmcnt(10)\n"
    "v_mfma_f32_32x32x16_bf16 v[128:143], a[208:211], v[88:91], v[128:143]\n"
    "s_barrier\n"
    "v_add_u32 v7, 0x8000, v6\n"
    "ds_read_b64_tr_b16 a[192:193], v7 offset:0\n"
    "s_waitcnt lgkmcnt(4)\n"
    "s_waitcnt vmcnt(19)\n"
    "v_fma_mix_f32 v48, v96, v24, v16 op_sel:[0,0,0] op_sel_hi:[1,0,0]\n"
    "v_fma_mix_f32 v49, v96, v25, v17 op_sel:[1,0,0] op_sel_hi:[1,0,0]\n"
    "v_mfma_f32_32x32x16_bf16 v[144:159], a[208:211], v[92:95], v[144:159]\n"
    "ds_read_b64_tr_b16 a[194:195], v7 offset:1024\n"
    "ds_read_b128 v[32:35], v8 offset:320\n"
    "v_fma_mix_f32 v50, v97, v26, v18 op_sel:[0,0,0] op_sel_hi:[1,0,0]\n"
    "v_fma_mix_f32 v51, v97, v27, v19 op_sel:[1,0,0] op_sel_hi:[1,0,0]\n"
    "v_cvt_pk_bf16_f32 v96, v48, v49\n"
    "s_waitcnt lgkmcnt(3)\n"
    "v_fma_mix_f32 v52, v98, v28, v20 op_sel:[0,0,0] op_sel_hi:[1,0,0]\n"
    "v_mfma_f32_32x32x16_bf16 v[160:175], a[212:215], v[88:91], v[160:175]\n"
    "ds_read_b64_tr_b16 a[196:197], v7 offset:256\n"
    "s_mov_b32 s77, 0x2000\n"
    "buffer_load_dwordx4 v[80:83], v9, s[52:55], s77 offen offset:2048 sc1\n"
    "v_fma_mix_f32 v53, v98, v29, v21 op_sel:[1,0,0] op_sel_hi:[1,0,0]\n"
    "v_cvt_pk_bf16_f32 v97, v50, v51\n"
    "v_mfma_f32_32x32x16_bf16 v[176:191], a[212:215], v[92:95], v[176:191]\n"
    "ds_read_b64_tr_b16 a[198:199], v7 offset:1280\n"
    "ds_read_b128 v[36:39], v8 offset:336\n"
    "v_fma_mix_f32 v54, v99, v30, v22 op_sel:[0,0,0] op_sel_hi:[1,0,0]\n"
    "v_fma_mix_f32 v55, v99, v31, v23 op_sel:[1,0,0] op_sel_hi:[1,0,0]\n"
    "v_cvt_pk_bf16_f32 v98, v52, v53\n"
    "s_waitcnt vmcnt(19)\n"
    "v_fma_mix_f32 v56, v100, v24, v16 op_sel:[0,0,0] op_sel_hi:[1,0,0]\n"
    "v_mfma_f32_32x32x16_bf16 v[192:207], a[216:219], v[88:91], v[192:207]\n"
    "ds_read_b64_tr_b16 a[200:201], v7 offset:512\n"
    "buffer_load_dwordx4 v[84:87], v10, s[52:55], s77 offen offset:2048 sc1\n"
    "v_fma_mix_f32 v57, v100, v25, v17 op_sel:[1,0,0] op_sel_hi:[1,0,0]\n"
    "v_cvt_pk_bf16_f32 v99, v54, v55\n"
    "v_fma_mix_f32 v58, v101, v26, v18 op_sel:[0,0,0] op_sel_hi:[1,0,0]\n"
    "v_mfma_f32_32x32x16_bf16 v[208:223], a[216:219], v[92:95], v[208:223]\n"
    "ds_read_b64_tr_b16 a[202:203], v7 offset:1536\n"
    "ds_read_b128 v[40:43], v8 offset:2368\n"
    "v_fma_mix_f32 v59, v101, v27, v19 op_sel:[1,0,0] op_sel_hi:[1,0,0]\n"
    "v_cvt_pk_bf16_f32 v100, v56, v57\n"
    "v_fma_mix_f32 v60, v102, v28, v20 op_sel:[0,0,0] op_sel_hi:[1,0,0]\n"
    "v_fma_mix_f32 v61, v102, v29, v21 op_sel:[1,0,0] op_sel_hi:[1,0,0]\n"
    "v_mfma_f32_32x32x16_bf16 v[224:239], a[220:223], v[88:91], v[224:239]\n"
    "ds_read_b64_tr_b16 a[204:205], v7 offset:768\n"
    "v_cvt_pk_bf16_f32 v101, v58, v59\n"
    "v_fma_mix_f32 v62, v103, v30, v22 op_sel:[0,0,0] op_sel_hi:[1,0,0]\n"
    "v_fma_mix_f32 v63, v103, v31, v23 op_sel:[1,0,0] op_sel_hi:[1,0,0]\n"
    "v_cvt_pk_bf16_f32 v102, v60, v61\n"
    "v_mfma_f32_32x32x16_bf16 v[240:255], a[220:223], v[92:95], v[240:255]\n"
    "ds_read_b64_tr_b16 a[206:207], v7 offset:1792\n"
    "ds_read_b128 v[44:47], v8 offset:2384\n"
    "v_cvt_pk_bf16_f32 v103, v62, v63\n"
    "s_waitcnt lgkmcnt(10)\n"
    "v_mfma_f32_32x32x16_bf16 v[128:143], a[192:195], v[96:99], v[128:143]\n"
    "ds_read_b64_tr_b16 a[208:209], v7 offset:4096\n"
    "s_waitcnt lgkmcnt(4)\n"
    "s_waitcnt vmcnt(19)\n"
    "v_fma_mix_f32 v48, v104, v40, v32 op_sel:[0,0,0] op_sel_hi:[1,0,0]\n"
    "v_fma_mix_f32 v49, v104, v41, v33 op_sel:[1,0,0] op_sel_hi:[1,0,0]\n"
    "s_add_u32 m0, s69, 0x1c000\n"
    "s_lshl_b32 s78, s64, 17\n"
    "s_add_u32 s78, s78, 0x300000\n"
    "v_mfma_f32_32x32x16_bf16 v[144:159], a[192:195], v[100:103], v[144:159]\n"
    "ds_read_b64_tr_b16 a[210:211], v7 offset:5120\n"
    "ds_read_b128 v[16:19], v8 offset:384\n"
    "buffer_load_dwordx4 v2, s[48:51], s78 offen offset:0 lds\n"
    "v_fma_mix_f32 v50, v105, v42, v34 op_sel:[0,0,0] op_sel_hi:[1,0,0]\n"
    "v_fma_mix_f32 v51, v105, v43, v35 op_sel:[1,0,0] op_sel_hi:[1,0,0]\n"
    "v_cvt_pk_bf16_f32 v104, v48, v49\n"
    "v_mfma_f32_32x32x16_bf16 v[160:175], a[196:199], v[96:99], v[160:175]\n"
    "ds_read_b64_tr_b16 a[212:213], v7 offset:4352\n"
    "s_mov_b32 s77, 0x2000\n"
    "buffer_load_dwordx4 v[88:91], v9, s[52:55], s77 offen offset:3072 sc1\n"
    "s_waitcnt lgkmcnt(4)\n"
    "v_fma_mix_f32 v52, v106, v44, v36 op_sel:[0,0,0] op_sel_hi:[1,0,0]\n"
    "v_fma_mix_f32 v53, v106, v45, v37 op_sel:[1,0,0] op_sel_hi:[1,0,0]\n"
    "v_cvt_pk_bf16_f32 v105, v50, v51\n"
    "v_mfma_f32_32x32x16_bf16 v[176:191], a[196:199], v[100:103], v[176:191]\n"
    "ds_read_b64_tr_b16 a[214:215], v7 offset:5376\n"
    "ds_read_b128 v[20:23], v8 offset:400\n"
    "buffer_load_dwordx4 v3, s[48:51], s78 offen offset:1024 lds\n"
    "v_fma_mix_f32 v54, v107, v46, v38 op_sel:[0,0,0] op_sel_hi:[1,0,0]\n"
    "v_fma_mix_f32 v55, v107, v47, v39 op_sel:[1,0,0] op_sel_hi:[1,0,0]\n"
    "v_cvt_pk_bf16_f32 v106, v52, v53\n"
    "v_mfma_f32_32x32x16_bf16 v[192:207], a[200:203], v[96:99], v[192:207]\n"
    "ds_read_b64_tr_b16 a[216:217], v7 offset:4608\n"
    "buffer_load_dwordx4 v[92:95], v10, s[52:55], s77 offen offset:3072 sc1\n"
    "s_waitcnt vmcnt(22)\n"
    "v_fma_mix_f32 v56, v108, v40, v32 op_sel:[0,0,0] op_sel_hi:[1,0,0]\n"
    "v_fma_mix_f32 v57, v108, v41, v33 op_sel:[1,0,0] op_sel_hi:[1,0,0]\n"
    "v_cvt_pk_bf16_f32 v107, v54, v55\n"
    "v_fma_mix_f32 v58, v109, v42, v34 op_sel:[0,0,0] op_sel_hi:[1,0,0]\n"
    "v_mfma_f32_32x32x16_bf16 v[208:223], a[200:203], v[100:103], v[208:223]\n"
    "ds_read_b64_tr_b16 a[218:219], v7 offset:5632\n"
    "ds_read_b128 v[24:27], v8 offset:2432\n"
    "buffer_load_dwordx4 v4, s[48:51], s78 offen offset:2048 lds\n"
    "v_fma_mix_f32 v59, v109, v43, v35 op_sel:[1,0,0] op_sel_hi:[1,0,0]\n"
    "v_cvt_pk_bf16_f32 v108, v56, v57\n"
    "v_fma_mix_f32 v60, v110, v44, v36 op_sel:[0,0,0] op_sel_hi:[1,0,0]\n"
    "v_mfma_f32_32x32x16_bf16 v[224:239], a[204:207], v[96:99], v[224:239]\n"
    "ds_read_b64_tr_b16 a[220:221], v7 offset:4864\n"
    "buffer_load_dwordx4 v5, s[48:51], s78 offen offset:3072 lds\n"
    "v_fma_mix_f32 v61, v110, v45, v37 op_sel:[1,0,0] op_sel_hi:[1,0,0]\n"
    "v_cvt_pk_bf16_f32 v109, v58, v59\n"
    "v_fma_mix_f32 v62, v111, v46, v38 op_sel:[0,0,0] op_sel_hi:[1,0,0]\n"
    "v_fma_mix_f32 v63, v111, v47, v39 op_sel:[1,0,0] op_sel_hi:[1,0,0]\n"
    "v_mfma_f32_32x32x16_bf16 v[240:255], a[204:207], v[100:103], v[240:255]\n"
    "ds_read_b64_tr_b16 a[222:223], v7 offset:5888\n"
    "ds_read_b128 v[28:31], v8 offset:2448\n"
    "v_cvt_pk_bf16_f32 v110, v60, v61\n"
    "v_cvt_pk_bf16_f32 v111, v62, v63\n"
    "s_waitcnt lgkmcnt(10)\n"
    "v_mfma_f32_32x32x16_bf16 v[128:143], a[208:211], v[104:107], v[128:143]\n"
    "ds_read_b64_tr_b16 a[192:193], v7 offset:8192\n"
    "s_waitcnt lgkmcnt(4)\n"
    "s_waitcnt vmcnt(23)\n"
    "v_fma_mix_f32 v48, v112, v24, v16 op_sel:[0,0,0] op_sel_hi:[1,0,0]\n"
    "v_fma_mix_f32 v49, v112, v25, v17 op_sel:[1,0,0] op_sel_hi:[1,0,0]\n"
    "v_fma_mix_f32 v50, v113, v26, v18 op_sel:[0,0,0] op_sel_hi:[1,0,0]\n"
    "v_fma_mix_f32 v51, v113, v27, v19 op_sel:[1,0,0] op_sel_hi:[1,0,0]\n"
    "v_mfma_f32_32x32x16_bf16 v[144:159], a[208:211], v[108:111], v[144:159]\n"
    "ds_read_b64_tr_b16 a[194:195], v7 offset:9216\n"
    "ds_read_b128 v[32:35], v8 offset:448\n"
    "v_cvt_pk_bf16_f32 v112, v48, v49\n"
    "s_waitcnt lgkmcnt(3)\n"
    "v_fma_mix_f32 v52, v114, v28, v20 op_sel:[0,0,0] op_sel_hi:[1,0,0]\n"
    "v_fma_mix_f32 v53, v114, v29, v21 op_sel:[1,0,0] op_sel_hi:[1,0,0]\n"
    "v_mfma_f32_32x32x16_bf16 v[160:175], a[212:215], v[104:107], v[160:175]\n"
    "ds_read_b64_tr_b16 a[196:197], v7 offset:8448\n"
    "s_mov_b32 s77, 0x3000\n"
    "buffer_load_dwordx4 v[96:99], v9, s[52:55], s77 offen offset:0 sc1\n"
    "v_cvt_pk_bf16_f32 v113, v50, v51\n"
    "v_fma_mix_f32 v54, v115, v30, v22 op_sel:[0,0,0] op_sel_hi:[1,0,0]\n"
    "v_mfma_f32_32x32x16_bf16 v[176:191], a[212:215], v[108:111], v[176:191]\n"
    "ds_read_b64_tr_b16 a[198:199], v7 offset:9472\n"
    "ds_read_b128 v[36:39], v8 offset:464\n"
    "v_fma_mix_f32 v55, v115, v31, v23 op_sel:[1,0,0] op_sel_hi:[1,0,0]\n"
    "v_cvt_pk_bf16_f32 v114, v52, v53\n"
    "s_waitcnt vmcnt(23)\n"
    "v_fma_mix_f32 v56, v116, v24, v16 op_sel:[0,0,0] op_sel_hi:[1,0,0]\n"
    "v_fma_mix_f32 v57, v116, v25, v17 op_sel:[1,0,0] op_sel_hi:[1,0,0]\n"
    "v_mfma_f32_32x32x16_bf16 v[192:207], a[216:219], v[104:107], v[192:207]\n"
    "ds_read_b64_tr_b16 a[200:201], v7 offset:8704\n"
    "buffer_load_dwordx4 v[100:103], v10, s[52:55], s77 offen offset:0 sc1\n"
    "v_cvt_pk_bf16_f32 v115, v54, v55\n"
    "v_fma_mix_f32 v58, v117, v26, v18 op_sel:[0,0,0] op_sel_hi:[1,0,0]\n"
    "v_fma_mix_f32 v59, v117, v27, v19 op_sel:[1,0,0] op_sel_hi:[1,0,0]\n"
    "v_mfma_f32_32x32x16_bf16 v[208:223], a[216:219], v[108:111], v[208:223]\n"
    "ds_read_b64_tr_b16 a[202:203], v7 offset:9728\n"
    "ds_read_b128 v[40:43], v8 offset:2496\n"
    "v_cvt_pk_bf16_f32 v116, v56, v57\n"
    "v_fma_mix_f32 v60, v118, v28, v20 op_sel:[0,0,0] op_sel_hi:[1,0,0]\n"
    "v_fma_mix_f32 v61, v118, v29, v21 op_sel:[1,0,0] op_sel_hi:[1,0,0]\n"
    "v_mfma_f32_32x32x16_bf16 v[224:239], a[220:223], v[104:107], v[224:239]\n"
    "ds_read_b64_tr_b16 a[204:205], v7 offset:8960\n"
    "v_cvt_pk_bf16_f32 v117, v58, v59\n"
    "v_fma_mix_f32 v62, v119, v30, v22 op_sel:[0,0,0] op_sel_hi:[1,0,0]\n"
    "v_fma_mix_f32 v63, v119, v31, v23 op_sel:[1,0,0] op_sel_hi:[1,0,0]\n"
    "v_cvt_pk_bf16_f32 v118, v60, v61\n"
    "v_mfma_f32_32x32x16_bf16 v[240:255], a[220:223], v[108:111], v[240:255]\n"
    "ds_read_b64_tr_b16 a[206:207], v7 offset:9984\n"
    "ds_read_b128 v[44:47], v8 offset:2512\n"
    "v_cvt_pk_bf16_f32 v119, v62, v63\n"
    "s_waitcnt lgkmcnt(10)\n"
    "v_mfma_f32_32x32x16_bf16 v[128:143], a[192:195], v[112:115], v[128:143]\n"
    "ds_read_b64_tr_b16 a[208:209], v7 offset:12288\n"
    "s_waitcnt lgkmcnt(4)\n"
    "s_waitcnt vmcnt(23)\n"
    "v_fma_mix_f32 v48, v120, v40, v32 op_sel:[0,0,0] op_sel_hi:[1,0,0]\n"
    "v_fma_mix_f32 v49, v120, v41, v33 op_sel:[1,0,0] op_sel_hi:[1,0,0]\n"
    "s_add_u32 m0, s69, 0x20000\n"
    "s_lshl_b32 s78, s64, 17\n"
    "s_add_u32 s78, s78, 0x380000\n"
    "v_mfma_f32_32x32x16_bf16 v[144:159], a[192:195], v[116:119], v[144:159]\n"
    "ds_read_b64_tr_b16 a[210:211], v7 offset:13312\n"
    "ds_read_b128 v[16:19], v8 offset:512\n"
    "buffer_load_dwordx4 v2, s[48:51], s78 offen offset:0 lds\n"
    "v_fma_mix_f32 v50, v121, v42, v34 op_sel:[0,0,0] op_sel_hi:[1,0,0]\n"
    "v_fma_mix_f32 v51, v121, v43, v35 op_sel:[1,0,0] op_sel_hi:[1,0,0]\n"
    "v_cvt_pk_bf16_f32 v120, v48, v49\n"
    "v_mfma_f32_32x32x16_bf16 v[160:175], a[196:199], v[112:115], v[160:175]\n"
    "ds_read_b64_tr_b16 a[212:213], v7 offset:12544\n"
    "s_mov_b32 s77, 0x3000\n"
    "buffer_load_dwordx4 v[104:107], v9, s[52:55], s77 offen offset:1024 sc1\n"
    "s_waitcnt lgkmcnt(4)\n"
    "v_fma_mix_f32 v52, v122, v44, v36 op_sel:[0,0,0] op_sel_hi:[1,0,0]\n"
    "v_fma_mix_f32 v53, v122, v45, v37 op_sel:[1,0,0] op_sel_hi:[1,0,0]\n"
    "v_cvt_pk_bf16_f32 v121, v50, v51\n"
    "v_mfma_f32_32x32x16_bf16 v[176:191], a[196:199], v[116:119], v[176:191]\n"
    "ds_read_b64_tr_b16 a[214:215], v7 offset:13568\n"
    "ds_read_b128 v[20:23], v8 offset:528\n"
    "buffer_load_dwordx4 v3, s[48:51], s78 offen offset:1024 lds\n"
    "v_fma_mix_f32 v54, v123, v46, v38 op_sel:[0,0,0] op_sel_hi:[1,0,0]\n"
    "v_fma_mix_f32 v55, v123, v47, v39 op_sel:[1,0,0] op_sel_hi:[1,0,0]\n"
    "v_cvt_pk_bf16_f32 v122, v52, v53\n"
    "v_mfma_f32_32x32x16_bf16 v[192:207], a[200:203], v[112:115], v[192:207]\n"
    "ds_read_b64_tr_b16 a[216:217], v7 offset:12800\n"
    "buffer_load_dwordx4 v[108:111], v10, s[52:55], s77 offen offset:1024 sc1\n"
    "s_waitcnt vmcnt(26)\n"
    "v_fma_mix_f32 v56, v124, v40, v32 op_sel:[0,0,0] op_sel_hi:[1,0,0]\n"
    "v_fma_mix_f32 v57, v124, v41, v33 op_sel:[1,0,0] op_sel_hi:[1,0,0]\n"
    "v_cvt_pk_bf16_f32 v123, v54, v55\n"
    "v_fma_mix_f32 v58, v125, v42, v34 op_sel:[0,0,0] op_sel_hi:[1,0,0]\n"
    "v_mfma_f32_32x32x16_bf16 v[208:223], a[200:203], v[116:119], v[208:223]\n"
    "ds_read_b64_tr_b16 a[218:219], v7 offset:13824\n"
    "ds_read_b128 v[24:27], v8 offset:2560\n"
    "buffer_load_dwordx4 v4, s[48:51], s78 offen offset:2048 lds\n"
    "v_fma_mix_f32 v59, v125, v43, v35 op_sel:[1,0,0] op_sel_hi:[1,0,0]\n"
    "v_cvt_pk_bf16_f32 v124, v56, v57\n"
    "v_fma_mix_f32 v60, v126, v44, v36 op_sel:[0,0,0] op_sel_hi:[1,0,0]\n"
    "v_mfma_f32_32x32x16_bf16 v[224:239], a[204:207], v[112:115], v[224:239]\n"
    "ds_read_b64_tr_b16 a[220:221], v7 offset:13056\n"
    "buffer_load_dwordx4 v5, s[48:51], s78 offen offset:3072 lds\n"
    "v_fma_mix_f32 v61, v126, v45, v37 op_sel:[1,0,0] op_sel_hi:[1,0,0]\n"
    "v_cvt_pk_bf16_f32 v125, v58, v59\n"
    "v_fma_mix_f32 v62, v127, v46, v38 op_sel:[0,0,0] op_sel_hi:[1,0,0]\n"
    "v_fma_mix_f32 v63, v127, v47, v39 op_sel:[1,0,0] op_sel_hi:[1,0,0]\n"
    "v_mfma_f32_32x32x16_bf16 v[240:255], a[204:207], v[116:119], v[240:255]\n"
    "ds_read_b64_tr_b16 a[222:223], v7 offset:14080\n"
    "ds_read_b128 v[28:31], v8 offset:2576\n"
    "v_cvt_pk_bf16_f32 v126, v60, v61\n"
    "v_cvt_pk_bf16_f32 v127, v62, v63\n"
    "s_waitcnt lgkmcnt(10)\n"
    "v_mfma_f32_32x32x16_bf16 v[128:143], a[208:211], v[120:123], v[128:143]\n"
    "s_barrier\n"
    "v_add_u32 v7, 0x10000, v6\n"
    "ds_read_b64_tr_b16 a[192:193], v7 offset:0\n"
    "s_waitcnt lgkmcnt(4)\n"
    "s_waitcnt vmcnt(23)\n"
    "v_fma_mix_f32 v48, v64, v24, v16 op_sel:[0,0,0] op_sel_hi:[1,0,0]\n"
    "v_fma_mix_f32 v49, v64, v25, v17 op_sel:[1,0,0] op_sel_hi:[1,0,0]\n"
    "v_mfma_f32_32x32x16_bf16 v[144:159], a[208:211], v[124:127], v[144:159]\n"
    "ds_read_b64_tr_b16 a[194:195], v7 offset:1024\n"
    "ds_read_b128 v[32:35], v8 offset:576\n"
    "v_fma_mix_f32 v50, v65, v26, v18 op_sel:[0,0,0] op_sel_hi:[1,0,0]\n"
    "v_fma_mix_f32 v51, v65, v27, v19 op_sel:[1,0,0] op_sel_hi:[1,0,0]\n"
    "v_cvt_pk_bf16_f32 v64, v48, v49\n"
    "s_waitcnt lgkmcnt(3)\n"
    "v_fma_mix_f32 v52, v66, v28, v20 op_sel:[0,0,0] op_sel_hi:[1,0,0]\n"
    "v_mfma_f32_32x32x16_bf16 v[160:175], a[212:215], v[120:123], v[160:175]\n"
    "ds_read_b64_tr_b16 a[196:197], v7 offset:256\n"
    "s_mov_b32 s77, 0x3000\n"
    "buffer_load_dwordx4 v[112:115], v9, s[52:55], s77 offen offset:2048 sc1\n"
    "v_fma_mix_f32 v53, v66, v29, v21 op_sel:[1,0,0] op_sel_hi:[1,0,0]\n"
    "v_cvt_pk_bf16_f32 v65, v50, v51\n"
    "v_mfma_f32_32x32x16_bf16 v[176:191], a[212:215], v[124:127], v[176:191]\n"
    "ds_read_b64_tr_b16 a[198:199], v7 offset:1280\n"
    "ds_read_b128 v[36:39], v8 offset:592\n"
    "v_fma_mix_f32 v54, v67, v30, v22 op_sel:[0,0,0] op_sel_hi:[1,0,0]\n"
    "v_fma_mix_f32 v55, v67, v31, v23 op_sel:[1,0,0] op_sel_hi:[1,0,0]\n"
    "v_cvt_pk_bf16_f32 v66, v52, v53\n"
    "s_waitcnt vmcnt(23)\n"
    "v_fma_mix_f32 v56, v68, v24, v16 op_sel:[0,0,0] op_sel_hi:[1,0,0]\n"
    "v_mfma_f32_32x32x16_bf16 v[192:207], a[216:219], v[120:123], v[192:207]\n"
    "ds_read_b64_tr_b16 a[200:201], v7 offset:512\n"
    "buffer_load_dwordx4 v[116:119], v10, s[52:55], s77 offen offset:2048 sc1\n"
    "v_fma_mix_f32 v57, v68, v25, v17 op_sel:[1,0,0] op_sel_hi:[1,0,0]\n"
    "v_cvt_pk_bf16_f32 v67, v54, v55\n"
    "v_fma_mix_f32 v58, v69, v26, v18 op_sel:[0,0,0] op_sel_hi:[1,0,0]\n"
    "v_mfma_f32_32x32x16_bf16 v[208:223], a[216:219], v[124:127], v[208:223]\n"
    "ds_read_b64_tr_b16 a[202:203], v7 offset:1536\n"
    "ds_read_b128 v[40:43], v8 offset:2624\n"
    "v_fma_mix_f32 v59, v69, v27, v19 op_sel:[1,0,0] op_sel_hi:[1,0,0]\n"
    "v_cvt_pk_bf16_f32 v68, v56, v57\n"
    "v_fma_mix_f32 v60, v70, v28, v20 op_sel:[0,0,0] op_sel_hi:[1,0,0]\n"
    "v_fma_mix_f32 v61, v70, v29, v21 op_sel:[1,0,0] op_sel_hi:[1,0,0]\n"
    "v_mfma_f32_32x32x16_bf16 v[224:239], a[220:223], v[120:123], v[224:239]\n"
    "ds_read_b64_tr_b16 a[204:205], v7 offset:768\n"
    "v_cvt_pk_bf16_f32 v69, v58, v59\n"
    "v_fma_mix_f32 v62, v71, v30, v22 op_sel:[0,0,0] op_sel_hi:[1,0,0]\n"
    "v_fma_mix_f32 v63, v71, v31, v23 op_sel:[1,0,0] op_sel_hi:[1,0,0]\n"
    "v_cvt_pk_bf16_f32 v70, v60, v61\n"
    "v_mfma_f32_32x32x16_bf16 v[240:255], a[220:223], v[124:127], v[240:255]\n"
    "ds_read_b64_tr_b16 a[206:207], v7 offset:1792\n"
    "ds_read_b128 v[44:47], v8 offset:2640\n"
    "v_cvt_pk_bf16_f32 v71, v62, v63\n"
    "s_waitcnt lgkmcnt(10)\n"
    "v_mfma_f32_32x32x16_bf16 v[128:143], a[192:195], v[64:67], v[128:143]\n"
    "ds_read_b64_tr_b16 a[208:209], v7 offset:4096\n"
    "s_waitcnt lgkmcnt(4)\n"
    "s_waitcnt vmcnt(22)\n"
    "v_fma_mix_f32 v48, v72, v40, v32 op_sel:[0,0,0] op_sel_hi:[1,0,0]\n"
    "v_fma_mix_f32 v49, v72, v41, v33 op_sel:[1,0,0] op_sel_hi:[1,0,0]\n"
    "v_fma_mix_f32 v50, v73, v42, v34 op_sel:[0,0,0] op_sel_hi:[1,0,0]\n"
    "s_add_u32 s67, s69, 0x4000\n"
    "s_add_u32 m0, s69, 0x4000\n"
    "v_mfma_f32_32x32x16_bf16 v[144:159], a[192:195], v[68:71], v[144:159]\n"
    "ds_read_b64_tr_b16 a[210:211], v7 offset:5120\n"
    "ds_read_b128 v[16:19], v8 offset:640\n"
    "buffer_load_dwordx4 v11, s[40:43], s67 offen offset:0 lds\n"
    "v_fma_mix_f32 v51, v73, v43, v35 op_sel:[1,0,0] op_sel_hi:[1,0,0]\n"
    "v_cvt_pk_bf16_f32 v72, v48, v49\n"
    "s_waitcnt lgkmcnt(3)\n"
    "v_fma_mix_f32 v52, v74, v44, v36 op_sel:[0,0,0] op_sel_hi:[1,0,0]\n"
    "v_mfma_f32_32x32x16_bf16 v[160:175], a[196:199], v[64:67], v[160:175]\n"
    "ds_read_b64_tr_b16 a[212:213], v7 offset:4352\n"
    "s_mov_b32 s77, 0x3000\n"
    "buffer_load_dwordx4 v[120:123], v9, s[52:55], s77 offen offset:3072 sc1\n"
    "v_fma_mix_f32 v53, v74, v45, v37 op_sel:[1,0,0] op_sel_hi:[1,0,0]\n"
    "v_cvt_pk_bf16_f32 v73, v50, v51\n"
    "v_fma_mix_f32 v54, v75, v46, v38 op_sel:[0,0,0] op_sel_hi:[1,0,0]\n"
    "v_mfma_f32_32x32x16_bf16 v[176:191], a[196:199], v[68:71], v[176:191]\n"
    "ds_read_b64_tr_b16 a[214:215], v7 offset:5376\n"
    "ds_read_b128 v[20:23], v8 offset:656\n"
    "buffer_load_dwordx4 v12, s[40:43], s67 offen offset:1024 lds\n"
    "v_fma_mix_f32 v55, v75, v47, v39 op_sel:[1,0,0] op_sel_hi:[1,0,0]\n"
    "v_cvt_pk_bf16_f32 v74, v52, v53\n"
    "s_waitcnt vmcnt(23)\n"
    "v_fma_mix_f32 v56, v76, v40, v32 op_sel:[0,0,0] op_sel_hi:[1,0,0]\n"
    "v_mfma_f32_32x32x16_bf16 v[192:207], a[200:203], v[64:67], v[192:207]\n"
    "ds_read_b64_tr_b16 a[216:217], v7 offset:4608\n"
    "buffer_load_dwordx4 v[124:127], v10, s[52:55], s77 offen offset:3072 sc1\n"
    "v_fma_mix_f32 v57, v76, v41, v33 op_sel:[1,0,0] op_sel_hi:[1,0,0]\n"
    "v_cvt_pk_bf16_f32 v75, v54, v55\n"
    "v_fma_mix_f32 v58, v77, v42, v34 op_sel:[0,0,0] op_sel_hi:[1,0,0]\n"
    "v_fma_mix_f32 v59, v77, v43, v35 op_sel:[1,0,0] op_sel_hi:[1,0,0]\n"
    "v_mfma_f32_32x32x16_bf16 v[208:223], a[200:203], v[68:71], v[208:223]\n"
    "ds_read_b64_tr_b16 a[218:219], v7 offset:5632\n"
    "ds_read_b128 v[24:27], v8 offset:2688\n"
    "buffer_load_dwordx4 v13, s[40:43], s67 offen offset:2048 lds\n"
    "v_cvt_pk_bf16_f32 v76, v56, v57\n"
    "v_fma_mix_f32 v60, v78, v44, v36 op_sel:[0,0,0] op_sel_hi:[1,0,0]\n"
    "v_fma_mix_f32 v61, v78, v45, v37 op_sel:[1,0,0] op_sel_hi:[1,0,0]\n"
    "v_mfma_f32_32x32x16_bf16 v[224:239], a[204:207], v[64:67], v[224:239]\n"
    "ds_read_b64_tr_b16 a[220:221], v7 offset:4864\n"
    "buffer_load_dwordx4 v14, s[40:43], s67 offen offset:3072 lds\n"
    "v_cvt_pk_bf16_f32 v77, v58, v59\n"
    "v_fma_mix_f32 v62, v79, v46, v38 op_sel:[0,0,0] op_sel_hi:[1,0,0]\n"
    "v_fma_mix_f32 v63, v79, v47, v39 op_sel:[1,0,0] op_sel_hi:[1,0,0]\n"
    "v_cvt_pk_bf16_f32 v78, v60, v61\n"
    "v_mfma_f32_32x32x16_bf16 v[240:255], a[204:207], v[68:71], v[240:255]\n"
    "ds_read_b64_tr_b16 a[222:223], v7 offset:5888\n"
    "ds_read_b128 v[28:31], v8 offset:2704\n"
    "v_cvt_pk_bf16_f32 v79, v62, v63\n"
    "s_waitcnt lgkmcnt(10)\n"
    "v_mfma_f32_32x32x16_bf16 v[128:143], a[208:211], v[72:75], v[128:143]\n"
    "ds_read_b64_tr_b16 a[192:193], v7 offset:8192\n"
    "s_waitcnt lgkmcnt(4)\n"
    "s_waitcnt vmcnt(23)\n"
    "v_fma_mix_f32 v48, v80, v24, v16 op_sel:[0,0,0] op_sel_hi:[1,0,0]\n"
    "v_fma_mix_f32 v49, v80, v25, v17 op_sel:[1,0,0] op_sel_hi:[1,0,0]\n"
    "v_fma_mix_f32 v50, v81, v26, v18 op_sel:[0,0,0] op_sel_hi:[1,0,0]\n"
    "v_fma_mix_f32 v51, v81, v27, v19 op_sel:[1,0,0] op_sel_hi:[1,0,0]\n"
    "v_mfma_f32_32x32x16_bf16 v[144:159], a[208:211], v[76:79], v[144:159]\n"
    "ds_read_b64_tr_b16 a[194:195], v7 offset:9216\n"
    "ds_read_b128 v[32:35], v8 offset:704\n"
    "v_cvt_pk_bf16_f32 v80, v48, v49\n"
    "s_waitcnt lgkmcnt(3)\n"
    "v_fma_mix_f32 v52, v82, v28, v20 op_sel:[0,0,0] op_sel_hi:[1,0,0]\n"
    "v_fma_mix_f32 v53, v82, v29, v21 op_sel:[1,0,0] op_sel_hi:[1,0,0]\n"
    "v_mfma_f32_32x32x16_bf16 v[160:175], a[212:215], v[72:75], v[160:175]\n"
    "ds_read_b64_tr_b16 a[196:197], v7 offset:8448\n"
    "s_mov_b32 s77, 0x4000\n"
    "buffer_load_dwordx4 v[64:67], v9, s[52:55], s77 offen offset:0 sc1\n"
    "v_cvt_pk_bf16_f32 v81, v50, v51\n"
    "v_fma_mix_f32 v54, v83, v30, v22 op_sel:[0,0,0] op_sel_hi:[1,0,0]\n"
    "v_mfma_f32_32x32x16_bf16 v[176:191], a[212:215], v[76:79], v[176:191]\n"
    "ds_read_b64_tr_b16 a[198:199], v7 offset:9472\n"
    "ds_read_b128 v[36:39], v8 offset:720\n"
    "v_fma_mix_f32 v55, v83, v31, v23 op_sel:[1,0,0] op_sel_hi:[1,0,0]\n"
    "v_cvt_pk_bf16_f32 v82, v52, v53\n"
    "s_waitcnt vmcnt(23)\n"
    "v_fma_mix_f32 v56, v84, v24, v16 op_sel:[0,0,0] op_sel_hi:[1,0,0]\n"
    "v_fma_mix_f32 v57, v84, v25, v17 op_sel:[1,0,0] op_sel_hi:[1,0,0]\n"
    "v_mfma_f32_32x32x16_bf16 v[192:207], a[216:219], v[72:75], v[192:207]\n"
    "ds_read_b64_tr_b16 a[200:201], v7 offset:8704\n"
    "buffer_load_dwordx4 v[68:71], v10, s[52:55], s77 offen offset:0 sc1\n"
    "v_cvt_pk_bf16_f32 v83, v54, v55\n"
    "v_fma_mix_f32 v58, v85, v26, v18 op_sel:[0,0,0] op_sel_hi:[1,0,0]\n"
    "v_fma_mix_f32 v59, v85, v27, v19 op_sel:[1,0,0] op_sel_hi:[1,0,0]\n"
    "v_mfma_f32_32x32x16_bf16 v[208:223], a[216:219], v[76:79], v[208:223]\n"
    "ds_read_b64_tr_b16 a[202:203], v7 offset:9728\n"
    "ds_read_b128 v[40:43], v8 offset:2752\n"
    "v_cvt_pk_bf16_f32 v84, v56, v57\n"
    "v_fma_mix_f32 v60, v86, v28, v20 op_sel:[0,0,0] op_sel_hi:[1,0,0]\n"
    "v_fma_mix_f32 v61, v86, v29, v21 op_sel:[1,0,0] op_sel_hi:[1,0,0]\n"
    "v_mfma_f32_32x32x16_bf16 v[224:239], a[220:223], v[72:75], v[224:239]\n"
    "ds_read_b64_tr_b16 a[204:205], v7 offset:8960\n"
    "v_cvt_pk_bf16_f32 v85, v58, v59\n"
    "v_fma_mix_f32 v62, v87, v30, v22 op_sel:[0,0,0] op_sel_hi:[1,0,0]\n"
    "v_fma_mix_f32 v63, v87, v31, v23 op_sel:[1,0,0] op_sel_hi:[1,0,0]\n"
    "v_cvt_pk_bf16_f32 v86, v60, v61\n"
    "v_mfma_f32_32x32x16_bf16 v[240:255], a[220:223], v[76:79], v[240:255]\n"
    "ds_read_b64_tr_b16 a[206:207], v7 offset:9984\n"
    "ds_read_b128 v[44:47], v8 offset:2768\n"
    "v_cvt_pk_bf16_f32 v87, v62, v63\n"
    "s_waitcnt lgkmcnt(10)\n"
    "v_mfma_f32_32x32x16_bf16 v[128:143], a[192:195], v[80:83], v[128:143]\n"
    "ds_read_b64_tr_b16 a[208:209], v7 offset:12288\n"
    "s_waitcnt lgkmcnt(4)\n"
    "s_waitcnt vmcnt(22)\n"
    "v_fma_mix_f32 v48, v88, v40, v32 op_sel:[0,0,0] op_sel_hi:[1,0,0]\n"
    "v_fma_mix_f32 v49, v88, v41, v33 op_sel:[1,0,0] op_sel_hi:[1,0,0]\n"
    "v_fma_mix_f32 v50, v89, v42, v34 op_sel:[0,0,0] op_sel_hi:[1,0,0]\n"
    "v_fma_mix_f32 v51, v89, v43, v35 op_sel:[1,0,0] op_sel_hi:[1,0,0]\n"
    "v_mfma_f32_32x32x16_bf16 v[144:159], a[192:195], v[84:87], v[144:159]\n"
    "ds_read_b64_tr_b16 a[210:211], v7 offset:13312\n"
    "ds_read_b128 v[16:19], v8 offset:768\n"
    "v_cvt_pk_bf16_f32 v88, v48, v49\n"
    "s_waitcnt lgkmcnt(3)\n"
    "v_fma_mix_f32 v52, v90, v44, v36 op_sel:[0,0,0] op_sel_hi:[1,0,0]\n"
    "v_fma_mix_f32 v53, v90, v45, v37 op_sel:[1,0,0] op_sel_hi:[1,0,0]\n"
    "v_mfma_f32_32x32x16_bf16 v[160:175], a[196:199], v[80:83], v[160:175]\n"
    "ds_read_b64_tr_b16 a[212:213], v7 offset:12544\n"
    "s_mov_b32 s77, 0x4000\n"
    "buffer_load_dwordx4 v[72:75], v9, s[52:55], s77 offen offset:1024 sc1\n"
    "v_cvt_pk_bf16_f32 v89, v50, v51\n"
    "v_fma_mix_f32 v54, v91, v46, v38 op_sel:[0,0,0] op_sel_hi:[1,0,0]\n"
    "v_mfma_f32_32x32x16_bf16 v[176:191], a[196:199], v[84:87], v[176:191]\n"
    "ds_read_b64_tr_b16 a[214:215], v7 offset:13568\n"
    "ds_read_b128 v[20:23], v8 offset:784\n"
    "v_fma_mix_f32 v55, v91, v47, v39 op_sel:[1,0,0] op_sel_hi:[1,0,0]\n"
    "v_cvt_pk_bf16_f32 v90, v52, v53\n"
    "s_waitcnt vmcnt(21)\n"
    "v_fma_mix_f32 v56, v92, v40, v32 op_sel:[0,0,0] op_sel_hi:[1,0,0]\n"
    "v_fma_mix_f32 v57, v92, v41, v33 op_sel:[1,0,0] op_sel_hi:[1,0,0]\n"
    "v_mfma_f32_32x32x16_bf16 v[192:207], a[200:203], v[80:83], v[192:207]\n"
    "ds_read_b64_tr_b16 a[216:217], v7 offset:12800\n"
    "buffer_load_dwordx4 v[76:79], v10, s[52:55], s77 offen offset:1024 sc1\n"
    "v_cvt_pk_bf16_f32 v91, v54, v55\n"
    "v_fma_mix_f32 v58, v93, v42, v34 op_sel:[0,0,0] op_sel_hi:[1,0,0]\n"
    "v_fma_mix_f32 v59, v93, v43, v35 op_sel:[1,0,0] op_sel_hi:[1,0,0]\n"
    "v_mfma_f32_32x32x16_bf16 v[208:223], a[200:203], v[84:87], v[208:223]\n"
    "ds_read_b64_tr_b16 a[218:219], v7 offset:13824\n"
    "ds_read_b128 v[24:27], v8 offset:2816\n"
    "v_cvt_pk_bf16_f32 v92, v56, v57\n"
    "v_fma_mix_f32 v60, v94, v44, v36 op_sel:[0,0,0] op_sel_hi:[1,0,0]\n"
    "v_fma_mix_f32 v61, v94, v45, v37 op_sel:[1,0,0] op_sel_hi:[1,0,0]\n"
    "v_mfma_f32_32x32x16_bf16 v[224:239], a[204:207], v[80:83], v[224:239]\n"
    "ds_read_b64_tr_b16 a[220:221], v7 offset:13056\n"
    "v_cvt_pk_bf16_f32 v93, v58, v59\n"
    "v_fma_mix_f32 v62, v95, v46, v38 op_sel:[0,0,0] op_sel_hi:[1,0,0]\n"
    "v_fma_mix_f32 v63, v95, v47, v39 op_sel:[1,0,0] op_sel_hi:[1,0,0]\n"
    "v_cvt_pk_bf16_f32 v94, v60, v61\n"
    "v_mfma_f32_32x32x16_bf16 v[240:255], a[204:207], v[84:87], v[240:255]\n"
    "ds_read_b64_tr_b16 a[222:223], v7 offset:14080\n"
    "ds_read_b128 v[28:31], v8 offset:2832\n"
    "v_cvt_pk_bf16_f32 v95, v62, v63\n"
    "s_waitcnt lgkmcnt(10)\n"
    "v_mfma_f32_32x32x16_bf16 v[128:143], a[208:211], v[88:91], v[128:143]\n"
    "s_barrier\n"
    "v_add_u32 v7, 0x14000, v6\n"
    "ds_read_b64_tr_b16 a[192:193], v7 offset:0\n"
    "s_waitcnt lgkmcnt(4)\n"
    "s_waitcnt vmcnt(19)\n"
    "v_fma_mix_f32 v48, v96, v24, v16 op_sel:[0,0,0] op_sel_hi:[1,0,0]\n"
    "v_fma_mix_f32 v49, v96, v25, v17 op_sel:[1,0,0] op_sel_hi:[1,0,0]\n"
    "v_mfma_f32_32x32x16_bf16 v[144:159], a[208:211], v[92:95], v[144:159]\n"
    "ds_read_b64_tr_b16 a[194:195], v7 offset:1024\n"
    "ds_read_b128 v[32:35], v8 offset:832\n"
    "v_fma_mix_f32 v50, v97, v26, v18 op_sel:[0,0,0] op_sel_hi:[1,0,0]\n"
    "v_fma_mix_f32 v51, v97, v27, v19 op_sel:[1,0,0] op_sel_hi:[1,0,0]\n"
    "v_cvt_pk_bf16_f32 v96, v48, v49\n"
    "s_waitcnt lgkmcnt(3)\n"
    "v_fma_mix_f32 v52, v98, v28, v20 op_sel:[0,0,0] op_sel_hi:[1,0,0]\n"
    "v_mfma_f32_32x32x16_bf16 v[160:175], a[212:215], v[88:91], v[160:175]\n"
    "ds_read_b64_tr_b16 a[196:197], v7 offset:256\n"
    "s_mov_b32 s77, 0x4000\n"
    "buffer_load_dwordx4 v[80:83], v9, s[52:55], s77 offen offset:2048 sc1\n"
    "v_fma_mix_f32 v53, v98, v29, v21 op_sel:[1,0,0] op_sel_hi:[1,0,0]\n"
    "v_cvt_pk_bf16_f32 v97, v50, v51\n"
    "v_mfma_f32_32x32x16_bf16 v[176:191], a[212:215], v[92:95], v[176:191]\n"
    "ds_read_b64_tr_b16 a[198:199], v7 offset:1280\n"
    "ds_read_b128 v[36:39], v8 offset:848\n"
    "v_fma_mix_f32 v54, v99, v30, v22 op_sel:[0,0,0] op_sel_hi:[1,0,0]\n"
    "v_fma_mix_f32 v55, v99, v31, v23 op_sel:[1,0,0] op_sel_hi:[1,0,0]\n"
    "v_cvt_pk_bf16_f32 v98, v52, v53\n"
    "s_waitcnt vmcnt(19)\n"
    "v_fma_mix_f32 v56, v100, v24, v16 op_sel:[0,0,0] op_sel_hi:[1,0,0]\n"
    "v_mfma_f32_32x32x16_bf16 v[192:207], a[216:219], v[88:91], v[192:207]\n"
    "ds_read_b64_tr_b16 a[200:201], v7 offset:512\n"
    "buffer_load_dwordx4 v[84:87], v10, s[52:55], s77 offen offset:2048 sc1\n"
    "v_fma_mix_f32 v57, v100, v25, v17 op_sel:[1,0,0] op_sel_hi:[1,0,0]\n"
    "v_cvt_pk_bf16_f32 v99, v54, v55\n"
    "v_fma_mix_f32 v58, v101, v26, v18 op_sel:[0,0,0] op_sel_hi:[1,0,0]\n"
    "v_mfma_f32_32x32x16_bf16 v[208:223], a[216:219], v[92:95], v[208:223]\n"
    "ds_read_b64_tr_b16 a[202:203], v7 offset:1536\n"
    "ds_read_b128 v[40:43], v8 offset:2880\n"
    "v_fma_mix_f32 v59, v101, v27, v19 op_sel:[1,0,0] op_sel_hi:[1,0,0]\n"
    "v_cvt_pk_bf16_f32 v100, v56, v57\n"
    "v_fma_mix_f32 v60, v102, v28, v20 op_sel:[0,0,0] op_sel_hi:[1,0,0]\n"
    "v_fma_mix_f32 v61, v102, v29, v21 op_sel:[1,0,0] op_sel_hi:[1,0,0]\n"
    "v_mfma_f32_32x32x16_bf16 v[224:239], a[220:223], v[88:91], v[224:239]\n"
    "ds_read_b64_tr_b16 a[204:205], v7 offset:768\n"
    "v_cvt_pk_bf16_f32 v101, v58, v59\n"
    "v_fma_mix_f32 v62, v103, v30, v22 op_sel:[0,0,0] op_sel_hi:[1,0,0]\n"
    "v_fma_mix_f32 v63, v103, v31, v23 op_sel:[1,0,0] op_sel_hi:[1,0,0]\n"
    "v_cvt_pk_bf16_f32 v102, v60, v61\n"
    "v_mfma_f32_32x32x16_bf16 v[240:255], a[220:223], v[92:95], v[240:255]\n"
    "ds_read_b64_tr_b16 a[206:207], v7 offset:1792\n"
    "ds_read_b128 v[44:47], v8 offset:2896\n"
    "v_cvt_pk_bf16_f32 v103, v62, v63\n"
    "s_waitcnt lgkmcnt(10)\n"
    "v_mfma_f32_32x32x16_bf16 v[128:143], a[192:195], v[96:99], v[128:143]\n"
    "ds_read_b64_tr_b16 a[208:209], v7 offset:4096\n"
    "s_waitcnt lgkmcnt(4)\n"
    "s_waitcnt vmcnt(18)\n"
    "v_fma_mix_f32 v48, v104, v40, v32 op_sel:[0,0,0] op_sel_hi:[1,0,0]\n"
    "v_fma_mix_f32 v49, v104, v41, v33 op_sel:[1,0,0] op_sel_hi:[1,0,0]\n"
    "v_fma_mix_f32 v50, v105, v42, v34 op_sel:[0,0,0] op_sel_hi:[1,0,0]\n"
    "s_add_u32 s67, s69, 0x8000\n"
    "s_add_u32 m0, s69, 0x8000\n"
    "v_mfma_f32_32x32x16_bf16 v[144:159], a[192:195], v[100:103], v[144:159]\n"
    "ds_read_b64_tr_b16 a[210:211], v7 offset:5120\n"
    "ds_read_b128 v[16:19], v8 offset:896\n"
    "buffer_load_dwordx4 v11, s[40:43], s67 offen offset:0 lds\n"
    "v_fma_mix_f32 v51, v105, v43, v35 op_sel:[1,0,0] op_sel_hi:[1,0,0]\n"
    "v_cvt_pk_bf16_f32 v104, v48, v49\n"
    "s_waitcnt lgkmcnt(3)\n"
    "v_fma_mix_f32 v52, v106, v44, v36 op_sel:[0,0,0] op_sel_hi:[1,0,0]\n"
    "v_mfma_f32_32x32x16_bf16 v[160:175], a[196:199], v[96:99], v[160:175]\n"
    "ds_read_b64_tr_b16 a[212:213], v7 offset:4352\n"
    "s_mov_b32 s77, 0x4000\n"
    "buffer_load_dwordx4 v[88:91], v9, s[52:55], s77 offen offset:3072 sc1\n"
    "v_fma_mix_f32 v53, v106, v45, v37 op_sel:[1,0,0] op_sel_hi:[1,0,0]\n"
    "v_cvt_pk_bf16_f32 v105, v50, v51\n"
    "v_fma_mix_f32 v54, v107, v46, v38 op_sel:[0,0,0] op_sel_hi:[1,0,0]\n"
    "v_mfma_f32_32x32x16_bf16 v[176:191], a[196:199], v[100:103], v[176:191]\n"
    "ds_read_b64_tr_b16 a[214:215], v7 offset:5376\n"
    "ds_read_b128 v[20:23], v8 offset:912\n"
    "buffer_load_dwordx4 v12, s[40:43], s67 offen offset:1024 lds\n"
    "v_fma_mix_f32 v55, v107, v47, v39 op_sel:[1,0,0] op_sel_hi:[1,0,0]\n"
    "v_cvt_pk_bf16_f32 v106, v52, v53\n"
    "s_waitcnt vmcnt(19)\n"
    "v_fma_mix_f32 v56, v108, v40, v32 op_sel:[0,0,0] op_sel_hi:[1,0,0]\n"
    "v_mfma_f32_32x32x16_bf16 v[192:207], a[200:203], v[96:99], v[192:207]\n"
    "ds_read_b64_tr_b16 a[216:217], v7 offset:4608\n"
    "buffer_load_dwordx4 v[92:95], v10, s[52:55], s77 offen offset:3072 sc1\n"
    "v_fma_mix_f32 v57, v108, v41, v33 op_sel:[1,0,0] op_sel_hi:[1,0,0]\n"
    "v_cvt_pk_bf16_f32 v107, v54, v55\n"
    "v_fma_mix_f32 v58, v109, v42, v34 op_sel:[0,0,0] op_sel_hi:[1,0,0]\n"
    "v_fma_mix_f32 v59, v109, v43, v35 op_sel:[1,0,0] op_sel_hi:[1,0,0]\n"
    "v_mfma_f32_32x32x16_bf16 v[208:223], a[200:203], v[100:103], v[208:223]\n"
    "ds_read_b64_tr_b16 a[218:219], v7 offset:5632\n"
    "ds_read_b128 v[24:27], v8 offset:2944\n"
    "buffer_load_dwordx4 v13, s[40:43], s67 offen offset:2048 lds\n"
    "v_cvt_pk_bf16_f32 v108, v56, v57\n"
    "v_fma_mix_f32 v60, v110, v44, v36 op_sel:[0,0,0] op_sel_hi:[1,0,0]\n"
    "v_fma_mix_f32 v61, v110, v45, v37 op_sel:[1,0,0] op_sel_hi:[1,0,0]\n"
    "v_mfma_f32_32x32x16_bf16 v[224:239], a[204:207], v[96:99], v[224:239]\n"
    "ds_read_b64_tr_b16 a[220:221], v7 offset:4864\n"
    "buffer_load_dwordx4 v14, s[40:43], s67 offen offset:3072 lds\n"
    "v_cvt_pk_bf16_f32 v109, v58, v59\n"
    "v_fma_mix_f32 v62, v111, v46, v38 op_sel:[0,0,0] op_sel_hi:[1,0,0]\n"
    "v_fma_mix_f32 v63, v111, v47, v39 op_sel:[1,0,0] op_sel_hi:[1,0,0]\n"
    "v_cvt_pk_bf16_f32 v110, v60, v61\n"
    "v_mfma_f32_32x32x16_bf16 v[240:255], a[204:207], v[100:103], v[240:255]\n"
    "ds_read_b64_tr_b16 a[222:223], v7 offset:5888\n"
    "ds_read_b128 v[28:31], v8 offset:2960\n"
    "v_cvt_pk_bf16_f32 v111, v62, v63\n"
    "s_waitcnt lgkmcnt(10)\n"
    "v_mfma_f32_32x32x16_bf16 v[128:143], a[208:211], v[104:107], v[128:143]\n"
    "ds_read_b64_tr_b16 a[192:193], v7 offset:8192\n"
    "s_waitcnt lgkmcnt(4)\n"
    "s_waitcnt vmcnt(19)\n"
    "v_fma_mix_f32 v48, v112, v24, v16 op_sel:[0,0,0] op_sel_hi:[1,0,0]\n"
    "v_fma_mix_f32 v49, v112, v25, v17 op_sel:[1,0,0] op_sel_hi:[1,0,0]\n"
    "v_fma_mix_f32 v50, v113, v26, v18 op_sel:[0,0,0] op_sel_hi:[1,0,0]\n"
    "v_fma_mix_f32 v51, v113, v27, v19 op_sel:[1,0,0] op_sel_hi:[1,0,0]\n"
    "v_mfma_f32_32x32x16_bf16 v[144:159], a[208:211], v[108:111], v[144:159]\n"
    "ds_read_b64_tr_b16 a[194:195], v7 offset:9216\n"
    "ds_read_b128 v[32:35], v8 offset:960\n"
    "v_cvt_pk_bf16_f32 v112, v48, v49\n"
    "s_waitcnt lgkmcnt(3)\n"
    "v_fma_mix_f32 v52, v114, v28, v20 op_sel:[0,0,0] op_sel_hi:[1,0,0]\n"
    "v_fma_mix_f32 v53, v114, v29, v21 op_sel:[1,0,0] op_sel_hi:[1,0,0]\n"
    "v_mfma_f32_32x32x16_bf16 v[160:175], a[212:215], v[104:107], v[160:175]\n"
    "ds_read_b64_tr_b16 a[196:197], v7 offset:8448\n"
    "s_mov_b32 s77, 0x5000\n"
    "buffer_load_dwordx4 v[96:99], v9, s[52:55], s77 offen offset:0 sc1\n"
    "v_cvt_pk_bf16_f32 v113, v50, v51\n"
    "v_fma_mix_f32 v54, v115, v30, v22 op_sel:[0,0,0] op_sel_hi:[1,0,0]\n"
    "v_mfma_f32_32x32x16_bf16 v[176:191], a[212:215], v[108:111], v[176:191]\n"
    "ds_read_b64_tr_b16 a[198:199], v7 offset:9472\n"
    "ds_read_b128 v[36:39], v8 offset:976\n"
    "v_fma_mix_f32 v55, v115, v31, v23 op_sel:[1,0,0] op_sel_hi:[1,0,0]\n"
    "v_cvt_pk_bf16_f32 v114, v52, v53\n"
    "s_waitcnt vmcnt(19)\n"
    "v_fma_mix_f32 v56, v116, v24, v16 op_sel:[0,0,0] op_sel_hi:[1,0,0]\n"
    "v_fma_mix_f32 v57, v116, v25, v17 op_sel:[1,0,0] op_sel_hi:[1,0,0]\n"
    "v_mfma_f32_32x32x16_bf16 v[192:207], a[216:219], v[104:107], v[192:207]\n"
    "ds_read_b64_tr_b16 a[200:201], v7 offset:8704\n"
    "buffer_load_dwordx4 v[100:103], v10, s[52:55], s77 offen offset:0 sc1\n"
    "v_cvt_pk_bf16_f32 v115, v54, v55\n"
    "v_fma_mix_f32 v58, v117, v26, v18 op_sel:[0,0,0] op_sel_hi:[1,0,0]\n"
    "v_fma_mix_f32 v59, v117, v27, v19 op_sel:[1,0,0] op_sel_hi:[1,0,0]\n"
    "v_mfma_f32_32x32x16_bf16 v[208:223], a[216:219], v[108:111], v[208:223]\n"
    "ds_read_b64_tr_b16 a[202:203], v7 offset:9728\n"
    "ds_read_b128 v[40:43], v8 offset:3008\n"
    "v_cvt_pk_bf16_f32 v116, v56, v57\n"
    "v_fma_mix_f32 v60, v118, v28, v20 op_sel:[0,0,0] op_sel_hi:[1,0,0]\n"
    "v_fma_mix_f32 v61, v118, v29, v21 op_sel:[1,0,0] op_sel_hi:[1,0,0]\n"
    "v_mfma_f32_32x32x16_bf16 v[224:239], a[220:223], v[104:107], v[224:239]\n"
    "ds_read_b64_tr_b16 a[204:205], v7 offset:8960\n"
    "v_cvt_pk_bf16_f32 v117, v58, v59\n"
    "v_fma_mix_f32 v62, v119, v30, v22 op_sel:[0,0,0] op_sel_hi:[1,0,0]\n"
    "v_fma_mix_f32 v63, v119, v31, v23 op_sel:[1,0,0] op_sel_hi:[1,0,0]\n"
    "v_cvt_pk_bf16_f32 v118, v60, v61\n"
    "v_mfma_f32_32x32x16_bf16 v[240:255], a[220:223], v[108:111], v[240:255]\n"
    "ds_read_b64_tr_b16 a[206:207], v7 offset:9984\n"
    "ds_read_b128 v[44:47], v8 offset:3024\n"
    "v_cvt_pk_bf16_f32 v119, v62, v63\n"
    "s_waitcnt lgkmcnt(10)\n"
    "v_mfma_f32_32x32x16_bf16 v[128:143], a[192:195], v[112:115], v[128:143]\n"
    "ds_read_b64_tr_b16 a[208:209], v7 offset:12288\n"
    "s_waitcnt lgkmcnt(4)\n"
    "s_waitcnt vmcnt(18)\n"
    "v_fma_mix_f32 v48, v120, v40, v32 op_sel:[0,0,0] op_sel_hi:[1,0,0]\n"
    "v_fma_mix_f32 v49, v120, v41, v33 op_sel:[1,0,0] op_sel_hi:[1,0,0]\n"
    "v_fma_mix_f32 v50, v121, v42, v34 op_sel:[0,0,0] op_sel_hi:[1,0,0]\n"
    "v_fma_mix_f32 v51, v121, v43, v35 op_sel:[1,0,0] op_sel_hi:[1,0,0]\n"
    "v_mfma_f32_32x32x16_bf16 v[144:159], a[192:195], v[116:119], v[144:159]\n"
    "ds_read_b64_tr_b16 a[210:211], v7 offset:13312\n"
    "ds_read_b128 v[16:19], v8 offset:1024\n"
    "v_cvt_pk_bf16_f32 v120, v48, v49\n"
    "s_waitcnt lgkmcnt(3)\n"
    "v_fma_mix_f32 v52, v122, v44, v36 op_sel:[0,0,0] op_sel_hi:[1,0,0]\n"
    "v_fma_mix_f32 v53, v122, v45, v37 op_sel:[1,0,0] op_sel_hi:[1,0,0]\n"
    "v_mfma_f32_32x32x16_bf16 v[160:175], a[196:199], v[112:115], v[160:175]\n"
    "ds_read_b64_tr_b16 a[212:213], v7 offset:12544\n"
    "s_mov_b32 s77, 0x5000\n"
    "buffer_load_dwordx4 v[104:107], v9, s[52:55], s77 offen offset:1024 sc1\n"
    "v_cvt_pk_bf16_f32 v121, v50, v51\n"
    "v_fma_mix_f32 v54, v123, v46, v38 op_sel:[0,0,0] op_sel_hi:[1,0,0]\n"
    "v_mfma_f32_32x32x16_bf16 v[176:191], a[196:199], v[116:119], v[176:191]\n"
    "ds_read_b64_tr_b16 a[214:215], v7 offset:13568\n"
    "ds_read_b128 v[20:23], v8 offset:1040\n"
    "v_fma_mix_f32 v55, v123, v47, v39 op_sel:[1,0,0] op_sel_hi:[1,0,0]\n"
    "v_cvt_pk_bf16_f32 v122, v52, v53\n"
    "s_waitcnt vmcnt(17)\n"
    "v_fma_mix_f32 v56, v124, v40, v32 op_sel:[0,0,0] op_sel_hi:[1,0,0]\n"
    "v_fma_mix_f32 v57, v124, v41, v33 op_sel:[1,0,0] op_sel_hi:[1,0,0]\n"
    "v_mfma_f32_32x32x16_bf16 v[192:207], a[200:203], v[112:115], v[192:207]\n"
    "ds_read_b64_tr_b16 a[216:217], v7 offset:12800\n"
    "buffer_load_dwordx4 v[108:111], v10, s[52:55], s77 offen offset:1024 sc1\n"
    "v_cvt_pk_bf16_f32 v123, v54, v55\n"
    "v_fma_mix_f32 v58, v125, v42, v34 op_sel:[0,0,0] op_sel_hi:[1,0,0]\n"
    "v_fma_mix_f32 v59, v125, v43, v35 op_sel:[1,0,0] op_sel_hi:[1,0,0]\n"
    "v_mfma_f32_32x32x16_bf16 v[208:223], a[200:203], v[116:119], v[208:223]\n"
    "ds_read_b64_tr_b16 a[218:219], v7 offset:13824\n"
    "ds_read_b128 v[24:27], v8 offset:3072\n"
    "v_cvt_pk_bf16_f32 v124, v56, v57\n"
    "v_fma_mix_f32 v60, v126, v44, v36 op_sel:[0,0,0] op_sel_hi:[1,0,0]\n"
    "v_fma_mix_f32 v61, v126, v45, v37 op_sel:[1,0,0] op_sel_hi:[1,0,0]\n"
    "v_mfma_f32_32x32x16_bf16 v[224:239], a[204:207], v[112:115], v[224:239]\n"
    "ds_read_b64_tr_b16 a[220:221], v7 offset:13056\n"
    "v_cvt_pk_bf16_f32 v125, v58, v59\n"
    "v_fma_mix_f32 v62, v127, v46, v38 op_sel:[0,0,0] op_sel_hi:[1,0,0]\n"
    "v_fma_mix_f32 v63, v127, v47, v39 op_sel:[1,0,0] op_sel_hi:[1,0,0]\n"
    "v_cvt_pk_bf16_f32 v126, v60, v61\n"
    "v_mfma_f32_32x32x16_bf16 v[240:255], a[204:207], v[116:119], v[240:255]\n"
    "ds_read_b64_tr_b16 a[222:223], v7 offset:14080\n"
    "ds_read_b128 v[28:31], v8 offset:3088\n"
    "v_cvt_pk_bf16_f32 v127, v62, v63\n"
    "s_waitcnt lgkmcnt(10)\n"
    "v_mfma_f32_32x32x16_bf16 v[128:143], a[208:211], v[120:123], v[128:143]\n"
    "s_barrier\n"
    "v_add_u32 v7, 0xc000, v6\n"
    "ds_read_b64_tr_b16 a[192:193], v7 offset:0\n"
    "s_waitcnt lgkmcnt(4)\n"
    "s_waitcnt vmcnt(15)\n"
    "v_fma_mix_f32 v48, v64, v24, v16 op_sel:[0,0,0] op_sel_hi:[1,0,0]\n"
    "v_fma_mix_f32 v49, v64, v25, v17 op_sel:[1,0,0] op_sel_hi:[1,0,0]\n"
    "v_mfma_f32_32x32x16_bf16 v[144:159], a[208:211], v[124:127], v[144:159]\n"
    "ds_read_b64_tr_b16 a[194:195], v7 offset:1024\n"
    "ds_read_b128 v[32:35], v8 offset:1088\n"
    "v_fma_mix_f32 v50, v65, v26, v18 op_sel:[0,0,0] op_sel_hi:[1,0,0]\n"
    "v_fma_mix_f32 v51, v65, v27, v19 op_sel:[1,0,0] op_sel_hi:[1,0,0]\n"
    "v_cvt_pk_bf16_f32 v64, v48, v49\n"
    "s_waitcnt lgkmcnt(3)\n"
    "v_fma_mix_f32 v52, v66, v28, v20 op_sel:[0,0,0] op_sel_hi:[1,0,0]\n"
    "v_mfma_f32_32x32x16_bf16 v[160:175], a[212:215], v[120:123], v[160:175]\n"
    "ds_read_b64_tr_b16 a[196:197], v7 offset:256\n"
    "s_mov_b32 s77, 0x5000\n"
    "buffer_load_dwordx4 v[112:115], v9, s[52:55], s77 offen offset:2048 sc1\n"
    "v_fma_mix_f32 v53, v66, v29, v21 op_sel:[1,0,0] op_sel_hi:[1,0,0]\n"
    "v_cvt_pk_bf16_f32 v65, v50, v51\n"
    "v_mfma_f32_32x32x16_bf16 v[176:191], a[212:215], v[124:127], v[176:191]\n"
    "ds_read_b64_tr_b16 a[198:199], v7 offset:1280\n"
    "ds_read_b128 v[36:39], v8 offset:1104\n"
    "v_fma_mix_f32 v54, v67, v30, v22 op_sel:[0,0,0] op_sel_hi:[1,0,0]\n"
    "v_fma_mix_f32 v55, v67, v31, v23 op_sel:[1,0,0] op_sel_hi:[1,0,0]\n"
    "v_cvt_pk_bf16_f32 v66, v52, v53\n"
    "s_waitcnt vmcnt(15)\n"
    "v_fma_mix_f32 v56, v68, v24, v16 op_sel:[0,0,0] op_sel_hi:[1,0,0]\n"
    "v_mfma_f32_32x32x16_bf16 v[192:207], a[216:219], v[120:123], v[192:207]\n"
    "ds_read_b64_tr_b16 a[200:201], v7 offset:512\n"
    "buffer_load_dwordx4 v[116:119], v10, s[52:55], s77 offen offset:2048 sc1\n"
    "v_fma_mix_f32 v57, v68, v25, v17 op_sel:[1,0,0] op_sel_hi:[1,0,0]\n"
    "v_cvt_pk_bf16_f32 v67, v54, v55\n"
    "v_fma_mix_f32 v58, v69, v26, v18 op_sel:[0,0,0] op_sel_hi:[1,0,0]\n"
    "v_mfma_f32_32x32x16_bf16 v[208:223], a[216:219], v[124:127], v[208:223]\n"
    "ds_read_b64_tr_b16 a[202:203], v7 offset:1536\n"
    "ds_read_b128 v[40:43], v8 offset:3136\n"
    "v_fma_mix_f32 v59, v69, v27, v19 op_sel:[1,0,0] op_sel_hi:[1,0,0]\n"
    "v_cvt_pk_bf16_f32 v68, v56, v57\n"
    "v_fma_mix_f32 v60, v70, v28, v20 op_sel:[0,0,0] op_sel_hi:[1,0,0]\n"
    "v_fma_mix_f32 v61, v70, v29, v21 op_sel:[1,0,0] op_sel_hi:[1,0,0]\n"
    "v_mfma_f32_32x32x16_bf16 v[224:239], a[220:223], v[120:123], v[224:239]\n"
    "ds_read_b64_tr_b16 a[204:205], v7 offset:768\n"
    "v_cvt_pk_bf16_f32 v69, v58, v59\n"
    "v_fma_mix_f32 v62, v71, v30, v22 op_sel:[0,0,0] op_sel_hi:[1,0,0]\n"
    "v_fma_mix_f32 v63, v71, v31, v23 op_sel:[1,0,0] op_sel_hi:[1,0,0]\n"
    "v_cvt_pk_bf16_f32 v70, v60, v61\n"
    "v_mfma_f32_32x32x16_bf16 v[240:255], a[220:223], v[124:127], v[240:255]\n"
    "ds_read_b64_tr_b16 a[206:207], v7 offset:1792\n"
    "ds_read_b128 v[44:47], v8 offset:3152\n"
    "v_cvt_pk_bf16_f32 v71, v62, v63\n"
    "s_waitcnt lgkmcnt(10)\n"
    "v_mfma_f32_32x32x16_bf16 v[128:143], a[192:195], v[64:67], v[128:143]\n"
    "ds_read_b64_tr_b16 a[208:209], v7 offset:4096\n"
    "s_waitcnt lgkmcnt(4)\n"
    "s_waitcnt vmcnt(15)\n"
    "v_fma_mix_f32 v48, v72, v40, v32 op_sel:[0,0,0] op_sel_hi:[1,0,0]\n"
    "v_fma_mix_f32 v49, v72, v41, v33 op_sel:[1,0,0] op_sel_hi:[1,0,0]\n"
    "v_fma_mix_f32 v50, v73, v42, v34 op_sel:[0,0,0] op_sel_hi:[1,0,0]\n"
    "s_add_u32 s68, s69, 0\n"
    "s_add_u32 m0, s69, 0x10000\n"
    "v_mfma_f32_32x32x16_bf16 v[144:159], a[192:195], v[68:71], v[144:159]\n"
    "ds_read_b64_tr_b16 a[210:211], v7 offset:5120\n"
    "ds_read_b128 v[16:19], v8 offset:1152\n"
    "buffer_load_dwordx4 v15, s[44:47], s68 offen offset:0 lds\n"
    "v_fma_mix_f32 v51, v73, v43, v35 op_sel:[1,0,0] op_sel_hi:[1,0,0]\n"
    "v_cvt_pk_bf16_f32 v72, v48, v49\n"
    "s_waitcnt lgkmcnt(3)\n"
    "v_fma_mix_f32 v52, v74, v44, v36 op_sel:[0,0,0] op_sel_hi:[1,0,0]\n"
    "v_mfma_f32_32x32x16_bf16 v[160:175], a[196:199], v[64:67], v[160:175]\n"
    "ds_read_b64_tr_b16 a[212:213], v7 offset:4352\n"
    "s_mov_b32 s77, 0x5000\n"
    "buffer_load_dwordx4 v[120:123], v9, s[52:55], s77 offen offset:3072 sc1\n"
    "v_fma_mix_f32 v53, v74, v45, v37 op_sel:[1,0,0] op_sel_hi:[1,0,0]\n"
    "v_cvt_pk_bf16_f32 v73, v50, v51\n"
    "v_fma_mix_f32 v54, v75, v46, v38 op_sel:[0,0,0] op_sel_hi:[1,0,0]\n"
    "v_mfma_f32_32x32x16_bf16 v[176:191], a[196:199], v[68:71], v[176:191]\n"
    "ds_read_b64_tr_b16 a[214:215], v7 offset:5376\n"
    "ds_read_b128 v[20:23], v8 offset:1168\n"
    "buffer_load_dwordx4 v15, s[44:47], s68 offen offset:1024 lds\n"
    "v_fma_mix_f32 v55, v75, v47, v39 op_sel:[1,0,0] op_sel_hi:[1,0,0]\n"
    "v_cvt_pk_bf16_f32 v74, v52, v53\n"
    "s_waitcnt vmcnt(17)\n"
    "v_fma_mix_f32 v56, v76, v40, v32 op_sel:[0,0,0] op_sel_hi:[1,0,0]\n"
    "v_mfma_f32_32x32x16_bf16 v[192:207], a[200:203], v[64:67], v[192:207]\n"
    "ds_read_b64_tr_b16 a[216:217], v7 offset:4608\n"
    "buffer_load_dwordx4 v[124:127], v10, s[52:55], s77 offen offset:3072 sc1\n"
    "v_fma_mix_f32 v57, v76, v41, v33 op_sel:[1,0,0] op_sel_hi:[1,0,0]\n"
    "v_cvt_pk_bf16_f32 v75, v54, v55\n"
    "v_fma_mix_f32 v58, v77, v42, v34 op_sel:[0,0,0] op_sel_hi:[1,0,0]\n"
    "v_fma_mix_f32 v59, v77, v43, v35 op_sel:[1,0,0] op_sel_hi:[1,0,0]\n"
    "v_mfma_f32_32x32x16_bf16 v[208:223], a[200:203], v[68:71], v[208:223]\n"
    "ds_read_b64_tr_b16 a[218:219], v7 offset:5632\n"
    "ds_read_b128 v[24:27], v8 offset:3200\n"
    "buffer_load_dwordx4 v15, s[44:47], s68 offen offset:2048 lds\n"
    "v_cvt_pk_bf16_f32 v76, v56, v57\n"
    "v_fma_mix_f32 v60, v78, v44, v36 op_sel:[0,0,0] op_sel_hi:[1,0,0]\n"
    "v_fma_mix_f32 v61, v78, v45, v37 op_sel:[1,0,0] op_sel_hi:[1,0,0]\n"
    "v_mfma_f32_32x32x16_bf16 v[224:239], a[204:207], v[64:67], v[224:239]\n"
    "ds_read_b64_tr_b16 a[220:221], v7 offset:4864\n"
    "buffer_load_dwordx4 v15, s[44:47], s68 offen offset:3072 lds\n"
    "v_cvt_pk_bf16_f32 v77, v58, v59\n"
    "v_fma_mix_f32 v62, v79, v46, v38 op_sel:[0,0,0] op_sel_hi:[1,0,0]\n"
    "v_fma_mix_f32 v63, v79, v47, v39 op_sel:[1,0,0] op_sel_hi:[1,0,0]\n"
    "v_cvt_pk_bf16_f32 v78, v60, v61\n"
    "v_mfma_f32_32x32x16_bf16 v[240:255], a[204:207], v[68:71], v[240:255]\n"
    "ds_read_b64_tr_b16 a[222:223], v7 offset:5888\n"
    "ds_read_b128 v[28:31], v8 offset:3216\n"
    "v_cvt_pk_bf16_f32 v79, v62, v63\n"
    "s_waitcnt lgkmcnt(10)\n"
    "v_mfma_f32_32x32x16_bf16 v[128:143], a[208:211], v[72:75], v[128:143]\n"
    "ds_read_b64_tr_b16 a[192:193], v7 offset:8192\n"
    "s_waitcnt lgkmcnt(4)\n"
    "s_waitcnt vmcnt(19)\n"
    "v_fma_mix_f32 v48, v80, v24, v16 op_sel:[0,0,0] op_sel_hi:[1,0,0]\n"
    "v_fma_mix_f32 v49, v80, v25, v17 op_sel:[1,0,0] op_sel_hi:[1,0,0]\n"
    "v_fma_mix_f32 v50, v81, v26, v18 op_sel:[0,0,0] op_sel_hi:[1,0,0]\n"
    "v_fma_mix_f32 v51, v81, v27, v19 op_sel:[1,0,0] op_sel_hi:[1,0,0]\n"
    "v_mfma_f32_32x32x16_bf16 v[144:159], a[208:211], v[76:79], v[144:159]\n"
    "ds_read_b64_tr_b16 a[194:195], v7 offset:9216\n"
    "ds_read_b128 v[32:35], v8 offset:1216\n"
    "v_cvt_pk_bf16_f32 v80, v48, v49\n"
    "s_waitcnt lgkmcnt(3)\n"
    "v_fma_mix_f32 v52, v82, v28, v20 op_sel:[0,0,0] op_sel_hi:[1,0,0]\n"
    "v_fma_mix_f32 v53, v82, v29, v21 op_sel:[1,0,0] op_sel_hi:[1,0,0]\n"
    "v_mfma_f32_32x32x16_bf16 v[160:175], a[212:215], v[72:75], v[160:175]\n"
    "ds_read_b64_tr_b16 a[196:197], v7 offset:8448\n"
    "s_mov_b32 s77, 0x6000\n"
    "buffer_load_dwordx4 v[64:67], v9, s[52:55], s77 offen offset:0 sc1\n"
    "v_cvt_pk_bf16_f32 v81, v50, v51\n"
    "v_fma_mix_f32 v54, v83, v30, v22 op_sel:[0,0,0] op_sel_hi:[1,0,0]\n"
    "v_mfma_f32_32x32x16_bf16 v[176:191], a[212:215], v[76:79], v[176:191]\n"
    "ds_read_b64_tr_b16 a[198:199], v7 offset:9472\n"
    "ds_read_b128 v[36:39], v8 offset:1232\n"
    "v_fma_mix_f32 v55, v83, v31, v23 op_sel:[1,0,0] op_sel_hi:[1,0,0]\n"
    "v_cvt_pk_bf16_f32 v82, v52, v53\n"
    "s_waitcnt vmcnt(19)\n"
    "v_fma_mix_f32 v56, v84, v24, v16 op_sel:[0,0,0] op_sel_hi:[1,0,0]\n"
    "v_fma_mix_f32 v57, v84, v25, v17 op_sel:[1,0,0] op_sel_hi:[1,0,0]\n"
    "v_mfma_f32_32x32x16_bf16 v[192:207], a[216:219], v[72:75], v[192:207]\n"
    "ds_read_b64_tr_b16 a[200:201], v7 offset:8704\n"
    "buffer_load_dwordx4 v[68:71], v10, s[52:55], s77 offen offset:0 sc1\n"
    "v_cvt_pk_bf16_f32 v83, v54, v55\n"
    "v_fma_mix_f32 v58, v85, v26, v18 op_sel:[0,0,0] op_sel_hi:[1,0,0]\n"
    "v_fma_mix_f32 v59, v85, v27, v19 op_sel:[1,0,0] op_sel_hi:[1,0,0]\n"
    "v_mfma_f32_32x32x16_bf16 v[208:223], a[216:219], v[76:79], v[208:223]\n"
    "ds_read_b64_tr_b16 a[202:203], v7 offset:9728\n"
    "ds_read_b128 v[40:43], v8 offset:3264\n"
    "v_cvt_pk_bf16_f32 v84, v56, v57\n"
    "v_fma_mix_f32 v60, v86, v28, v20 op_sel:[0,0,0] op_sel_hi:[1,0,0]\n"
    "v_fma_mix_f32 v61, v86, v29, v21 op_sel:[1,0,0] op_sel_hi:[1,0,0]\n"
    "v_mfma_f32_32x32x16_bf16 v[224:239], a[220:223], v[72:75], v[224:239]\n"
    "ds_read_b64_tr_b16 a[204:205], v7 offset:8960\n"
    "v_cvt_pk_bf16_f32 v85, v58, v59\n"
    "v_fma_mix_f32 v62, v87, v30, v22 op_sel:[0,0,0] op_sel_hi:[1,0,0]\n"
    "v_fma_mix_f32 v63, v87, v31, v23 op_sel:[1,0,0] op_sel_hi:[1,0,0]\n"
    "v_cvt_pk_bf16_f32 v86, v60, v61\n"
    "v_mfma_f32_32x32x16_bf16 v[240:255], a[220:223], v[76:79], v[240:255]\n"
    "ds_read_b64_tr_b16 a[206:207], v7 offset:9984\n"
    "ds_read_b128 v[44:47], v8 offset:3280\n"
    "v_cvt_pk_bf16_f32 v87, v62, v63\n"
    "s_waitcnt lgkmcnt(10)\n"
    "v_mfma_f32_32x32x16_bf16 v[128:143], a[192:195], v[80:83], v[128:143]\n"
    "ds_read_b64_tr_b16 a[208:209], v7 offset:12288\n"
    "s_waitcnt lgkmcnt(4)\n"
    "s_waitcnt vmcnt(18)\n"
    "v_fma_mix_f32 v48, v88, v40, v32 op_sel:[0,0,0] op_sel_hi:[1,0,0]\n"
    "v_fma_mix_f32 v49, v88, v41, v33 op_sel:[1,0,0] op_sel_hi:[1,0,0]\n"
    "v_fma_mix_f32 v50, v89, v42, v34 op_sel:[0,0,0] op_sel_hi:[1,0,0]\n"
    "v_fma_mix_f32 v51, v89, v43, v35 op_sel:[1,0,0] op_sel_hi:[1,0,0]\n"
    "v_mfma_f32_32x32x16_bf16 v[144:159], a[192:195], v[84:87], v[144:159]\n"
    "ds_read_b64_tr_b16 a[210:211], v7 offset:13312\n"
    "ds_read_b128 v[16:19], v8 offset:1280\n"
    "v_cvt_pk_bf16_f32 v88, v48, v49\n"
    "s_waitcnt lgkmcnt(3)\n"
    "v_fma_mix_f32 v52, v90, v44, v36 op_sel:[0,0,0] op_sel_hi:[1,0,0]\n"
    "v_fma_mix_f32 v53, v90, v45, v37 op_sel:[1,0,0] op_sel_hi:[1,0,0]\n"
    "v_mfma_f32_32x32x16_bf16 v[160:175], a[196:199], v[80:83], v[160:175]\n"
    "ds_read_b64_tr_b16 a[212:213], v7 offset:12544\n"
    "s_mov_b32 s77, 0x6000\n"
    "buffer_load_dwordx4 v[72:75], v9, s[52:55], s77 offen offset:1024 sc1\n"
    "v_cvt_pk_bf16_f32 v89, v50, v51\n"
    "v_fma_mix_f32 v54, v91, v46, v38 op_sel:[0,0,0] op_sel_hi:[1,0,0]\n"
    "v_mfma_f32_32x32x16_bf16 v[176:191], a[196:199], v[84:87], v[176:191]\n"
    "ds_read_b64_tr_b16 a[214:215], v7 offset:13568\n"
    "ds_read_b128 v[20:23], v8 offset:1296\n"
    "v_fma_mix_f32 v55, v91, v47, v39 op_sel:[1,0,0] op_sel_hi:[1,0,0]\n"
    "v_cvt_pk_bf16_f32 v90, v52, v53\n"
    "s_waitcnt vmcnt(17)\n"
    "v_fma_mix_f32 v56, v92, v40, v32 op_sel:[0,0,0] op_sel_hi:[1,0,0]\n"
    "v_fma_mix_f32 v57, v92, v41, v33 op_sel:[1,0,0] op_sel_hi:[1,0,0]\n"
    "v_mfma_f32_32x32x16_bf16 v[192:207], a[200:203], v[80:83], v[192:207]\n"
    "ds_read_b64_tr_b16 a[216:217], v7 offset:12800\n"
    "buffer_load_dwordx4 v[76:79], v10, s[52:55], s77 offen offset:1024 sc1\n"
    "v_cvt_pk_bf16_f32 v91, v54, v55\n"
    "v_fma_mix_f32 v58, v93, v42, v34 op_sel:[0,0,0] op_sel_hi:[1,0,0]\n"
    "v_fma_mix_f32 v59, v93, v43, v35 op_sel:[1,0,0] op_sel_hi:[1,0,0]\n"
    "v_mfma_f32_32x32x16_bf16 v[208:223], a[200:203], v[84:87], v[208:223]\n"
    "ds_read_b64_tr_b16 a[218:219], v7 offset:13824\n"
    "ds_read_b128 v[24:27], v8 offset:3328\n"
    "v_cvt_pk_bf16_f32 v92, v56, v57\n"
    "v_fma_mix_f32 v60, v94, v44, v36 op_sel:[0,0,0] op_sel_hi:[1,0,0]\n"
    "v_fma_mix_f32 v61, v94, v45, v37 op_sel:[1,0,0] op_sel_hi:[1,0,0]\n"
    "v_mfma_f32_32x32x16_bf16 v[224:239], a[204:207], v[80:83], v[224:239]\n"
    "ds_read_b64_tr_b16 a[220:221], v7 offset:13056\n"
    "v_cvt_pk_bf16_f32 v93, v58, v59\n"
    "v_fma_mix_f32 v62, v95, v46, v38 op_sel:[0,0,0] op_sel_hi:[1,0,0]\n"
    "v_fma_mix_f32 v63, v95, v47, v39 op_sel:[1,0,0] op_sel_hi:[1,0,0]\n"
    "v_cvt_pk_bf16_f32 v94, v60, v61\n"
    "v_mfma_f32_32x32x16_bf16 v[240:255], a[204:207], v[84:87], v[240:255]\n"
    "ds_read_b64_tr_b16 a[222:223], v7 offset:14080\n"
    "ds_read_b128 v[28:31], v8 offset:3344\n"
    "v_cvt_pk_bf16_f32 v95, v62, v63\n"
    "s_waitcnt lgkmcnt(10)\n"
    "v_mfma_f32_32x32x16_bf16 v[128:143], a[208:211], v[88:91], v[128:143]\n"
    "s_barrier\n"
    "v_add_u32 v7, 0x18000, v6\n"
    "ds_read_b64_tr_b16 a[192:193], v7 offset:0\n"
    "s_waitcnt lgkmcnt(4)\n"
    "s_waitcnt vmcnt(15)\n"
    "v_fma_mix_f32 v48, v96, v24, v16 op_sel:[0,0,0] op_sel_hi:[1,0,0]\n"
    "v_fma_mix_f32 v49, v96, v25, v17 op_sel:[1,0,0] op_sel_hi:[1,0,0]\n"
    "v_mfma_f32_32x32x16_bf16 v[144:159], a[208:211], v[92:95], v[144:159]\n"
    "ds_read_b64_tr_b16 a[194:195], v7 offset:1024\n"
    "ds_read_b128 v[32:35], v8 offset:1344\n"
    "v_fma_mix_f32 v50, v97, v26, v18 op_sel:[0,0,0] op_sel_hi:[1,0,0]\n"
    "v_fma_mix_f32 v51, v97, v27, v19 op_sel:[1,0,0] op_sel_hi:[1,0,0]\n"
    "v_cvt_pk_bf16_f32 v96, v48, v49\n"
    "s_waitcnt lgkmcnt(3)\n"
    "v_fma_mix_f32 v52, v98, v28, v20 op_sel:[0,0,0] op_sel_hi:[1,0,0]\n"
    "v_mfma_f32_32x32x16_bf16 v[160:175], a[212:215], v[88:91], v[160:175]\n"
    "ds_read_b64_tr_b16 a[196:197], v7 offset:256\n"
    "s_mov_b32 s77, 0x6000\n"
    "buffer_load_dwordx4 v[80:83], v9, s[52:55], s77 offen offset:2048 sc1\n"
    "v_fma_mix_f32 v53, v98, v29, v21 op_sel:[1,0,0] op_sel_hi:[1,0,0]\n"
    "v_cvt_pk_bf16_f32 v97, v50, v51\n"
    "v_mfma_f32_32x32x16_bf16 v[176:191], a[212:215], v[92:95], v[176:191]\n"
    "ds_read_b64_tr_b16 a[198:199], v7 offset:1280\n"
    "ds_read_b128 v[36:39], v8 offset:1360\n"
    "v_fma_mix_f32 v54, v99, v30, v22 op_sel:[0,0,0] op_sel_hi:[1,0,0]\n"
    "v_fma_mix_f32 v55, v99, v31, v23 op_sel:[1,0,0] op_sel_hi:[1,0,0]\n"
    "v_cvt_pk_bf16_f32 v98, v52, v53\n"
    "s_waitcnt vmcnt(15)\n"
    "v_fma_mix_f32 v56, v100, v24, v16 op_sel:[0,0,0] op_sel_hi:[1,0,0]\n"
    "v_mfma_f32_32x32x16_bf16 v[192:207], a[216:219], v[88:91], v[192:207]\n"
    "ds_read_b64_tr_b16 a[200:201], v7 offset:512\n"
    "buffer_load_dwordx4 v[84:87], v10, s[52:55], s77 offen offset:2048 sc1\n"
    "v_fma_mix_f32 v57, v100, v25, v17 op_sel:[1,0,0] op_sel_hi:[1,0,0]\n"
    "v_cvt_pk_bf16_f32 v99, v54, v55\n"
    "v_fma_mix_f32 v58, v101, v26, v18 op_sel:[0,0,0] op_sel_hi:[1,0,0]\n"
    "v_mfma_f32_32x32x16_bf16 v[208:223], a[216:219], v[92:95], v[208:223]\n"
    "ds_read_b64_tr_b16 a[202:203], v7 offset:1536\n"
    "ds_read_b128 v[40:43], v8 offset:3392\n"
    "v_fma_mix_f32 v59, v101, v27, v19 op_sel:[1,0,0] op_sel_hi:[1,0,0]\n"
    "v_cvt_pk_bf16_f32 v100, v56, v57\n"
    "v_fma_mix_f32 v60, v102, v28, v20 op_sel:[0,0,0] op_sel_hi:[1,0,0]\n"
    "v_fma_mix_f32 v61, v102, v29, v21 op_sel:[1,0,0] op_sel_hi:[1,0,0]\n"
    "v_mfma_f32_32x32x16_bf16 v[224:239], a[220:223], v[88:91], v[224:239]\n"
    "ds_read_b64_tr_b16 a[204:205], v7 offset:768\n"
    "v_cvt_pk_bf16_f32 v101, v58, v59\n"
    "v_fma_mix_f32 v62, v103, v30, v22 op_sel:[0,0,0] op_sel_hi:[1,0,0]\n"
    "v_fma_mix_f32 v63, v103, v31, v23 op_sel:[1,0,0] op_sel_hi:[1,0,0]\n"
    "v_cvt_pk_bf16_f32 v102, v60, v61\n"
    "v_mfma_f32_32x32x16_bf16 v[240:255], a[220:223], v[92:95], v[240:255]\n"
    "ds_read_b64_tr_b16 a[206:207], v7 offset:1792\n"
    "ds_read_b128 v[44:47], v8 offset:3408\n"
    "v_cvt_pk_bf16_f32 v103, v62, v63\n"
    "s_waitcnt lgkmcnt(10)\n"
    "v_mfma_f32_32x32x16_bf16 v[128:143], a[192:195], v[96:99], v[128:143]\n"
    "ds_read_b64_tr_b16 a[208:209], v7 offset:4096\n"
    "s_waitcnt lgkmcnt(4)\n"
    "s_waitcnt vmcnt(15)\n"
    "v_fma_mix_f32 v48, v104, v40, v32 op_sel:[0,0,0] op_sel_hi:[1,0,0]\n"
    "v_fma_mix_f32 v49, v104, v41, v33 op_sel:[1,0,0] op_sel_hi:[1,0,0]\n"
    "v_fma_mix_f32 v50, v105, v42, v34 op_sel:[0,0,0] op_sel_hi:[1,0,0]\n"
    "s_add_u32 s68, s69, 0x4000\n"
    "s_add_u32 m0, s69, 0x14000\n"
    "v_mfma_f32_32x32x16_bf16 v[144:159], a[192:195], v[100:103], v[144:159]\n"
    "ds_read_b64_tr_b16 a[210:211], v7 offset:5120\n"
    "ds_read_b128 v[16:19], v8 offset:1408\n"
    "buffer_load_dwordx4 v15, s[44:47], s68 offen offset:0 lds\n"
    "v_fma_mix_f32 v51, v105, v43, v35 op_sel:[1,0,0] op_sel_hi:[1,0,0]\n"
    "v_cvt_pk_bf16_f32 v104, v48, v49\n"
    "s_waitcnt lgkmcnt(3)\n"
    "v_fma_mix_f32 v52, v106, v44, v36 op_sel:[0,0,0] op_sel_hi:[1,0,0]\n"
    "v_mfma_f32_32x32x16_bf16 v[160:175], a[196:199], v[96:99], v[160:175]\n"
    "ds_read_b64_tr_b16 a[212:213], v7 offset:4352\n"
    "s_mov_b32 s77, 0x6000\n"
    "buffer_load_dwordx4 v[88:91], v9, s[52:55], s77 offen offset:3072 sc1\n"
    "v_fma_mix_f32 v53, v106, v45, v37 op_sel:[1,0,0] op_sel_hi:[1,0,0]\n"
    "v_cvt_pk_bf16_f32 v105, v50, v51\n"
    "v_fma_mix_f32 v54, v107, v46, v38 op_sel:[0,0,0] op_sel_hi:[1,0,0]\n"
    "v_mfma_f32_32x32x16_bf16 v[176:191], a[196:199], v[100:103], v[176:191]\n"
    "ds_read_b64_tr_b16 a[214:215], v7 offset:5376\n"
    "ds_read_b128 v[20:23], v8 offset:1424\n"
    "buffer_load_dwordx4 v15, s[44:47], s68 offen offset:1024 lds\n"
    "v_fma_mix_f32 v55, v107, v47, v39 op_sel:[1,0,0] op_sel_hi:[1,0,0]\n"
    "v_cvt_pk_bf16_f32 v106, v52, v53\n"
    "s_waitcnt vmcnt(17)\n"
    "v_fma_mix_f32 v56, v108, v40, v32 op_sel:[0,0,0] op_sel_hi:[1,0,0]\n"
    "v_mfma_f32_32x32x16_bf16 v[192:207], a[200:203], v[96:99], v[192:207]\n"
    "ds_read_b64_tr_b16 a[216:217], v7 offset:4608\n"
    "buffer_load_dwordx4 v[92:95], v10, s[52:55], s77 offen offset:3072 sc1\n"
    "v_fma_mix_f32 v57, v108, v41, v33 op_sel:[1,0,0] op_sel_hi:[1,0,0]\n"
    "v_cvt_pk_bf16_f32 v107, v54, v55\n"
    "v_fma_mix_f32 v58, v109, v42, v34 op_sel:[0,0,0] op_sel_hi:[1,0,0]\n"
    "v_fma_mix_f32 v59, v109, v43, v35 op_sel:[1,0,0] op_sel_hi:[1,0,0]\n"
    "v_mfma_f32_32x32x16_bf16 v[208:223], a[200:203], v[100:103], v[208:223]\n"
    "ds_read_b64_tr_b16 a[218:219], v7 offset:5632\n"
    "ds_read_b128 v[24:27], v8 offset:3456\n"
    "buffer_load_dwordx4 v15, s[44:47], s68 offen offset:2048 lds\n"
    "v_cvt_pk_bf16_f32 v108, v56, v57\n"
    "v_fma_mix_f32 v60, v110, v44, v36 op_sel:[0,0,0] op_sel_hi:[1,0,0]\n"
    "v_fma_mix_f32 v61, v110, v45, v37 op_sel:[1,0,0] op_sel_hi:[1,0,0]\n"
    "v_mfma_f32_32x32x16_bf16 v[224:239], a[204:207], v[96:99], v[224:239]\n"
    "ds_read_b64_tr_b16 a[220:221], v7 offset:4864\n"
    "buffer_load_dwordx4 v15, s[44:47], s68 offen offset:3072 lds\n"
    "v_cvt_pk_bf16_f32 v109, v58, v59\n"
    "v_fma_mix_f32 v62, v111, v46, v38 op_sel:[0,0,0] op_sel_hi:[1,0,0]\n"
    "v_fma_mix_f32 v63, v111, v47, v39 op_sel:[1,0,0] op_sel_hi:[1,0,0]\n"
    "v_cvt_pk_bf16_f32 v110, v60, v61\n"
    "v_mfma_f32_32x32x16_bf16 v[240:255], a[204:207], v[100:103], v[240:255]\n"
    "ds_read_b64_tr_b16 a[222:223], v7 offset:5888\n"
    "ds_read_b128 v[28:31], v8 offset:3472\n"
    "v_cvt_pk_bf16_f32 v111, v62, v63\n"
    "s_waitcnt lgkmcnt(10)\n"
    "v_mfma_f32_32x32x16_bf16 v[128:143], a[208:211], v[104:107], v[128:143]\n"
    "ds_read_b64_tr_b16 a[192:193], v7 offset:8192\n"
    "s_waitcnt lgkmcnt(4)\n"
    "s_waitcnt vmcnt(19)\n"
    "v_fma_mix_f32 v48, v112, v24, v16 op_sel:[0,0,0] op_sel_hi:[1,0,0]\n"
    "v_fma_mix_f32 v49, v112, v25, v17 op_sel:[1,0,0] op_sel_hi:[1,0,0]\n"
    "v_fma_mix_f32 v50, v113, v26, v18 op_sel:[0,0,0] op_sel_hi:[1,0,0]\n"
    "v_fma_mix_f32 v51, v113, v27, v19 op_sel:[1,0,0] op_sel_hi:[1,0,0]\n"
    "v_mfma_f32_32x32x16_bf16 v[144:159], a[208:211], v[108:111], v[144:159]\n"
    "ds_read_b64_tr_b16 a[194:195], v7 offset:9216\n"
    "ds_read_b128 v[32:35], v8 offset:1472\n"
    "v_cvt_pk_bf16_f32 v112, v48, v49\n"
    "s_waitcnt lgkmcnt(3)\n"
    "v_fma_mix_f32 v52, v114, v28, v20 op_sel:[0,0,0] op_sel_hi:[1,0,0]\n"
    "v_fma_mix_f32 v53, v114, v29, v21 op_sel:[1,0,0] op_sel_hi:[1,0,0]\n"
    "v_mfma_f32_32x32x16_bf16 v[160:175], a[212:215], v[104:107], v[160:175]\n"
    "ds_read_b64_tr_b16 a[196:197], v7 offset:8448\n"
    "s_mov_b32 s77, 0x7000\n"
    "buffer_load_dwordx4 v[96:99], v9, s[52:55], s77 offen offset:0 sc1\n"
    "v_cvt_pk_bf16_f32 v113, v50, v51\n"
    "v_fma_mix_f32 v54, v115, v30, v22 op_sel:[0,0,0] op_sel_hi:[1,0,0]\n"
    "v_mfma_f32_32x32x16_bf16 v[176:191], a[212:215], v[108:111], v[176:191]\n"
    "ds_read_b64_tr_b16 a[198:199], v7 offset:9472\n"
    "ds_read_b128 v[36:39], v8 offset:1488\n"
    "v_fma_mix_f32 v55, v115, v31, v23 op_sel:[1,0,0] op_sel_hi:[1,0,0]\n"
    "v_cvt_pk_bf16_f32 v114, v52, v53\n"
    "s_waitcnt vmcnt(19)\n"
    "v_fma_mix_f32 v56, v116, v24, v16 op_sel:[0,0,0] op_sel_hi:[1,0,0]\n"
    "v_fma_mix_f32 v57, v116, v25, v17 op_sel:[1,0,0] op_sel_hi:[1,0,0]\n"
    "v_mfma_f32_32x32x16_bf16 v[192:207], a[216:219], v[104:107], v[192:207]\n"
    "ds_read_b64_tr_b16 a[200:201], v7 offset:8704\n"
    "buffer_load_dwordx4 v[100:103], v10, s[52:55], s77 offen offset:0 sc1\n"
    "v_cvt_pk_bf16_f32 v115, v54, v55\n"
    "v_fma_mix_f32 v58, v117, v26, v18 op_sel:[0,0,0] op_sel_hi:[1,0,0]\n"
    "v_fma_mix_f32 v59, v117, v27, v19 op_sel:[1,0,0] op_sel_hi:[1,0,0]\n"
    "v_mfma_f32_32x32x16_bf16 v[208:223], a[216:219], v[108:111], v[208:223]\n"
    "ds_read_b64_tr_b16 a[202:203], v7 offset:9728\n"
    "ds_read_b128 v[40:43], v8 offset:3520\n"
    "v_cvt_pk_bf16_f32 v116, v56, v57\n"
    "v_fma_mix_f32 v60, v118, v28, v20 op_sel:[0,0,0] op_sel_hi:[1,0,0]\n"
    "v_fma_mix_f32 v61, v118, v29, v21 op_sel:[1,0,0] op_sel_hi:[1,0,0]\n"
    "v_mfma_f32_32x32x16_bf16 v[224:239], a[220:223], v[104:107], v[224:239]\n"
    "ds_read_b64_tr_b16 a[204:205], v7 offset:8960\n"
    "v_cvt_pk_bf16_f32 v117, v58, v59\n"
    "v_fma_mix_f32 v62, v119, v30, v22 op_sel:[0,0,0] op_sel_hi:[1,0,0]\n"
    "v_fma_mix_f32 v63, v119, v31, v23 op_sel:[1,0,0] op_sel_hi:[1,0,0]\n"
    "v_cvt_pk_bf16_f32 v118, v60, v61\n"
    "v_mfma_f32_32x32x16_bf16 v[240:255], a[220:223], v[108:111], v[240:255]\n"
    "ds_read_b64_tr_b16 a[206:207], v7 offset:9984\n"
    "ds_read_b128 v[44:47], v8 offset:3536\n"
    "v_cvt_pk_bf16_f32 v119, v62, v63\n"
    "s_waitcnt lgkmcnt(10)\n"
    "v_mfma_f32_32x32x16_bf16 v[128:143], a[192:195], v[112:115], v[128:143]\n"
    "ds_read_b64_tr_b16 a[208:209], v7 offset:12288\n"
    "s_waitcnt lgkmcnt(4)\n"
    "s_waitcnt vmcnt(18)\n"
    "v_fma_mix_f32 v48, v120, v40, v32 op_sel:[0,0,0] op_sel_hi:[1,0,0]\n"
    "v_fma_mix_f32 v49, v120, v41, v33 op_sel:[1,0,0] op_sel_hi:[1,0,0]\n"
    "v_fma_mix_f32 v50, v121, v42, v34 op_sel:[0,0,0] op_sel_hi:[1,0,0]\n"
    "v_fma_mix_f32 v51, v121, v43, v35 op_sel:[1,0,0] op_sel_hi:[1,0,0]\n"
    "v_mfma_f32_32x32x16_bf16 v[144:159], a[192:195], v[116:119], v[144:159]\n"
    "ds_read_b64_tr_b16 a[210:211], v7 offset:13312\n"
    "ds_read_b128 v[16:19], v8 offset:1536\n"
    "v_cvt_pk_bf16_f32 v120, v48, v49\n"
    "s_waitcnt lgkmcnt(3)\n"
    "v_fma_mix_f32 v52, v122, v44, v36 op_sel:[0,0,0] op_sel_hi:[1,0,0]\n"
    "v_fma_mix_f32 v53, v122, v45, v37 op_sel:[1,0,0] op_sel_hi:[1,0,0]\n"
    "v_mfma_f32_32x32x16_bf16 v[160:175], a[196:199], v[112:115], v[160:175]\n"
    "ds_read_b64_tr_b16 a[212:213], v7 offset:12544\n"
    "s_mov_b32 s77, 0x7000\n"
    "buffer_load_dwordx4 v[104:107], v9, s[52:55], s77 offen offset:1024 sc1\n"
    "v_cvt_pk_bf16_f32 v121, v50, v51\n"
    "v_fma_mix_f32 v54, v123, v46, v38 op_sel:[0,0,0] op_sel_hi:[1,0,0]\n"
    "v_mfma_f32_32x32x16_bf16 v[176:191], a[196:199], v[116:119], v[176:191]\n"
    "ds_read_b64_tr_b16 a[214:215], v7 offset:13568\n"
    "ds_read_b128 v[20:23], v8 offset:1552\n"
    "v_fma_mix_f32 v55, v123, v47, v39 op_sel:[1,0,0] op_sel_hi:[1,0,0]\n"
    "v_cvt_pk_bf16_f32 v122, v52, v53\n"
    "s_waitcnt vmcnt(17)\n"
    "v_fma_mix_f32 v56, v124, v40, v32 op_sel:[0,0,0] op_sel_hi:[1,0,0]\n"
    "v_fma_mix_f32 v57, v124, v41, v33 op_sel:[1,0,0] op_sel_hi:[1,0,0]\n"
    "v_mfma_f32_32x32x16_bf16 v[192:207], a[200:203], v[112:115], v[192:207]\n"
    "ds_read_b64_tr_b16 a[216:217], v7 offset:12800\n"
    "buffer_load_dwordx4 v[108:111], v10, s[52:55], s77 offen offset:1024 sc1\n"
    "v_cvt_pk_bf16_f32 v123, v54, v55\n"
    "v_fma_mix_f32 v58, v125, v42, v34 op_sel:[0,0,0] op_sel_hi:[1,0,0]\n"
    "v_fma_mix_f32 v59, v125, v43, v35 op_sel:[1,0,0] op_sel_hi:[1,0,0]\n"
    "v_mfma_f32_32x32x16_bf16 v[208:223], a[200:203], v[116:119], v[208:223]\n"
    "ds_read_b64_tr_b16 a[218:219], v7 offset:13824\n"
    "ds_read_b128 v[24:27], v8 offset:3584\n"
    "v_cvt_pk_bf16_f32 v124, v56, v57\n"
    "v_fma_mix_f32 v60, v126, v44, v36 op_sel:[0,0,0] op_sel_hi:[1,0,0]\n"
    "v_fma_mix_f32 v61, v126, v45, v37 op_sel:[1,0,0] op_sel_hi:[1,0,0]\n"
    "v_mfma_f32_32x32x16_bf16 v[224:239], a[204:207], v[112:115], v[224:239]\n"
    "ds_read_b64_tr_b16 a[220:221], v7 offset:13056\n"
    "v_cvt_pk_bf16_f32 v125, v58, v59\n"
    "v_fma_mix_f32 v62, v127, v46, v38 op_sel:[0,0,0] op_sel_hi:[1,0,0]\n"
    "v_fma_mix_f32 v63, v127, v47, v39 op_sel:[1,0,0] op_sel_hi:[1,0,0]\n"
    "v_cvt_pk_bf16_f32 v126, v60, v61\n"
    "v_mfma_f32_32x32x16_bf16 v[240:255], a[204:207], v[116:119], v[240:255]\n"
    "ds_read_b64_tr_b16 a[222:223], v7 offset:14080\n"
    "ds_read_b128 v[28:31], v8 offset:3600\n"
    "v_cvt_pk_bf16_f32 v127, v62, v63\n"
    "s_waitcnt lgkmcnt(10)\n"
    "v_mfma_f32_32x32x16_bf16 v[128:143], a[208:211], v[120:123], v[128:143]\n"
    "s_barrier\n"
    "v_add_u32 v7, 0x1c000, v6\n"
    "ds_read_b64_tr_b16 a[192:193], v7 offset:0\n"
    "s_waitcnt lgkmcnt(4)\n"
    "s_waitcnt vmcnt(15)\n"
    "v_fma_mix_f32 v48, v64, v24, v16 op_sel:[0,0,0] op_sel_hi:[1,0,0]\n"
    "v_fma_mix_f32 v49, v64, v25, v17 op_sel:[1,0,0] op_sel_hi:[1,0,0]\n"
    "v_mfma_f32_32x32x16_bf16 v[144:159], a[208:211], v[124:127], v[144:159]\n"
    "ds_read_b64_tr_b16 a[194:195], v7 offset:1024\n"
    "ds_read_b128 v[32:35], v8 offset:1600\n"
    "v_fma_mix_f32 v50, v65, v26, v18 op_sel:[0,0,0] op_sel_hi:[1,0,0]\n"
    "v_fma_mix_f32 v51, v65, v27, v19 op_sel:[1,0,0] op_sel_hi:[1,0,0]\n"
    "v_cvt_pk_bf16_f32 v64, v48, v49\n"
    "s_waitcnt lgkmcnt(3)\n"
    "v_fma_mix_f32 v52, v66, v28, v20 op_sel:[0,0,0] op_sel_hi:[1,0,0]\n"
    "v_mfma_f32_32x32x16_bf16 v[160:175], a[212:215], v[120:123], v[160:175]\n"
    "ds_read_b64_tr_b16 a[196:197], v7 offset:256\n"
    "s_mov_b32 s77, 0x7000\n"
    "buffer_load_dwordx4 v[112:115], v9, s[52:55], s77 offen offset:2048 sc1\n"
    "v_fma_mix_f32 v53, v66, v29, v21 op_sel:[1,0,0] op_sel_hi:[1,0,0]\n"
    "v_cvt_pk_bf16_f32 v65, v50, v51\n"
    "v_mfma_f32_32x32x16_bf16 v[176:191], a[212:215], v[124:127], v[176:191]\n"
    "ds_read_b64_tr_b16 a[198:199], v7 offset:1280\n"
    "ds_read_b128 v[36:39], v8 offset:1616\n"
    "v_fma_mix_f32 v54, v67, v30, v22 op_sel:[0,0,0] op_sel_hi:[1,0,0]\n"
    "v_fma_mix_f32 v55, v67, v31, v23 op_sel:[1,0,0] op_sel_hi:[1,0,0]\n"
    "v_cvt_pk_bf16_f32 v66, v52, v53\n"
    "s_waitcnt vmcnt(15)\n"
    "v_fma_mix_f32 v56, v68, v24, v16 op_sel:[0,0,0] op_sel_hi:[1,0,0]\n"
    "v_mfma_f32_32x32x16_bf16 v[192:207], a[216:219], v[120:123], v[192:207]\n"
    "ds_read_b64_tr_b16 a[200:201], v7 offset:512\n"
    "buffer_load_dwordx4 v[116:119], v10, s[52:55], s77 offen offset:2048 sc1\n"
    "v_fma_mix_f32 v57, v68, v25, v17 op_sel:[1,0,0] op_sel_hi:[1,0,0]\n"
    "v_cvt_pk_bf16_f32 v67, v54, v55\n"
    "v_fma_mix_f32 v58, v69, v26, v18 op_sel:[0,0,0] op_sel_hi:[1,0,0]\n"
    "v_mfma_f32_32x32x16_bf16 v[208:223], a[216:219], v[124:127], v[208:223]\n"
    "ds_read_b64_tr_b16 a[202:203], v7 offset:1536\n"
    "ds_read_b128 v[40:43], v8 offset:3648\n"
    "v_fma_mix_f32 v59, v69, v27, v19 op_sel:[1,0,0] op_sel_hi:[1,0,0]\n"
    "v_cvt_pk_bf16_f32 v68, v56, v57\n"
    "v_fma_mix_f32 v60, v70, v28, v20 op_sel:[0,0,0] op_sel_hi:[1,0,0]\n"
    "v_fma_mix_f32 v61, v70, v29, v21 op_sel:[1,0,0] op_sel_hi:[1,0,0]\n"
    "v_mfma_f32_32x32x16_bf16 v[224:239], a[220:223], v[120:123], v[224:239]\n"
    "ds_read_b64_tr_b16 a[204:205], v7 offset:768\n"
    "v_cvt_pk_bf16_f32 v69, v58, v59\n"
    "v_fma_mix_f32 v62, v71, v30, v22 op_sel:[0,0,0] op_sel_hi:[1,0,0]\n"
    "v_fma_mix_f32 v63, v71, v31, v23 op_sel:[1,0,0] op_sel_hi:[1,0,0]\n"
    "v_cvt_pk_bf16_f32 v70, v60, v61\n"
    "v_mfma_f32_32x32x16_bf16 v[240:255], a[220:223], v[124:127], v[240:255]\n"
    "ds_read_b64_tr_b16 a[206:207], v7 offset:1792\n"
    "ds_read_b128 v[44:47], v8 offset:3664\n"
    "v_cvt_pk_bf16_f32 v71, v62, v63\n"
    "s_waitcnt lgkmcnt(10)\n"
    "v_mfma_f32_32x32x16_bf16 v[128:143], a[192:195], v[64:67], v[128:143]\n"
    "ds_read_b64_tr_b16 a[208:209], v7 offset:4096\n"
    "s_waitcnt lgkmcnt(4)\n"
    "s_waitcnt vmcnt(15)\n"
    "v_fma_mix_f32 v48, v72, v40, v32 op_sel:[0,0,0] op_sel_hi:[1,0,0]\n"
    "v_fma_mix_f32 v49, v72, v41, v33 op_sel:[1,0,0] op_sel_hi:[1,0,0]\n"
    "v_fma_mix_f32 v50, v73, v42, v34 op_sel:[0,0,0] op_sel_hi:[1,0,0]\n"
    "v_fma_mix_f32 v51, v73, v43, v35 op_sel:[1,0,0] op_sel_hi:[1,0,0]\n"
    "v_mfma_f32_32x32x16_bf16 v[144:159], a[192:195], v[68:71], v[144:159]\n"
    "ds_read_b64_tr_b16 a[210:211], v7 offset:5120\n"
    "ds_read_b128 v[16:19], v8 offset:1664\n"
    "v_cvt_pk_bf16_f32 v72, v48, v49\n"
    "s_waitcnt lgkmcnt(3)\n"
    "v_fma_mix_f32 v52, v74, v44, v36 op_sel:[0,0,0] op_sel_hi:[1,0,0]\n"
    "v_fma_mix_f32 v53, v74, v45, v37 op_sel:[1,0,0] op_sel_hi:[1,0,0]\n"
    "v_mfma_f32_32x32x16_bf16 v[160:175], a[196:199], v[64:67], v[160:175]\n"
    "ds_read_b64_tr_b16 a[212:213], v7 offset:4352\n"
    "s_mov_b32 s77, 0x7000\n"
    "buffer_load_dwordx4 v[120:123], v9, s[52:55], s77 offen offset:3072 sc1\n"
    "v_cvt_pk_bf16_f32 v73, v50, v51\n"
    "v_fma_mix_f32 v54, v75, v46, v38 op_sel:[0,0,0] op_sel_hi:[1,0,0]\n"
    "v_mfma_f32_32x32x16_bf16 v[176:191], a[196:199], v[68:71], v[176:191]\n"
    "ds_read_b64_tr_b16 a[214:215], v7 offset:5376\n"
    "ds_read_b128 v[20:23], v8 offset:1680\n"
    "v_fma_mix_f32 v55, v75, v47, v39 op_sel:[1,0,0] op_sel_hi:[1,0,0]\n"
    "v_cvt_pk_bf16_f32 v74, v52, v53\n"
    "s_waitcnt vmcnt(15)\n"
    "v_fma_mix_f32 v56, v76, v40, v32 op_sel:[0,0,0] op_sel_hi:[1,0,0]\n"
    "v_fma_mix_f32 v57, v76, v41, v33 op_sel:[1,0,0] op_sel_hi:[1,0,0]\n"
    "v_mfma_f32_32x32x16_bf16 v[192:207], a[200:203], v[64:67], v[192:207]\n"
    "ds_read_b64_tr_b16 a[216:217], v7 offset:4608\n"
    "buffer_load_dwordx4 v[124:127], v10, s[52:55], s77 offen offset:3072 sc1\n"
    "v_cvt_pk_bf16_f32 v75, v54, v55\n"
    "v_fma_mix_f32 v58, v77, v42, v34 op_sel:[0,0,0] op_sel_hi:[1,0,0]\n"
    "v_fma_mix_f32 v59, v77, v43, v35 op_sel:[1,0,0] op_sel_hi:[1,0,0]\n"
    "v_mfma_f32_32x32x16_bf16 v[208:223], a[200:203], v[68:71], v[208:223]\n"
    "ds_read_b64_tr_b16 a[218:219], v7 offset:5632\n"
    "ds_read_b128 v[24:27], v8 offset:3712\n"
    "v_cvt_pk_bf16_f32 v76, v56, v57\n"
    "v_fma_mix_f32 v60, v78, v44, v36 op_sel:[0,0,0] op_sel_hi:[1,0,0]\n"
    "v_fma_mix_f32 v61, v78, v45, v37 op_sel:[1,0,0] op_sel_hi:[1,0,0]\n"
    "v_mfma_f32_32x32x16_bf16 v[224:239], a[204:207], v[64:67], v[224:239]\n"
    "ds_read_b64_tr_b16 a[220:221], v7 offset:4864\n"
    "v_cvt_pk_bf16_f32 v77, v58, v59\n"
    "v_fma_mix_f32 v62, v79, v46, v38 op_sel:[0,0,0] op_sel_hi:[1,0,0]\n"
    "v_fma_mix_f32 v63, v79, v47, v39 op_sel:[1,0,0] op_sel_hi:[1,0,0]\n"
    "v_cvt_pk_bf16_f32 v78, v60, v61\n"
    "v_mfma_f32_32x32x16_bf16 v[240:255], a[204:207], v[68:71], v[240:255]\n"
    "ds_read_b64_tr_b16 a[222:223], v7 offset:5888\n"
    "ds_read_b128 v[28:31], v8 offset:3728\n"
    "v_cvt_pk_bf16_f32 v79, v62, v63\n"
    "s_waitcnt lgkmcnt(10)\n"
    "v_mfma_f32_32x32x16_bf16 v[128:143], a[208:211], v[72:75], v[128:143]\n"
    "ds_read_b64_tr_b16 a[192:193], v7 offset:8192\n"
    "s_waitcnt lgkmcnt(4)\n"
    "s_waitcnt vmcnt(15)\n"
    "v_fma_mix_f32 v48, v80, v24, v16 op_sel:[0,0,0] op_sel_hi:[1,0,0]\n"
    "v_fma_mix_f32 v49, v80, v25, v17 op_sel:[1,0,0] op_sel_hi:[1,0,0]\n"
    "v_fma_mix_f32 v50, v81, v26, v18 op_sel:[0,0,0] op_sel_hi:[1,0,0]\n"
    "v_fma_mix_f32 v51, v81, v27, v19 op_sel:[1,0,0] op_sel_hi:[1,0,0]\n"
    "v_mfma_f32_32x32x16_bf16 v[144:159], a[208:211], v[76:79], v[144:159]\n"
    "ds_read_b64_tr_b16 a[194:195], v7 offset:9216\n"
    "ds_read_b128 v[32:35], v8 offset:1728\n"
    "v_cvt_pk_bf16_f32 v80, v48, v49\n"
    "s_waitcnt lgkmcnt(3)\n"
    "v_fma_mix_f32 v52, v82, v28, v20 op_sel:[0,0,0] op_sel_hi:[1,0,0]\n"
    "v_fma_mix_f32 v53, v82, v29, v21 op_sel:[1,0,0] op_sel_hi:[1,0,0]\n"
    "v_mfma_f32_32x32x16_bf16 v[160:175], a[212:215], v[72:75], v[160:175]\n"
    "ds_read_b64_tr_b16 a[196:197], v7 offset:8448\n"
    "v_cvt_pk_bf16_f32 v81, v50, v51\n"
    "v_fma_mix_f32 v54, v83, v30, v22 op_sel:[0,0,0] op_sel_hi:[1,0,0]\n"
    "v_fma_mix_f32 v55, v83, v31, v23 op_sel:[1,0,0] op_sel_hi:[1,0,0]\n"
    "v_cvt_pk_bf16_f32 v82, v52, v53\n"
    "v_mfma_f32_32x32x16_bf16 v[176:191], a[212:215], v[76:79], v[176:191]\n"
    "ds_read_b64_tr_b16 a[198:199], v7 offset:9472\n"
    "ds_read_b128 v[36:39], v8 offset:1744\n"
    "s_waitcnt vmcnt(14)\n"
    "v_fma_mix_f32 v56, v84, v24, v16 op_sel:[0,0,0] op_sel_hi:[1,0,0]\n"
    "v_fma_mix_f32 v57, v84, v25, v17 op_sel:[1,0,0] op_sel_hi:[1,0,0]\n"
    "v_cvt_pk_bf16_f32 v83, v54, v55\n"
    "v_mfma_f32_32x32x16_bf16 v[192:207], a[216:219], v[72:75], v[192:207]\n"
    "ds_read_b64_tr_b16 a[200:201], v7 offset:8704\n"
    "v_fma_mix_f32 v58, v85, v26, v18 op_sel:[0,0,0] op_sel_hi:[1,0,0]\n"
    "v_fma_mix_f32 v59, v85, v27, v19 op_sel:[1,0,0] op_sel_hi:[1,0,0]\n"
    "v_cvt_pk_bf16_f32 v84, v56, v57\n"
    "v_fma_mix_f32 v60, v86, v28, v20 op_sel:[0,0,0] op_sel_hi:[1,0,0]\n"
    "v_mfma_f32_32x32x16_bf16 v[208:223], a[216:219], v[76:79], v[208:223]\n"
    "ds_read_b64_tr_b16 a[202:203], v7 offset:9728\n"
    "ds_read_b128 v[40:43], v8 offset:3776\n"
    "v_fma_mix_f32 v61, v86, v29, v21 op_sel:[1,0,0] op_sel_hi:[1,0,0]\n"
    "v_cvt_pk_bf16_f32 v85, v58, v59\n"
    "v_fma_mix_f32 v62, v87, v30, v22 op_sel:[0,0,0] op_sel_hi:[1,0,0]\n"
    "v_mfma_f32_32x32x16_bf16 v[224:239], a[220:223], v[72:75], v[224:239]\n"
    "ds_read_b64_tr_b16 a[204:205], v7 offset:8960\n"
    "v_fma_mix_f32 v63, v87, v31, v23 op_sel:[1,0,0] op_sel_hi:[1,0,0]\n"
    "v_cvt_pk_bf16_f32 v86, v60, v61\n"
    "v_cvt_pk_bf16_f32 v87, v62, v63\n"
    "v_mfma_f32_32x32x16_bf16 v[240:255], a[220:223], v[76:79], v[240:255]\n"
    "ds_read_b64_tr_b16 a[206:207], v7 offset:9984\n"
    "ds_read_b128 v[44:47], v8 offset:3792\n"
    "s_waitcnt lgkmcnt(10)\n"
    "v_mfma_f32_32x32x16_bf16 v[128:143], a[192:195], v[80:83], v[128:143]\n"
    "ds_read_b64_tr_b16 a[208:209], v7 offset:12288\n"
    "s_waitcnt lgkmcnt(4)\n"
    "s_waitcnt vmcnt(12)\n"
    "v_fma_mix_f32 v48, v88, v40, v32 op_sel:[0,0,0] op_sel_hi:[1,0,0]\n"
    "v_fma_mix_f32 v49, v88, v41, v33 op_sel:[1,0,0] op_sel_hi:[1,0,0]\n"
    "v_fma_mix_f32 v50, v89, v42, v34 op_sel:[0,0,0] op_sel_hi:[1,0,0]\n"
    "v_fma_mix_f32 v51, v89, v43, v35 op_sel:[1,0,0] op_sel_hi:[1,0,0]\n"
    "v_mfma_f32_32x32x16_bf16 v[144:159], a[192:195], v[84:87], v[144:159]\n"
    "ds_read_b64_tr_b16 a[210:211], v7 offset:13312\n"
    "ds_read_b128 v[16:19], v8 offset:1792\n"
    "v_cvt_pk_bf16_f32 v88, v48, v49\n"
    "s_waitcnt lgkmcnt(3)\n"
    "v_fma_mix_f32 v52, v90, v44, v36 op_sel:[0,0,0] op_sel_hi:[1,0,0]\n"
    "v_fma_mix_f32 v53, v90, v45, v37 op_sel:[1,0,0] op_sel_hi:[1,0,0]\n"
    "v_mfma_f32_32x32x16_bf16 v[160:175], a[196:199], v[80:83], v[160:175]\n"
    "ds_read_b64_tr_b16 a[212:213], v7 offset:12544\n"
    "v_cvt_pk_bf16_f32 v89, v50, v51\n"
    "v_fma_mix_f32 v54, v91, v46, v38 op_sel:[0,0,0] op_sel_hi:[1,0,0]\n"
    "v_fma_mix_f32 v55, v91, v47, v39 op_sel:[1,0,0] op_sel_hi:[1,0,0]\n"
    "v_cvt_pk_bf16_f32 v90, v52, v53\n"
    "v_mfma_f32_32x32x16_bf16 v[176:191], a[196:199], v[84:87], v[176:191]\n"
    "ds_read_b64_tr_b16 a[214:215], v7 offset:13568\n"
    "ds_read_b128 v[20:23], v8 offset:1808\n"
    "s_waitcnt vmcnt(10)\n"
    "v_fma_mix_f32 v56, v92, v40, v32 op_sel:[0,0,0] op_sel_hi:[1,0,0]\n"
    "v_fma_mix_f32 v57, v92, v41, v33 op_sel:[1,0,0] op_sel_hi:[1,0,0]\n"
    "v_cvt_pk_bf16_f32 v91, v54, v55\n"
    "v_mfma_f32_32x32x16_bf16 v[192:207], a[200:203], v[80:83], v[192:207]\n"
    "ds_read_b64_tr_b16 a[216:217], v7 offset:12800\n"
    "v_fma_mix_f32 v58, v93, v42, v34 op_sel:[0,0,0] op_sel_hi:[1,0,0]\n"
    "v_fma_mix_f32 v59, v93, v43, v35 op_sel:[1,0,0] op_sel_hi:[1,0,0]\n"
    "v_cvt_pk_bf16_f32 v92, v56, v57\n"
    "v_fma_mix_f32 v60, v94, v44, v36 op_sel:[0,0,0] op_sel_hi:[1,0,0]\n"
    "v_mfma_f32_32x32x16_bf16 v[208:223], a[200:203], v[84:87], v[208:223]\n"
    "ds_read_b64_tr_b16 a[218:219], v7 offset:13824\n"
    "ds_read_b128 v[24:27], v8 offset:3840\n"
    "v_fma_mix_f32 v61, v94, v45, v37 op_sel:[1,0,0] op_sel_hi:[1,0,0]\n"
    "v_cvt_pk_bf16_f32 v93, v58, v59\n"
    "v_fma_mix_f32 v62, v95, v46, v38 op_sel:[0,0,0] op_sel_hi:[1,0,0]\n"
    "v_mfma_f32_32x32x16_bf16 v[224:239], a[204:207], v[80:83], v[224:239]\n"
    "ds_read_b64_tr_b16 a[220:221], v7 offset:13056\n"
    "v_fma_mix_f32 v63, v95, v47, v39 op_sel:[1,0,0] op_sel_hi:[1,0,0]\n"
    "v_cvt_pk_bf16_f32 v94, v60, v61\n"
    "v_cvt_pk_bf16_f32 v95, v62, v63\n"
    "v_mfma_f32_32x32x16_bf16 v[240:255], a[204:207], v[84:87], v[240:255]\n"
    "ds_read_b64_tr_b16 a[222:223], v7 offset:14080\n"
    "ds_read_b128 v[28:31], v8 offset:3856\n"
    "s_waitcnt lgkmcnt(10)\n"
    "v_mfma_f32_32x32x16_bf16 v[128:143], a[208:211], v[88:91], v[128:143]\n"
    "s_barrier\n"
    "v_add_u32 v7, 0x20000, v6\n"
    "ds_read_b64_tr_b16 a[192:193], v7 offset:0\n"
    "s_waitcnt lgkmcnt(4)\n"
    "s_waitcnt vmcnt(7)\n"
    "v_fma_mix_f32 v48, v96, v24, v16 op_sel:[0,0,0] op_sel_hi:[1,0,0]\n"
    "v_fma_mix_f32 v49, v96, v25, v17 op_sel:[1,0,0] op_sel_hi:[1,0,0]\n"
    "v_mfma_f32_32x32x16_bf16 v[144:159], a[208:211], v[92:95], v[144:159]\n"
    "ds_read_b64_tr_b16 a[194:195], v7 offset:1024\n"
    "ds_read_b128 v[32:35], v8 offset:1856\n"
    "v_fma_mix_f32 v50, v97, v26, v18 op_sel:[0,0,0] op_sel_hi:[1,0,0]\n"
    "v_fma_mix_f32 v51, v97, v27, v19 op_sel:[1,0,0] op_sel_hi:[1,0,0]\n"
    "v_cvt_pk_bf16_f32 v96, v48, v49\n"
    "v_mfma_f32_32x32x16_bf16 v[160:175], a[212:215], v[88:91], v[160:175]\n"
    "ds_read_b64_tr_b16 a[196:197], v7 offset:256\n"
    "s_waitcnt lgkmcnt(4)\n"
    "v_fma_mix_f32 v52, v98, v28, v20 op_sel:[0,0,0] op_sel_hi:[1,0,0]\n"
    "v_fma_mix_f32 v53, v98, v29, v21 op_sel:[1,0,0] op_sel_hi:[1,0,0]\n"
    "v_cvt_pk_bf16_f32 v97, v50, v51\n"
    "v_fma_mix_f32 v54, v99, v30, v22 op_sel:[0,0,0] op_sel_hi:[1,0,0]\n"
    "v_mfma_f32_32x32x16_bf16 v[176:191], a[212:215], v[92:95], v[176:191]\n"
    "ds_read_b64_tr_b16 a[198:199], v7 offset:1280\n"
    "ds_read_b128 v[36:39], v8 offset:1872\n"
    "v_fma_mix_f32 v55, v99, v31, v23 op_sel:[1,0,0] op_sel_hi:[1,0,0]\n"
    "v_cvt_pk_bf16_f32 v98, v52, v53\n"
    "s_waitcnt vmcnt(6)\n"
    "v_fma_mix_f32 v56, v100, v24, v16 op_sel:[0,0,0] op_sel_hi:[1,0,0]\n"
    "v_fma_mix_f32 v57, v100, v25, v17 op_sel:[1,0,0] op_sel_hi:[1,0,0]\n"
    "v_mfma_f32_32x32x16_bf16 v[192:207], a[216:219], v[88:91], v[192:207]\n"
    "ds_read_b64_tr_b16 a[200:201], v7 offset:512\n"
    "v_cvt_pk_bf16_f32 v99, v54, v55\n"
    "v_fma_mix_f32 v58, v101, v26, v18 op_sel:[0,0,0] op_sel_hi:[1,0,0]\n"
    "v_fma_mix_f32 v59, v101, v27, v19 op_sel:[1,0,0] op_sel_hi:[1,0,0]\n"
    "v_cvt_pk_bf16_f32 v100, v56, v57\n"
    "v_mfma_f32_32x32x16_bf16 v[208:223], a[216:219], v[92:95], v[208:223]\n"
    "ds_read_b64_tr_b16 a[202:203], v7 offset:1536\n"
    "ds_read_b128 v[40:43], v8 offset:3904\n"
    "v_fma_mix_f32 v60, v102, v28, v20 op_sel:[0,0,0] op_sel_hi:[1,0,0]\n"
    "v_fma_mix_f32 v61, v102, v29, v21 op_sel:[1,0,0] op_sel_hi:[1,0,0]\n"
    "v_cvt_pk_bf16_f32 v101, v58, v59\n"
    "v_mfma_f32_32x32x16_bf16 v[224:239], a[220:223], v[88:91], v[224:239]\n"
    "ds_read_b64_tr_b16 a[204:205], v7 offset:768\n"
    "v_fma_mix_f32 v62, v103, v30, v22 op_sel:[0,0,0] op_sel_hi:[1,0,0]\n"
    "v_fma_mix_f32 v63, v103, v31, v23 op_sel:[1,0,0] op_sel_hi:[1,0,0]\n"
    "v_cvt_pk_bf16_f32 v102, v60, v61\n"
    "v_cvt_pk_bf16_f32 v103, v62, v63\n"
    "v_mfma_f32_32x32x16_bf16 v[240:255], a[220:223], v[92:95], v[240:255]\n"
    "ds_read_b64_tr_b16 a[206:207], v7 offset:1792\n"
    "ds_read_b128 v[44:47], v8 offset:3920\n"
    "s_waitcnt lgkmcnt(10)\n"
    "v_mfma_f32_32x32x16_bf16 v[128:143], a[192:195], v[96:99], v[128:143]\n"
    "ds_read_b64_tr_b16 a[208:209], v7 offset:4096\n"
    "s_waitcnt lgkmcnt(4)\n"
    "s_waitcnt vmcnt(5)\n"
    "v_fma_mix_f32 v48, v104, v40, v32 op_sel:[0,0,0] op_sel_hi:[1,0,0]\n"
    "v_fma_mix_f32 v49, v104, v41, v33 op_sel:[1,0,0] op_sel_hi:[1,0,0]\n"
    "v_fma_mix_f32 v50, v105, v42, v34 op_sel:[0,0,0] op_sel_hi:[1,0,0]\n"
    "v_fma_mix_f32 v51, v105, v43, v35 op_sel:[1,0,0] op_sel_hi:[1,0,0]\n"
    "v_mfma_f32_32x32x16_bf16 v[144:159], a[192:195], v[100:103], v[144:159]\n"
    "ds_read_b64_tr_b16 a[210:211], v7 offset:5120\n"
    "ds_read_b128 v[16:19], v8 offset:1920\n"
    "v_cvt_pk_bf16_f32 v104, v48, v49\n"
    "s_waitcnt lgkmcnt(3)\n"
    "v_fma_mix_f32 v52, v106, v44, v36 op_sel:[0,0,0] op_sel_hi:[1,0,0]\n"
    "v_fma_mix_f32 v53, v106, v45, v37 op_sel:[1,0,0] op_sel_hi:[1,0,0]\n"
    "v_mfma_f32_32x32x16_bf16 v[160:175], a[196:199], v[96:99], v[160:175]\n"
    "ds_read_b64_tr_b16 a[212:213], v7 offset:4352\n"
    "v_cvt_pk_bf16_f32 v105, v50, v51\n"
    "v_fma_mix_f32 v54, v107, v46, v38 op_sel:[0,0,0] op_sel_hi:[1,0,0]\n"
    "v_fma_mix_f32 v55, v107, v47, v39 op_sel:[1,0,0] op_sel_hi:[1,0,0]\n"
    "v_cvt_pk_bf16_f32 v106, v52, v53\n"
    "v_mfma_f32_32x32x16_bf16 v[176:191], a[196:199], v[100:103], v[176:191]\n"
    "ds_read_b64_tr_b16 a[214:215], v7 offset:5376\n"
    "ds_read_b128 v[20:23], v8 offset:1936\n"
    "s_waitcnt vmcnt(4)\n"
    "v_fma_mix_f32 v56, v108, v40, v32 op_sel:[0,0,0] op_sel_hi:[1,0,0]\n"
    "v_fma_mix_f32 v57, v108, v41, v33 op_sel:[1,0,0] op_sel_hi:[1,0,0]\n"
    "v_cvt_pk_bf16_f32 v107, v54, v55\n"
    "v_mfma_f32_32x32x16_bf16 v[192:207], a[200:203], v[96:99], v[192:207]\n"
    "ds_read_b64_tr_b16 a[216:217], v7 offset:4608\n"
    "v_fma_mix_f32 v58, v109, v42, v34 op_sel:[0,0,0] op_sel_hi:[1,0,0]\n"
    "v_fma_mix_f32 v59, v109, v43, v35 op_sel:[1,0,0] op_sel_hi:[1,0,0]\n"
    "v_cvt_pk_bf16_f32 v108, v56, v57\n"
    "v_fma_mix_f32 v60, v110, v44, v36 op_sel:[0,0,0] op_sel_hi:[1,0,0]\n"
    "v_mfma_f32_32x32x16_bf16 v[208:223], a[200:203], v[100:103], v[208:223]\n"
    "ds_read_b64_tr_b16 a[218:219], v7 offset:5632\n"
    "ds_read_b128 v[24:27], v8 offset:3968\n"
    "v_fma_mix_f32 v61, v110, v45, v37 op_sel:[1,0,0] op_sel_hi:[1,0,0]\n"
    "v_cvt_pk_bf16_f32 v109, v58, v59\n"
    "v_fma_mix_f32 v62, v111, v46, v38 op_sel:[0,0,0] op_sel_hi:[1,0,0]\n"
    "v_mfma_f32_32x32x16_bf16 v[224:239], a[204:207], v[96:99], v[224:239]\n"
    "ds_read_b64_tr_b16 a[220:221], v7 offset:4864\n"
    "v_fma_mix_f32 v63, v111, v47, v39 op_sel:[1,0,0] op_sel_hi:[1,0,0]\n"
    "v_cvt_pk_bf16_f32 v110, v60, v61\n"
    "v_cvt_pk_bf16_f32 v111, v62, v63\n"
    "v_mfma_f32_32x32x16_bf16 v[240:255], a[204:207], v[100:103], v[240:255]\n"
    "ds_read_b64_tr_b16 a[222:223], v7 offset:5888\n"
    "ds_read_b128 v[28:31], v8 offset:3984\n"
    "s_waitcnt lgkmcnt(10)\n"
    "v_mfma_f32_32x32x16_bf16 v[128:143], a[208:211], v[104:107], v[128:143]\n"
    "ds_read_b64_tr_b16 a[192:193], v7 offset:8192\n"
    "s_waitcnt lgkmcnt(4)\n"
    "s_waitcnt vmcnt(3)\n"
    "v_fma_mix_f32 v48, v112, v24, v16 op_sel:[0,0,0] op_sel_hi:[1,0,0]\n"
    "v_fma_mix_f32 v49, v112, v25, v17 op_sel:[1,0,0] op_sel_hi:[1,0,0]\n"
    "v_fma_mix_f32 v50, v113, v26, v18 op_sel:[0,0,0] op_sel_hi:[1,0,0]\n"
    "v_fma_mix_f32 v51, v113, v27, v19 op_sel:[1,0,0] op_sel_hi:[1,0,0]\n"
    "v_mfma_f32_32x32x16_bf16 v[144:159], a[208:211], v[108:111], v[144:159]\n"
    "ds_read_b64_tr_b16 a[194:195], v7 offset:9216\n"
    "ds_read_b128 v[32:35], v8 offset:1984\n"
    "v_cvt_pk_bf16_f32 v112, v48, v49\n"
    "s_waitcnt lgkmcnt(3)\n"
    "v_fma_mix_f32 v52, v114, v28, v20 op_sel:[0,0,0] op_sel_hi:[1,0,0]\n"
    "v_fma_mix_f32 v53, v114, v29, v21 op_sel:[1,0,0] op_sel_hi:[1,0,0]\n"
    "v_mfma_f32_32x32x16_bf16 v[160:175], a[212:215], v[104:107], v[160:175]\n"
    "ds_read_b64_tr_b16 a[196:197], v7 offset:8448\n"
    "v_cvt_pk_bf16_f32 v113, v50, v51\n"
    "v_fma_mix_f32 v54, v115, v30, v22 op_sel:[0,0,0] op_sel_hi:[1,0,0]\n"
    "v_fma_mix_f32 v55, v115, v31, v23 op_sel:[1,0,0] op_sel_hi:[1,0,0]\n"
    "v_cvt_pk_bf16_f32 v114, v52, v53\n"
    "v_mfma_f32_32x32x16_bf16 v[176:191], a[212:215], v[108:111], v[176:191]\n"
    "ds_read_b64_tr_b16 a[198:199], v7 offset:9472\n"
    "ds_read_b128 v[36:39], v8 offset:2000\n"
    "s_waitcnt vmcnt(2)\n"
    "v_fma_mix_f32 v56, v116, v24, v16 op_sel:[0,0,0] op_sel_hi:[1,0,0]\n"
    "v_fma_mix_f32 v57, v116, v25, v17 op_sel:[1,0,0] op_sel_hi:[1,0,0]\n"
    "v_cvt_pk_bf16_f32 v115, v54, v55\n"
    "v_mfma_f32_32x32x16_bf16 v[192:207], a[216:219], v[104:107], v[192:207]\n"
    "ds_read_b64_tr_b16 a[200:201], v7 offset:8704\n"
    "v_fma_mix_f32 v58, v117, v26, v18 op_sel:[0,0,0] op_sel_hi:[1,0,0]\n"
    "v_fma_mix_f32 v59, v117, v27, v19 op_sel:[1,0,0] op_sel_hi:[1,0,0]\n"
    "v_cvt_pk_bf16_f32 v116, v56, v57\n"
    "v_fma_mix_f32 v60, v118, v28, v20 op_sel:[0,0,0] op_sel_hi:[1,0,0]\n"
    "v_mfma_f32_32x32x16_bf16 v[208:223], a[216:219], v[108:111], v[208:223]\n"
    "ds_read_b64_tr_b16 a[202:203], v7 offset:9728\n"
    "ds_read_b128 v[40:43], v8 offset:4032\n"
    "v_fma_mix_f32 v61, v118, v29, v21 op_sel:[1,0,0] op_sel_hi:[1,0,0]\n"
    "v_cvt_pk_bf16_f32 v117, v58, v59\n"
    "v_fma_mix_f32 v62, v119, v30, v22 op_sel:[0,0,0] op_sel_hi:[1,0,0]\n"
    "v_mfma_f32_32x32x16_bf16 v[224:239], a[220:223], v[104:107], v[224:239]\n"
    "ds_read_b64_tr_b16 a[204:205], v7 offset:8960\n"
    "v_fma_mix_f32 v63, v119, v31, v23 op_sel:[1,0,0] op_sel_hi:[1,0,0]\n"
    "v_cvt_pk_bf16_f32 v118, v60, v61\n"
    "v_cvt_pk_bf16_f32 v119, v62, v63\n"
    "v_mfma_f32_32x32x16_bf16 v[240:255], a[220:223], v[108:111], v[240:255]\n"
    "ds_read_b64_tr_b16 a[206:207], v7 offset:9984\n"
    "ds_read_b128 v[44:47], v8 offset:4048\n"
    "s_waitcnt lgkmcnt(10)\n"
    "v_mfma_f32_32x32x16_bf16 v[128:143], a[192:195], v[112:115], v[128:143]\n"
    "ds_read_b64_tr_b16 a[208:209], v7 offset:12288\n"
    "s_waitcnt lgkmcnt(4)\n"
    "s_waitcnt vmcnt(1)\n"
    "v_fma_mix_f32 v48, v120, v40, v32 op_sel:[0,0,0] op_sel_hi:[1,0,0]\n"
    "v_fma_mix_f32 v49, v120, v41, v33 op_sel:[1,0,0] op_sel_hi:[1,0,0]\n"
    "v_fma_mix_f32 v50, v121, v42, v34 op_sel:[0,0,0] op_sel_hi:[1,0,0]\n"
    "v_mfma_f32_32x32x16_bf16 v[144:159], a[192:195], v[116:119], v[144:159]\n"
    "ds_read_b64_tr_b16 a[210:211], v7 offset:13312\n"
    "v_fma_mix_f32 v51, v121, v43, v35 op_sel:[1,0,0] op_sel_hi:[1,0,0]\n"
    "v_cvt_pk_bf16_f32 v120, v48, v49\n"
    "s_waitcnt lgkmcnt(2)\n"
    "v_fma_mix_f32 v52, v122, v44, v36 op_sel:[0,0,0] op_sel_hi:[1,0,0]\n"
    "v_fma_mix_f32 v53, v122, v45, v37 op_sel:[1,0,0] op_sel_hi:[1,0,0]\n"
    "v_mfma_f32_32x32x16_bf16 v[160:175], a[196:199], v[112:115], v[160:175]\n"
    "ds_read_b64_tr_b16 a[212:213], v7 offset:12544\n"
    "v_cvt_pk_bf16_f32 v121, v50, v51\n"
    "v_fma_mix_f32 v54, v123, v46, v38 op_sel:[0,0,0] op_sel_hi:[1,0,0]\n"
    "v_fma_mix_f32 v55, v123, v47, v39 op_sel:[1,0,0] op_sel_hi:[1,0,0]\n"
    "v_mfma_f32_32x32x16_bf16 v[176:191], a[196:199], v[116:119], v[176:191]\n"
    "ds_read_b64_tr_b16 a[214:215], v7 offset:13568\n"
    "v_cvt_pk_bf16_f32 v122, v52, v53\n"
    "s_waitcnt vmcnt(0)\n"
    "v_fma_mix_f32 v56, v124, v40, v32 op_sel:[0,0,0] op_sel_hi:[1,0,0]\n"
    "v_fma_mix_f32 v57, v124, v41, v33 op_sel:[1,0,0] op_sel_hi:[1,0,0]\n"
    "v_cvt_pk_bf16_f32 v123, v54, v55\n"
    "v_mfma_f32_32x32x16_bf16 v[192:207], a[200:203], v[112:115], v[192:207]\n"
    "ds_read_b64_tr_b16 a[216:217], v7 offset:12800\n"
    "v_fma_mix_f32 v58, v125, v42, v34 op_sel:[0,0,0] op_sel_hi:[1,0,0]\n"
    "v_fma_mix_f32 v59, v125, v43, v35 op_sel:[1,0,0] op_sel_hi:[1,0,0]\n"
    "v_cvt_pk_bf16_f32 v124, v56, v57\n"
    "v_mfma_f32_32x32x16_bf16 v[208:223], a[200:203], v[116:119], v[208:223]\n"
    "ds_read_b64_tr_b16 a[218:219], v7 offset:13824\n"
    "v_fma_mix_f32 v60, v126, v44, v36 op_sel:[0,0,0] op_sel_hi:[1,0,0]\n"
    "v_fma_mix_f32 v61, v126, v45, v37 op_sel:[1,0,0] op_sel_hi:[1,0,0]\n"
    "v_cvt_pk_bf16_f32 v125, v58, v59\n"
    "v_fma_mix_f32 v62, v127, v46, v38 op_sel:[0,0,0] op_sel_hi:[1,0,0]\n"
    "v_mfma_f32_32x32x16_bf16 v[224:239], a[204:207], v[112:115], v[224:239]\n"
    "ds_read_b64_tr_b16 a[220:221], v7 offset:13056\n"
    "v_fma_mix_f32 v63, v127, v47, v39 op_sel:[1,0,0] op_sel_hi:[1,0,0]\n"
    "v_cvt_pk_bf16_f32 v126, v60, v61\n"
    "v_cvt_pk_bf16_f32 v127, v62, v63\n"
    "v_mfma_f32_32x32x16_bf16 v[240:255], a[204:207], v[116:119], v[240:255]\n"
    "ds_read_b64_tr_b16 a[222:223], v7 offset:14080\n"
    "s_waitcnt lgkmcnt(6)\n"
    "v_mfma_f32_32x32x16_bf16 v[128:143], a[208:211], v[120:123], v[128:143]\n"
    "v_mfma_f32_32x32x16_bf16 v[144:159], a[208:211], v[124:127], v[144:159]\n"
    "s_waitcnt lgkmcnt(4)\n"
    "v_mfma_f32_32x32x16_bf16 v[160:175], a[212:215], v[120:123], v[160:175]\n"
    "v_mfma_f32_32x32x16_bf16 v[176:191], a[212:215], v[124:127], v[176:191]\n"
    "s_waitcnt lgkmcnt(2)\n"
    "v_mfma_f32_32x32x16_bf16 v[192:207], a[216:219], v[120:123], v[192:207]\n"
    "v_mfma_f32_32x32x16_bf16 v[208:223], a[216:219], v[124:127], v[208:223]\n"
    "s_waitcnt lgkmcnt(0)\n"
    "v_mfma_f32_32x32x16_bf16 v[224:239], a[220:223], v[120:123], v[224:239]\n"
    "v_mfma_f32_32x32x16_bf16 v[240:255], a[220:223], v[124:127], v[240:255]\n"
    "s_nop 7\n"
    "v_and_b32 v20, 31, v1\n"
    "v_lshrrev_b32 v21, 5, v1\n"
    "v_and_b32 v22, 15, v1\n"
    "v_xor_b32 v21, v21, v22\n"
    "v_lshlrev_b32 v20, 8, v20\n"
    "v_xor_b32 v22, 0, v21\n"
    "v_lshl_add_u32 v2, v22, 4, v20\n"
    "v_xor_b32 v22, 2, v21\n"
    "v_lshl_add_u32 v3, v22, 4, v20\n"
    "v_xor_b32 v22, 4, v21\n"
    "v_lshl_add_u32 v4, v22, 4, v20\n"
    "v_xor_b32 v22, 6, v21\n"
    "v_lshl_add_u32 v5, v22, 4, v20\n"
    "v_xor_b32 v22, 8, v21\n"
    "v_lshl_add_u32 v6, v22, 4, v20\n"
    "v_xor_b32 v22, 10, v21\n"
    "v_lshl_add_u32 v7, v22, 4, v20\n"
    "v_xor_b32 v22, 12, v21\n"
    "v_lshl_add_u32 v8, v22, 4, v20\n"
    "v_xor_b32 v22, 14, v21\n"
    "v_lshl_add_u32 v9, v22, 4, v20\n"
    "v_lshrrev_b32 v20, 5, v1\n"
    "v_lshlrev_b32 v20, 10, v20\n"
    "v_bfe_u32 v21, v1, 2, 2\n"
    "v_lshl_add_u32 v20, v21, 6, v20\n"
    "v_bfe_u32 v21, v1, 4, 1\n"
    "v_lshl_add_u32 v20, v21, 5, v20\n"
    "v_and_b32 v21, 3, v1\n"
    "v_lshl_add_u32 v20, v21, 3, v20\n"
    "v_add_u32 v10, 0x10000, v20\n"
    "s_nop 3\n"
    "ds_read_b128 a[128:131], v2 offset:0\n"
    "ds_read_b128 a[132:135], v3 offset:0\n"
    "ds_read_b128 a[136:139], v4 offset:0\n"
    "ds_read_b128 a[140:143], v5 offset:0\n"
    "ds_read_b128 a[144:147], v6 offset:0\n"
    "ds_read_b128 a[148:151], v7 offset:0\n"
    "ds_read_b128 a[152:155], v8 offset:0\n"
    "ds_read_b128 a[156:159], v9 offset:0\n"
    "ds_read_b128 a[224:227], v2 offset:8192\n"
    "ds_read_b128 a[228:231], v3 offset:8192\n"
    "ds_read_b128 a[232:235], v4 offset:8192\n"
    "ds_read_b128 a[236:239], v5 offset:8192\n"
    "ds_read_b128 a[240:243], v6 offset:8192\n"
    "ds_read_b128 a[244:247], v7 offset:8192\n"
    "ds_read_b128 a[248:251], v8 offset:8192\n"
    "ds_read_b128 a[252:255], v9 offset:8192\n"
    "v_cvt_pk_bf16_f32 v64, v128, v129\n"
    "v_cvt_pk_bf16_f32 v65, v130, v131\n"
    "v_cvt_pk_bf16_f32 v66, v132, v133\n"
    "v_cvt_pk_bf16_f32 v67, v134, v135\n"
    "v_cvt_pk_bf16_f32 v96, v144, v145\n"
    "v_cvt_pk_bf16_f32 v97, v146, v147\n"
    "v_cvt_pk_bf16_f32 v98, v148, v149\n"
    "v_cvt_pk_bf16_f32 v99, v150, v151\n"
    "v_cvt_pk_bf16_f32 v68, v136, v137\n"
    "v_cvt_pk_bf16_f32 v69, v138, v139\n"
    "v_cvt_pk_bf16_f32 v70, v140, v141\n"
    "v_cvt_pk_bf16_f32 v71, v142, v143\n"
    "v_cvt_pk_bf16_f32 v100, v152, v153\n"
    "v_cvt_pk_bf16_f32 v101, v154, v155\n"
    "v_cvt_pk_bf16_f32 v102, v156, v157\n"
    "v_cvt_pk_bf16_f32 v103, v158, v159\n"
    "s_nop 1\n"
    "s_waitcnt lgkmcnt(15)\n"
    "v_mfma_f32_32x32x16_bf16 v[128:143], a[128:131], v[64:67], 0\n"
    "v_cvt_pk_bf16_f32 v72, v160, v161\n"
    "v_cvt_pk_bf16_f32 v73, v162, v163\n"
    "v_cvt_pk_bf16_f32 v74, v164, v165\n"
    "v_cvt_pk_bf16_f32 v75, v166, v167\n"
    "v_mfma_f32_32x32x16_bf16 v[144:159], a[128:131], v[96:99], 0\n"
    "v_cvt_pk_bf16_f32 v104, v176, v177\n"
    "v_cvt_pk_bf16_f32 v105, v178, v179\n"
    "v_cvt_pk_bf16_f32 v106, v180, v181\n"
    "v_cvt_pk_bf16_f32 v107, v182, v183\n"
    "s_waitcnt lgkmcnt(14)\n"
    "v_mfma_f32_32x32x16_bf16 v[128:143], a[132:135], v[68:71], v[128:143]\n"
    "v_cvt_pk_bf16_f32 v76, v168, v169\n"
    "v_cvt_pk_bf16_f32 v77, v170, v171\n"
    "v_cvt_pk_bf16_f32 v78, v172, v173\n"
    "v_cvt_pk_bf16_f32 v79, v174, v175\n"
    "v_mfma_f32_32x32x16_bf16 v[144:159], a[132:135], v[100:103], v[144:159]\n"
    "v_cvt_pk_bf16_f32 v108, v184, v185\n"
    "v_cvt_pk_bf16_f32 v109, v186, v187\n"
    "v_cvt_pk_bf16_f32 v110, v188, v189\n"
    "v_cvt_pk_bf16_f32 v111, v190, v191\n"
    "s_waitcnt lgkmcnt(13)\n"
    "v_mfma_f32_32x32x16_bf16 v[128:143], a[136:139], v[72:75], v[128:143]\n"
    "v_cvt_pk_bf16_f32 v80, v192, v193\n"
    "v_cvt_pk_bf16_f32 v81, v194, v195\n"
    "v_cvt_pk_bf16_f32 v82, v196, v197\n"
    "v_cvt_pk_bf16_f32 v83, v198, v199\n"
    "v_mfma_f32_32x32x16_bf16 v[144:159], a[136:139], v[104:107], v[144:159]\n"
    "v_cvt_pk_bf16_f32 v112, v208, v209\n"
    "v_cvt_pk_bf16_f32 v113, v210, v211\n"
    "v_cvt_pk_bf16_f32 v114, v212, v213\n"
    "v_cvt_pk_bf16_f32 v115, v214, v215\n"
    "s_waitcnt lgkmcnt(12)\n"
    "v_mfma_f32_32x32x16_bf16 v[128:143], a[140:143], v[76:79], v[128:143]\n"
    "v_cvt_pk_bf16_f32 v84, v200, v201\n"
    "v_cvt_pk_bf16_f32 v85, v202, v203\n"
    "v_cvt_pk_bf16_f32 v86, v204, v205\n"
    "v_cvt_pk_bf16_f32 v87, v206, v207\n"
    "v_mfma_f32_32x32x16_bf16 v[144:159], a[140:143], v[108:111], v[144:159]\n"
    "v_cvt_pk_bf16_f32 v116, v216, v217\n"
    "v_cvt_pk_bf16_f32 v117, v218, v219\n"
    "v_cvt_pk_bf16_f32 v118, v220, v221\n"
    "v_cvt_pk_bf16_f32 v119, v222, v223\n"
    "s_waitcnt lgkmcnt(11)\n"
    "v_mfma_f32_32x32x16_bf16 v[128:143], a[144:147], v[80:83], v[128:143]\n"
    "v_cvt_pk_bf16_f32 v88, v224, v225\n"
    "v_cvt_pk_bf16_f32 v89, v226, v227\n"
    "v_cvt_pk_bf16_f32 v90, v228, v229\n"
    "v_cvt_pk_bf16_f32 v91, v230, v231\n"
    "v_mfma_f32_32x32x16_bf16 v[144:159], a[144:147], v[112:115], v[144:159]\n"
    "v_cvt_pk_bf16_f32 v120, v240, v241\n"
    "v_cvt_pk_bf16_f32 v121, v242, v243\n"
    "v_cvt_pk_bf16_f32 v122, v244, v245\n"
    "v_cvt_pk_bf16_f32 v123, v246, v247\n"
    "s_waitcnt lgkmcnt(10)\n"
    "v_mfma_f32_32x32x16_bf16 v[128:143], a[148:151], v[84:87], v[128:143]\n"
    "v_cvt_pk_bf16_f32 v92, v232, v233\n"
    "v_cvt_pk_bf16_f32 v93, v234, v235\n"
    "v_cvt_pk_bf16_f32 v94, v236, v237\n"
    "v_cvt_pk_bf16_f32 v95, v238, v239\n"
    "v_mfma_f32_32x32x16_bf16 v[144:159], a[148:151], v[116:119], v[144:159]\n"
    "v_cvt_pk_bf16_f32 v124, v248, v249\n"
    "v_cvt_pk_bf16_f32 v125, v250, v251\n"
    "v_cvt_pk_bf16_f32 v126, v252, v253\n"
    "v_cvt_pk_bf16_f32 v127, v254, v255\n"
    "s_waitcnt lgkmcnt(9)\n"
    "v_mfma_f32_32x32x16_bf16 v[128:143], a[152:155], v[88:91], v[128:143]\n"
    "v_mfma_f32_32x32x16_bf16 v[144:159], a[152:155], v[120:123], v[144:159]\n"
    "s_waitcnt lgkmcnt(8)\n"
    "v_mfma_f32_32x32x16_bf16 v[128:143], a[156:159], v[92:95], v[128:143]\n"
    "v_mfma_f32_32x32x16_bf16 v[144:159], a[156:159], v[124:127], v[144:159]\n"
    "s_waitcnt lgkmcnt(7)\n"
    "v_mfma_f32_32x32x16_bf16 v[160:175], a[224:227], v[64:67], 0\n"
    "v_mfma_f32_32x32x16_bf16 v[176:191], a[224:227], v[96:99], 0\n"
    "s_waitcnt lgkmcnt(6)\n"
    "v_mfma_f32_32x32x16_bf16 v[160:175], a[228:231], v[68:71], v[160:175]\n"
    "v_mfma_f32_32x32x16_bf16 v[176:191], a[228:231], v[100:103], v[176:191]\n"
    "s_waitcnt lgkmcnt(5)\n"
    "v_mfma_f32_32x32x16_bf16 v[160:175], a[232:235], v[72:75], v[160:175]\n"
    "v_mfma_f32_32x32x16_bf16 v[176:191], a[232:235], v[104:107], v[176:191]\n"
    "s_waitcnt lgkmcnt(4)\n"
    "v_mfma_f32_32x32x16_bf16 v[160:175], a[236:239], v[76:79], v[160:175]\n"
    "v_mfma_f32_32x32x16_bf16 v[176:191], a[236:239], v[108:111], v[176:191]\n"
    "s_waitcnt lgkmcnt(3)\n"
    "v_mfma_f32_32x32x16_bf16 v[160:175], a[240:243], v[80:83], v[160:175]\n"
    "v_max3_f32 v20, v128, v129, v130\n"
    "v_max3_f32 v21, v131, v132, v133\n"
    "v_mfma_f32_32x32x16_bf16 v[176:191], a[240:243], v[112:115], v[176:191]\n"
    "v_max3_f32 v20, v20, v134, v135\n"
    "v_max3_f32 v21, v21, v136, v137\n"
    "s_waitcnt lgkmcnt(2)\n"
    "v_mfma_f32_32x32x16_bf16 v[160:175], a[244:247], v[84:87], v[160:175]\n"
    "v_max3_f32 v20, v20, v138, v139\n"
    "v_max3_f32 v21, v21, v140, v141\n"
    "v_mfma_f32_32x32x16_bf16 v[176:191], a[244:247], v[116:119], v[176:191]\n"
    "v_max3_f32 v20, v20, v142, v143\n"
    "v_max3_f32 v22, v144, v145, v146\n"
    "s_waitcnt lgkmcnt(1)\n"
    "v_mfma_f32_32x32x16_bf16 v[160:175], a[248:251], v[88:91], v[160:175]\n"
    "v_max3_f32 v23, v147, v148, v149\n"
    "v_max3_f32 v22, v22, v150, v151\n"
    "v_mfma_f32_32x32x16_bf16 v[176:191], a[248:251], v[120:123], v[176:191]\n"
    "v_max3_f32 v23, v23, v152, v153\n"
    "v_max3_f32 v22, v22, v154, v155\n"
    "s_waitcnt lgkmcnt(0)\n"
    "v_mfma_f32_32x32x16_bf16 v[160:175], a[252:255], v[92:95], v[160:175]\n"
    "v_max3_f32 v23, v23, v156, v157\n"
    "v_max3_f32 v22, v22, v158, v159\n"
    "v_mfma_f32_32x32x16_bf16 v[176:191], a[252:255], v[124:127], v[176:191]\n"
    "s_nop 15\n"
    "v_max3_f32 v20, v20, v160, v161\n"
    "v_max3_f32 v21, v21, v162, v163\n"
    "v_max3_f32 v20, v20, v164, v165\n"
    "v_max3_f32 v21, v21, v166, v167\n"
    "v_max3_f32 v20, v20, v168, v169\n"
    "v_max3_f32 v21, v21, v170, v171\n"
    "v_max3_f32 v20, v20, v172, v173\n"
    "v_max3_f32 v21, v21, v174, v175\n"
    "v_max_f32 v20, v20, v21\n"
    "v_mov_b32 v21, v20\n"
    "s_nop 1\n"
    "v_permlane32_swap_b32 v20, v21\n"
    "v_max_f32 v20, v20, v21\n"
    "v_add_f32 v20, 0x42cc0000, v20\n"
    "v_sub_f32 v20, 0, v20\n"
    "v_add_f32 v128, v128, v20\n"
    "v_add_f32 v160, v160, v20\n"
    "v_mov_b32 v224, v20\n"
    "v_add_f32 v129, v129, v20\n"
    "v_add_f32 v161, v161, v20\n"
    "v_mov_b32 v225, v20\n"
    "v_add_f32 v130, v130, v20\n"
    "v_add_f32 v162, v162, v20\n"
    "v_mov_b32 v226, v20\n"
    "v_add_f32 v131, v131, v20\n"
    "v_add_f32 v163, v163, v20\n"
    "v_mov_b32 v227, v20\n"
    "v_add_f32 v132, v132, v20\n"
    "v_add_f32 v164, v164, v20\n"
    "v_mov_b32 v228, v20\n"
    "v_add_f32 v133, v133, v20\n"
    "v_add_f32 v165, v165, v20\n"
    "v_mov_b32 v229, v20\n"
    "v_add_f32 v134, v134, v20\n"
    "v_add_f32 v166, v166, v20\n"
    "v_mov_b32 v230, v20\n"
    "v_add_f32 v135, v135, v20\n"
    "v_add_f32 v167, v167, v20\n"
    "v_mov_b32 v231, v20\n"
    "v_add_f32 v136, v136, v20\n"
    "v_add_f32 v168, v168, v20\n"
    "v_mov_b32 v232, v20\n"
    "v_add_f32 v137, v137, v20\n"
    "v_add_f32 v169, v169, v20\n"
    "v_mov_b32 v233, v20\n"
    "v_add_f32 v138, v138, v20\n"
    "v_add_f32 v170, v170, v20\n"
    "v_mov_b32 v234, v20\n"
    "v_add_f32 v139, v139, v20\n"
    "v_add_f32 v171, v171, v20\n"
    "v_mov_b32 v235, v20\n"
    "v_add_f32 v140, v140, v20\n"
    "v_add_f32 v172, v172, v20\n"
    "v_mov_b32 v236, v20\n"
    "v_add_f32 v141, v141, v20\n"
    "v_add_f32 v173, v173, v20\n"
    "v_mov_b32 v237, v20\n"
    "v_add_f32 v142, v142, v20\n"
    "v_add_f32 v174, v174, v20\n"
    "v_mov_b32 v238, v20\n"
    "v_add_f32 v143, v143, v20\n"
    "v_add_f32 v175, v175, v20\n"
    "v_mov_b32 v239, v20\n"
    "v_max3_f32 v22, v22, v176, v177\n"
    "v_max3_f32 v23, v23, v178, v179\n"
    "v_max3_f32 v22, v22, v180, v181\n"
    "v_max3_f32 v23, v23, v182, v183\n"
    "v_max3_f32 v22, v22, v184, v185\n"
    "v_max3_f32 v23, v23, v186, v187\n"
    "v_max3_f32 v22, v22, v188, v189\n"
    "v_max3_f32 v23, v23, v190, v191\n"
    "v_max_f32 v22, v22, v23\n"
    "v_mov_b32 v23, v22\n"
    "s_nop 1\n"
    "v_permlane32_swap_b32 v22, v23\n"
    "v_max_f32 v22, v22, v23\n"
    "v_add_f32 v22, 0x42cc0000, v22\n"
    "v_sub_f32 v22, 0, v22\n"
    "v_add_f32 v144, v144, v22\n"
    "v_add_f32 v176, v176, v22\n"
    "v_mov_b32 v240, v22\n"
    "v_add_f32 v145, v145, v22\n"
    "v_add_f32 v177, v177, v22\n"
    "v_mov_b32 v241, v22\n"
    "v_add_f32 v146, v146, v22\n"
    "v_add_f32 v178, v178, v22\n"
    "v_mov_b32 v242, v22\n"
    "v_add_f32 v147, v147, v22\n"
    "v_add_f32 v179, v179, v22\n"
    "v_mov_b32 v243, v22\n"
    "v_add_f32 v148, v148, v22\n"
    "v_add_f32 v180, v180, v22\n"
    "v_mov_b32 v244, v22\n"
    "v_add_f32 v149, v149, v22\n"
    "v_add_f32 v181, v181, v22\n"
    "v_mov_b32 v245, v22\n"
    "v_add_f32 v150, v150, v22\n"
    "v_add_f32 v182, v182, v22\n"
    "v_mov_b32 v246, v22\n"
    "v_add_f32 v151, v151, v22\n"
    "v_add_f32 v183, v183, v22\n"
    "v_mov_b32 v247, v22\n"
    "v_add_f32 v152, v152, v22\n"
    "v_add_f32 v184, v184, v22\n"
    "v_mov_b32 v248, v22\n"
    "v_add_f32 v153, v153, v22\n"
    "v_add_f32 v185, v185, v22\n"
    "v_mov_b32 v249, v22\n"
    "v_add_f32 v154, v154, v22\n"
    "v_add_f32 v186, v186, v22\n"
    "v_mov_b32 v250, v22\n"
    "v_add_f32 v155, v155, v22\n"
    "v_add_f32 v187, v187, v22\n"
    "v_mov_b32 v251, v22\n"
    "v_add_f32 v156, v156, v22\n"
    "v_add_f32 v188, v188, v22\n"
    "v_mov_b32 v252, v22\n"
    "v_add_f32 v157, v157, v22\n"
    "v_add_f32 v189, v189, v22\n"
    "v_mov_b32 v253, v22\n"
    "v_add_f32 v158, v158, v22\n"
    "v_add_f32 v190, v190, v22\n"
    "v_mov_b32 v254, v22\n"
    "v_add_f32 v159, v159, v22\n"
    "v_add_f32 v191, v191, v22\n"
    "v_mov_b32 v255, v22\n"
    "v_mov_b32 v16, 0\n"
    "v_mov_b32 v17, 0\n"
    "s_add_u32 s67, s69, 0xc000\n"
    "s_add_u32 s68, s69, 0x8000\n"
    "s_barrier\n"
    "ds_read_b128 a[128:131], v2 offset:16384\n"
    "ds_read_b128 a[132:135], v3 offset:16384\n"
    "ds_read_b128 a[136:139], v4 offset:16384\n"
    "ds_read_b128 a[140:143], v5 offset:16384\n"
    "ds_read_b128 a[144:147], v6 offset:16384\n"
    "ds_read_b128 a[148:151], v7 offset:16384\n"
    "ds_read_b128 a[152:155], v8 offset:16384\n"
    "ds_read_b128 a[156:159], v9 offset:16384\n"
    "ds_read_b64_tr_b16 a[160:161], v10 offset:0\n"
    "ds_read_b64_tr_b16 a[162:163], v10 offset:2048\n"
    "ds_read_b64_tr_b16 a[164:165], v10 offset:256\n"
    "ds_read_b64_tr_b16 a[166:167], v10 offset:2304\n"
    "ds_read_b64_tr_b16 a[168:169], v10 offset:512\n"
    "ds_read_b64_tr_b16 a[170:171], v10 offset:2560\n"
    "ds_read_b64_tr_b16 a[172:173], v10 offset:768\n"
    "ds_read_b64_tr_b16 a[174:175], v10 offset:2816\n"
    "ds_read_b64_tr_b16 a[176:177], v10 offset:4096\n"
    "ds_read_b64_tr_b16 a[178:179], v10 offset:6144\n"
    "ds_read_b64_tr_b16 a[180:181], v10 offset:4352\n"
    "ds_read_b64_tr_b16 a[182:183], v10 offset:6400\n"
    "ds_read_b64_tr_b16 a[184:185], v10 offset:4608\n"
    "ds_read_b64_tr_b16 a[186:187], v10 offset:6656\n"
    "ds_read_b64_tr_b16 a[188:189], v10 offset:4864\n"
    "ds_read_b64_tr_b16 a[190:191], v10 offset:6912\n"
    "s_add_u32 m0, s69, 0xc000\n"
    "s_nop 0\n"
    "buffer_load_dwordx4 v11, s[40:43], s67 offen offset:0 lds\n"
    "buffer_load_dwordx4 v12, s[40:43], s67 offen offset:1024 lds\n"
    "buffer_load_dwordx4 v13, s[40:43], s67 offen offset:2048 lds\n"
    "buffer_load_dwordx4 v14, s[40:43], s67 offen offset:3072 lds\n"
    "s_add_u32 s67, s67, 0x4000\n"
    "v_exp_f32 v32, v128\n"
    "v_exp_f32 v33, v129\n"
    "v_exp_f32 v34, v130\n"
    "v_add_f32 v18, v32, v33\n"
    "v_cvt_pk_bf16_f32 v192, v32, v33\n"
    "v_exp_f32 v35, v131\n"
    "v_add_f32 v18, v18, v34\n"
    "v_exp_f32 v36, v132\n"
    "v_add_f32 v18, v18, v35\n"
    "v_cvt_pk_bf16_f32 v193, v34, v35\n"
    "v_exp_f32 v37, v133\n"
    "v_add_f32 v18, v18, v36\n"
    "v_exp_f32 v38, v134\n"
    "v_add_f32 v18, v18, v37\n"
    "v_cvt_pk_bf16_f32 v194, v36, v37\n"
    "v_exp_f32 v39, v135\n"
    "v_add_f32 v18, v18, v38\n"
    "v_exp_f32 v32, v136\n"
    "v_add_f32 v18, v18, v39\n"
    "v_cvt_pk_bf16_f32 v195, v38, v39\n"
    "v_exp_f32 v33, v137\n"
    "v_add_f32 v18, v18, v32\n"
    "v_exp_f32 v34, v138\n"
    "v_add_f32 v18, v18, v33\n"
    "v_cvt_pk_bf16_f32 v196, v32, v33\n"
    "v_exp_f32 v35, v139\n"
    "v_add_f32 v18, v18, v34\n"
    "v_exp_f32 v36, v140\n"
    "v_add_f32 v18, v18, v35\n"
    "v_cvt_pk_bf16_f32 v197, v34, v35\n"
    "v_exp_f32 v37, v141\n"
    "v_add_f32 v18, v18, v36\n"
    "v_exp_f32 v38, v142\n"
    "v_add_f32 v18, v18, v37\n"
    "v_cvt_pk_bf16_f32 v198, v36, v37\n"
    "v_exp_f32 v39, v143\n"
    "v_add_f32 v18, v18, v38\n"
    "v_exp_f32 v32, v144\n"
    "v_add_f32 v18, v18, v39\n"
    "v_cvt_pk_bf16_f32 v199, v38, v39\n"
    "v_exp_f32 v33, v145\n"
    "v_exp_f32 v34, v146\n"
    "v_add_f32 v19, v32, v33\n"
    "v_cvt_pk_bf16_f32 v200, v32, v33\n"
    "v_exp_f32 v35, v147\n"
    "v_add_f32 v19, v19, v34\n"
    "v_exp_f32 v36, v148\n"
    "v_add_f32 v19, v19, v35\n"
    "v_cvt_pk_bf16_f32 v201, v34, v35\n"
    "v_exp_f32 v37, v149\n"
    "v_add_f32 v19, v19, v36\n"
    "v_exp_f32 v38, v150\n"
    "v_add_f32 v19, v19, v37\n"
    "v_cvt_pk_bf16_f32 v202, v36, v37\n"
    "v_exp_f32 v39, v151\n"
    "v_add_f32 v19, v19, v38\n"
    "v_exp_f32 v32, v152\n"
    "v_add_f32 v19, v19, v39\n"
    "v_cvt_pk_bf16_f32 v203, v38, v39\n"
    "v_exp_f32 v33, v153\n"
    "v_add_f32 v19, v19, v32\n"
    "v_exp_f32 v34, v154\n"
    "v_add_f32 v19, v19, v33\n"
    "v_cvt_pk_bf16_f32 v204, v32, v33\n"
    "v_exp_f32 v35, v155\n"
    "v_add_f32 v19, v19, v34\n"
    "v_exp_f32 v36, v156\n"
    "v_add_f32 v19, v19, v35\n"
    "v_cvt_pk_bf16_f32 v205, v34, v35\n"
    "v_exp_f32 v37, v157\n"
    "v_add_f32 v19, v19, v36\n"
    "v_exp_f32 v38, v158\n"
    "v_add_f32 v19, v19, v37\n"
    "v_cvt_pk_bf16_f32 v206, v36, v37\n"
    "v_exp_f32 v39, v159\n"
    "v_add_f32 v19, v19, v38\n"
    "v_add_f32 v19, v19, v39\n"
    "v_cvt_pk_bf16_f32 v207, v38, v39\n"
    "v_max_f32 v20, v18, v19\n"
    "v_cmp_lt_f32 vcc, s72, v20\n"
    "s_cbranch_vccnz .Lfb_stub0\n"
    ".Lfb_ret0:\n"
    "s_waitcnt lgkmcnt(15)\n"
    "v_mfma_f32_32x32x16_bf16 v[128:143], a[128:131], v[64:67], v[224:239]\n"
    "v_add_f32 v16, v16, v18\n"
    "v_add_f32 v17, v17, v19\n"
    "v_exp_f32 v32, v160\n"
    "v_mfma_f32_32x32x16_bf16 v[144:159], a[128:131], v[96:99], v[240:255]\n"
    "ds_read_b128 a[128:131], v2 offset:24576\n"
    "v_exp_f32 v33, v161\n"
    "s_add_u32 m0, s69, 0x18000\n"
    "v_mfma_f32_32x32x16_bf16 v[128:143], a[132:135], v[68:71], v[128:143]\n"
    "buffer_load_dwordx4 v15, s[44:47], s68 offen offset:0 lds\n"
    "v_exp_f32 v34, v162\n"
    "v_add_f32 v18, v32, v33\n"
    "v_cvt_pk_bf16_f32 v208, v32, v33\n"
    "v_mfma_f32_32x32x16_bf16 v[144:159], a[132:135], v[100:103], v[144:159]\n"
    "ds_read_b128 a[132:135], v3 offset:24576\n"
    "v_exp_f32 v35, v163\n"
    "v_add_f32 v18, v18, v34\n"
    "v_exp_f32 v36, v164\n"
    "v_mfma_f32_32x32x16_bf16 v[128:143], a[136:139], v[72:75], v[128:143]\n"
    "v_add_f32 v18, v18, v35\n"
    "v_cvt_pk_bf16_f32 v209, v34, v35\n"
    "v_exp_f32 v37, v165\n"
    "v_mfma_f32_32x32x16_bf16 v[144:159], a[136:139], v[104:107], v[144:159]\n"
    "ds_read_b128 a[136:139], v4 offset:24576\n"
    "v_add_f32 v18, v18, v36\n"
    "v_exp_f32 v38, v166\n"
    "v_add_f32 v18, v18, v37\n"
    "v_mfma_f32_32x32x16_bf16 v[128:143], a[140:143], v[76:79], v[128:143]\n"
    "buffer_load_dwordx4 v15, s[44:47], s68 offen offset:1024 lds\n"
    "v_cvt_pk_bf16_f32 v210, v36, v37\n"
    "v_exp_f32 v39, v167\n"
    "v_add_f32 v18, v18, v38\n"
    "v_mfma_f32_32x32x16_bf16 v[144:159], a[140:143], v[108:111], v[144:159]\n"
    "ds_read_b128 a[140:143], v5 offset:24576\n"
    "v_exp_f32 v32, v168\n"
    "v_add_f32 v18, v18, v39\n"
    "v_cvt_pk_bf16_f32 v211, v38, v39\n"
    "v_mfma_f32_32x32x16_bf16 v[128:143], a[144:147], v[80:83], v[128:143]\n"
    "v_exp_f32 v33, v169\n"
    "v_add_f32 v18, v18, v32\n"
    "v_mfma_f32_32x32x16_bf16 v[144:159], a[144:147], v[112:115], v[144:159]\n"
    "ds_read_b128 a[144:147], v6 offset:24576\n"
    "v_exp_f32 v34, v170\n"
    "v_add_f32 v18, v18, v33\n"
    "v_cvt_pk_bf16_f32 v212, v32, v33\n"
    "v_mfma_f32_32x32x16_bf16 v[128:143], a[148:151], v[84:87], v[128:143]\n"
    "buffer_load_dwordx4 v15, s[44:47], s68 offen offset:2048 lds\n"
    "v_exp_f32 v35, v171\n"
    "v_add_f32 v18, v18, v34\n"
    "v_exp_f32 v36, v172\n"
    "v_mfma_f32_32x32x16_bf16 v[144:159], a[148:151], v[116:119], v[144:159]\n"
    "ds_read_b128 a[148:151], v7 offset:24576\n"
    "v_add_f32 v18, v18, v35\n"
    "v_cvt_pk_bf16_f32 v213, v34, v35\n"
    "v_exp_f32 v37, v173\n"
    "v_mfma_f32_32x32x16_bf16 v[128:143], a[152:155], v[88:91], v[128:143]\n"
    "v_add_f32 v18, v18, v36\n"
    "v_exp_f32 v38, v174\n"
    "v_add_f32 v18, v18, v37\n"
    "v_mfma_f32_32x32x16_bf16 v[144:159], a[152:155], v[120:123], v[144:159]\n"
    "ds_read_b128 a[152:155], v8 offset:24576\n"
    "v_cvt_pk_bf16_f32 v214, v36, v37\n"
    "v_exp_f32 v39, v175\n"
    "v_add_f32 v18, v18, v38\n"
    "v_mfma_f32_32x32x16_bf16 v[128:143], a[156:159], v[92:95], v[128:143]\n"
    "buffer_load_dwordx4 v15, s[44:47], s68 offen offset:3072 lds\n"
    "v_exp_f32 v32, v176\n"
    "v_add_f32 v18, v18, v39\n"
    "v_cvt_pk_bf16_f32 v215, v38, v39\n"
    "v_mfma_f32_32x32x16_bf16 v[144:159], a[156:159], v[124:127], v[144:159]\n"
    "ds_read_b128 a[156:159], v9 offset:24576\n"
    "v_exp_f32 v33, v177\n"
    "s_add_u32 s68, s68, 0x4000\n"
    "s_waitcnt lgkmcnt(15)\n"
    "v_mfma_f32_32x32x16_bf16 a[0:15], a[160:163], v[192:195], 0\n"
    "v_exp_f32 v34, v178\n"
    "v_add_f32 v19, v32, v33\n"
    "v_cvt_pk_bf16_f32 v216, v32, v33\n"
    "v_mfma_f32_32x32x16_bf16 a[16:31], a[160:163], v[200:203], 0\n"
    "ds_read_b64_tr_b16 a[160:161], v10 offset:8192\n"
    "ds_read_b64_tr_b16 a[162:163], v10 offset:10240\n"
    "v_exp_f32 v35, v179\n"
    "v_add_f32 v19, v19, v34\n"
    "v_mfma_f32_32x32x16_bf16 a[32:47], a[164:167], v[192:195], 0\n"
    "v_exp_f32 v36, v180\n"
    "v_add_f32 v19, v19, v35\n"
    "v_cvt_pk_bf16_f32 v217, v34, v35\n"
    "v_exp_f32 v37, v181\n"
    "v_mfma_f32_32x32x16_bf16 a[48:63], a[164:167], v[200:203], 0\n"
    "ds_read_b64_tr_b16 a[164:165], v10 offset:8448\n"
    "ds_read_b64_tr_b16 a[166:167], v10 offset:10496\n"
    "v_add_f32 v19, v19, v36\n"
    "v_exp_f32 v38, v182\n"
    "v_mfma_f32_32x32x16_bf16 a[64:79], a[168:171], v[192:195], 0\n"
    "v_add_f32 v19, v19, v37\n"
    "v_cvt_pk_bf16_f32 v218, v36, v37\n"
    "v_exp_f32 v39, v183\n"
    "v_mfma_f32_32x32x16_bf16 a[80:95], a[168:171], v[200:203], 0\n"
    "ds_read_b64_tr_b16 a[168:169], v10 offset:8704\n"
    "ds_read_b64_tr_b16 a[170:171], v10 offset:10752\n"
    "v_add_f32 v19, v19, v38\n"
    "v_exp_f32 v32, v184\n"
    "v_mfma_f32_32x32x16_bf16 a[96:111], a[172:175], v[192:195], 0\n"
    "v_add_f32 v19, v19, v39\n"
    "v_cvt_pk_bf16_f32 v219, v38, v39\n"
    "v_exp_f32 v33, v185\n"
    "v_add_f32 v19, v19, v32\n"
    "v_mfma_f32_32x32x16_bf16 a[112:127], a[172:175], v[200:203], 0\n"
    "ds_read_b64_tr_b16 a[172:173], v10 offset:8960\n"
    "ds_read_b64_tr_b16 a[174:175], v10 offset:11008\n"
    "v_exp_f32 v34, v186\n"
    "v_add_f32 v19, v19, v33\n"
    "s_waitcnt lgkmcnt(15)\n"
    "v_mfma_f32_32x32x16_bf16 a[0:15], a[176:179], v[196:199], a[0:15]\n"
    "v_cvt_pk_bf16_f32 v220, v32, v33\n"
    "v_exp_f32 v35, v187\n"
    "v_add_f32 v19, v19, v34\n"
    "v_mfma_f32_32x32x16_bf16 a[16:31], a[176:179], v[204:207], a[16:31]\n"
    "ds_read_b64_tr_b16 a[176:177], v10 offset:12288\n"
    "ds_read_b64_tr_b16 a[178:179], v10 offset:14336\n"
    "v_exp_f32 v36, v188\n"
    "v_add_f32 v19, v19, v35\n"
    "v_mfma_f32_32x32x16_bf16 a[32:47], a[180:183], v[196:199], a[32:47]\n"
    "v_cvt_pk_bf16_f32 v221, v34, v35\n"
    "v_exp_f32 v37, v189\n"
    "v_add_f32 v19, v19, v36\n"
    "v_mfma_f32_32x32x16_bf16 a[48:63], a[180:183], v[204:207], a[48:63]\n"
    "ds_read_b64_tr_b16 a[180:181], v10 offset:12544\n"
    "ds_read_b64_tr_b16 a[182:183], v10 offset:14592\n"
    "v_exp_f32 v38, v190\n"
    "v_add_f32 v19, v19, v37\n"
    "v_cvt_pk_bf16_f32 v222, v36, v37\n"
    "v_mfma_f32_32x32x16_bf16 a[64:79], a[184:187], v[196:199], a[64:79]\n"
    "v_exp_f32 v39, v191\n"
    "v_add_f32 v19, v19, v38\n"
    "v_add_f32 v19, v19, v39\n"
    "v_mfma_f32_32x32x16_bf16 a[80:95], a[184:187], v[204:207], a[80:95]\n"
    "ds_read_b64_tr_b16 a[184:185], v10 offset:12800\n"
    "ds_read_b64_tr_b16 a[186:187], v10 offset:14848\n"
    "v_cvt_pk_bf16_f32 v223, v38, v39\n"
    "v_max_f32 v20, v18, v19\n"
    "v_cmp_lt_f32 vcc, s72, v20\n"
    "v_mfma_f32_32x32x16_bf16 a[96:111], a[188:191], v[196:199], a[96:111]\n"
    "v_mfma_f32_32x32x16_bf16 a[112:127], a[188:191], v[204:207], a[112:127]\n"
    "ds_read_b64_tr_b16 a[188:189], v10 offset:13056\n"
    "ds_read_b64_tr_b16 a[190:191], v10 offset:15104\n"
    "s_cbranch_vccnz .Lfb_stub1\n"
    ".Lfb_ret1:\n"
    "s_mov_b32 s71, 7\n"
    ".Lattn_loop:\n"
    "v_mfma_f32_32x32x16_bf16 v[160:175], a[128:131], v[64:67], v[224:239]\n"
    "s_waitcnt vmcnt(8)\n"
    "s_barrier\n"
    "v_add_f32 v16, v16, v18\n"
    "v_add_f32 v17, v17, v19\n"
    "v_mfma_f32_32x32x16_bf16 v[176:191], a[128:131], v[96:99], v[240:255]\n"
    "ds_read_b128 a[128:131], v2 offset:32768\n"
    "v_exp_f32 v32, v128\n"
    "v_exp_f32 v33, v129\n"
    "s_add_u32 m0, s69, 0\n"
    "s_waitcnt lgkmcnt(15)\n"
    "v_mfma_f32_32x32x16_bf16 v[160:175], a[132:135], v[68:71], v[160:175]\n"
    "buffer_load_dwordx4 v11, s[40:43], s67 offen offset:0 lds\n"
    "v_exp_f32 v34, v130\n"
    "v_add_f32 v18, v32, v33\n"
    "v_cvt_pk_bf16_f32 v192, v32, v33\n"
    "v_mfma_f32_32x32x16_bf16 v[176:191], a[132:135], v[100:103], v[176:191]\n"
    "ds_read_b128 a[132:135], v3 offset:32768\n"
    "v_exp_f32 v35, v131\n"
    "v_add_f32 v18, v18, v34\n"
    "v_mfma_f32_32x32x16_bf16 v[160:175], a[136:139], v[72:75], v[160:175]\n"
    "v_exp_f32 v36, v132\n"
    "v_add_f32 v18, v18, v35\n"
    "v_cvt_pk_bf16_f32 v193, v34, v35\n"
    "v_mfma_f32_32x32x16_bf16 v[176:191], a[136:139], v[104:107], v[176:191]\n"
    "ds_read_b128 a[136:139], v4 offset:32768\n"
    "v_exp_f32 v37, v133\n"
    "v_add_f32 v18, v18, v36\n"
    "v_exp_f32 v38, v134\n"
    "v_mfma_f32_32x32x16_bf16 v[160:175], a[140:143], v[76:79], v[160:175]\n"
    "buffer_load_dwordx4 v12, s[40:43], s67 offen offset:1024 lds\n"
    "v_add_f32 v18, v18, v37\n"
    "v_cvt_pk_bf16_f32 v194, v36, v37\n"
    "v_exp_f32 v39, v135\n"
    "v_mfma_f32_32x32x16_bf16 v[176:191], a[140:143], v[108:111], v[176:191]\n"
    "ds_read_b128 a[140:143], v5 offset:32768\n"
    "v_add_f32 v18, v18, v38\n"
    "v_exp_f32 v32, v136\n"
    "v_add_f32 v18, v18, v39\n"
    "v_mfma_f32_32x32x16_bf16 v[160:175], a[144:147], v[80:83], v[160:175]\n"
    "v_cvt_pk_bf16_f32 v195, v38, v39\n"
    "v_exp_f32 v33, v137\n"
    "v_add_f32 v18, v18, v32\n"
    "v_mfma_f32_32x32x16_bf16 v[176:191], a[144:147], v[112:115], v[176:191]\n"
    "ds_read_b128 a[144:147], v6 offset:32768\n"
    "v_exp_f32 v34, v138\n"
    "v_add_f32 v18, v18, v33\n"
    "v_cvt_pk_bf16_f32 v196, v32, v33\n"
    "v_mfma_f32_32x32x16_bf16 v[160:175], a[148:151], v[84:87], v[160:175]\n"
    "buffer_load_dwordx4 v13, s[40:43], s67 offen offset:2048 lds\n"
    "v_exp_f32 v35, v139\n"
    "v_add_f32 v18, v18, v34\n"
    "v_mfma_f32_32x32x16_bf16 v[176:191], a[148:151], v[116:119], v[176:191]\n"
    "ds_read_b128 a[148:151], v7 offset:32768\n"
    "v_exp_f32 v36, v140\n"
    "v_add_f32 v18, v18, v35\n"
    "v_cvt_pk_bf16_f32 v197, v34, v35\n"
    "v_mfma_f32_32x32x16_bf16 v[160:175], a[152:155], v[88:91], v[160:175]\n"
    "v_exp_f32 v37, v141\n"
    "v_add_f32 v18, v18, v36\n"
    "v_exp_f32 v38, v142\n"
    "v_mfma_f32_32x32x16_bf16 v[176:191], a[152:155], v[120:123], v[176:191]\n"
    "ds_read_b128 a[152:155], v8 offset:32768\n"
    "v_add_f32 v18, v18, v37\n"
    "v_cvt_pk_bf16_f32 v198, v36, v37\n"
    "v_exp_f32 v39, v143\n"
    "v_mfma_f32_32x32x16_bf16 v[160:175], a[156:159], v[92:95], v[160:175]\n"
    "buffer_load_dwordx4 v14, s[40:43], s67 offen offset:3072 lds\n"
    "v_add_f32 v18, v18, v38\n"
    "v_exp_f32 v32, v144\n"
    "v_add_f32 v18, v18, v39\n"
    "v_mfma_f32_32x32x16_bf16 v[176:191], a[156:159], v[124:127], v[176:191]\n"
    "ds_read_b128 a[156:159], v9 offset:32768\n"
    "v_cvt_pk_bf16_f32 v199, v38, v39\n"
    "v_exp_f32 v33, v145\n"
    "s_add_u32 s67, s67, 0x4000\n"
    "v_mfma_f32_32x32x16_bf16 a[0:15], a[160:163], v[208:211], a[0:15]\n"
    "v_exp_f32 v34, v146\n"
    "v_add_f32 v19, v32, v33\n"
    "v_cvt_pk_bf16_f32 v200, v32, v33\n"
    "v_mfma_f32_32x32x16_bf16 a[16:31], a[160:163], v[216:219], a[16:31]\n"
    "ds_read_b64_tr_b16 a[160:161], v10 offset:16384\n"
    "ds_read_b64_tr_b16 a[162:163], v10 offset:18432\n"
    "v_exp_f32 v35, v147\n"
    "v_add_f32 v19, v19, v34\n"
    "s_waitcnt lgkmcnt(15)\n"
    "v_mfma_f32_32x32x16_bf16 a[32:47], a[164:167], v[208:211], a[32:47]\n"
    "v_exp_f32 v36, v148\n"
    "v_add_f32 v19, v19, v35\n"
    "v_cvt_pk_bf16_f32 v201, v34, v35\n"
    "v_mfma_f32_32x32x16_bf16 a[48:63], a[164:167], v[216:219], a[48:63]\n"
    "ds_read_b64_tr_b16 a[164:165], v10 offset:16640\n"
    "ds_read_b64_tr_b16 a[166:167], v10 offset:18688\n"
    "v_exp_f32 v37, v149\n"
    "v_add_f32 v19, v19, v36\n"
    "v_mfma_f32_32x32x16_bf16 a[64:79], a[168:171], v[208:211], a[64:79]\n"
    "v_exp_f32 v38, v150\n"
    "v_add_f32 v19, v19, v37\n"
    "v_cvt_pk_bf16_f32 v202, v36, v37\n"
    "v_mfma_f32_32x32x16_bf16 a[80:95], a[168:171], v[216:219], a[80:95]\n"
    "ds_read_b64_tr_b16 a[168:169], v10 offset:16896\n"
    "ds_read_b64_tr_b16 a[170:171], v10 offset:18944\n"
    "v_exp_f32 v39, v151\n"
    "v_add_f32 v19, v19, v38\n"
    "v_mfma_f32_32x32x16_bf16 a[96:111], a[172:175], v[208:211], a[96:111]\n"
    "v_exp_f32 v32, v152\n"
    "v_add_f32 v19, v19, v39\n"
    "v_cvt_pk_bf16_f32 v203, v38, v39\n"
    "v_exp_f32 v33, v153\n"
    "v_mfma_f32_32x32x16_bf16 a[112:127], a[172:175], v[216:219], a[112:127]\n"
    "ds_read_b64_tr_b16 a[172:173], v10 offset:17152\n"
    "ds_read_b64_tr_b16 a[174:175], v10 offset:19200\n"
    "v_add_f32 v19, v19, v32\n"
    "v_exp_f32 v34, v154\n"
    "v_mfma_f32_32x32x16_bf16 a[0:15], a[176:179], v[212:215], a[0:15]\n"
    "v_add_f32 v19, v19, v33\n"
    "v_cvt_pk_bf16_f32 v204, v32, v33\n"
    "v_exp_f32 v35, v155\n"
    "v_mfma_f32_32x32x16_bf16 a[16:31], a[176:179], v[220:223], a[16:31]\n"
    "ds_read_b64_tr_b16 a[176:177], v10 offset:20480\n"
    "ds_read_b64_tr_b16 a[178:179], v10 offset:22528\n"
    "v_add_f32 v19, v19, v34\n"
    "v_exp_f32 v36, v156\n"
    "s_waitcnt lgkmcnt(15)\n"
    "v_mfma_f32_32x32x16_bf16 a[32:47], a[180:183], v[212:215], a[32:47]\n"
    "v_add_f32 v19, v19, v35\n"
    "v_cvt_pk_bf16_f32 v205, v34, v35\n"
    "v_exp_f32 v37, v157\n"
    "v_add_f32 v19, v19, v36\n"
    "v_mfma_f32_32x32x16_bf16 a[48:63], a[180:183], v[220:223], a[48:63]\n"
    "ds_read_b64_tr_b16 a[180:181], v10 offset:20736\n"
    "ds_read_b64_tr_b16 a[182:183], v10 offset:22784\n"
    "v_exp_f32 v38, v158\n"
    "v_add_f32 v19, v19, v37\n"
    "v_mfma_f32_32x32x16_bf16 a[64:79], a[184:187], v[212:215], a[64:79]\n"
    "v_cvt_pk_bf16_f32 v206, v36, v37\n"
    "v_exp_f32 v39, v159\n"
    "v_add_f32 v19, v19, v38\n"
    "v_mfma_f32_32x32x16_bf16 a[80:95], a[184:187], v[220:223], a[80:95]\n"
    "ds_read_b64_tr_b16 a[184:185], v10 offset:20992\n"
    "ds_read_b64_tr_b16 a[186:187], v10 offset:23040\n"
    "v_add_f32 v19, v19, v39\n"
    "v_cvt_pk_bf16_f32 v207, v38, v39\n"
    "v_max_f32 v20, v18, v19\n"
    "v_mfma_f32_32x32x16_bf16 a[96:111], a[188:191], v[212:215], a[96:111]\n"
    "v_cmp_lt_f32 vcc, s72, v20\n"
    "v_mfma_f32_32x32x16_bf16 a[112:127], a[188:191], v[220:223], a[112:127]\n"
    "ds_read_b64_tr_b16 a[188:189], v10 offset:21248\n"
    "ds_read_b64_tr_b16 a[190:191], v10 offset:23296\n"
    "s_cbranch_vccnz .Lfb_stub2\n"
    ".Lfb_ret2:\n"
    "v_mfma_f32_32x32x16_bf16 v[128:143], a[128:131], v[64:67], v[224:239]\n"
    "v_add_f32 v16, v16, v18\n"
    "v_add_f32 v17, v17, v19\n"
    "v_exp_f32 v32, v160\n"
    "v_mfma_f32_32x32x16_bf16 v[144:159], a[128:131], v[96:99], v[240:255]\n"
    "ds_read_b128 a[128:131], v2 offset:40960\n"
    "v_exp_f32 v33, v161\n"
    "s_add_u32 m0, s69, 0x1c000\n"
    "v_mfma_f32_32x32x16_bf16 v[128:143], a[132:135], v[68:71], v[128:143]\n"
    "buffer_load_dwordx4 v15, s[44:47], s68 offen offset:0 lds\n"
    "v_exp_f32 v34, v162\n"
    "v_add_f32 v18, v32, v33\n"
    "v_cvt_pk_bf16_f32 v208, v32, v33\n"
    "v_mfma_f32_32x32x16_bf16 v[144:159], a[132:135], v[100:103], v[144:159]\n"
    "ds_read_b128 a[132:135], v3 offset:40960\n"
    "v_exp_f32 v35, v163\n"
    "v_add_f32 v18, v18, v34\n"
    "v_exp_f32 v36, v164\n"
    "v_mfma_f32_32x32x16_bf16 v[128:143], a[136:139], v[72:75], v[128:143]\n"
    "v_add_f32 v18, v18, v35\n"
    "v_cvt_pk_bf16_f32 v209, v34, v35\n"
    "v_exp_f32 v37, v165\n"
    "v_mfma_f32_32x32x16_bf16 v[144:159], a[136:139], v[104:107], v[144:159]\n"
    "ds_read_b128 a[136:139], v4 offset:40960\n"
    "v_add_f32 v18, v18, v36\n"
    "v_exp_f32 v38, v166\n"
    "v_add_f32 v18, v18, v37\n"
    "s_waitcnt lgkmcnt(15)\n"
    "v_mfma_f32_32x32x16_bf16 v[128:143], a[140:143], v[76:79], v[128:143]\n"
    "buffer_load_dwordx4 v15, s[44:47], s68 offen offset:1024 lds\n"
    "v_cvt_pk_bf16_f32 v210, v36, v37\n"
    "v_exp_f32 v39, v167\n"
    "v_add_f32 v18, v18, v38\n"
    "v_mfma_f32_32x32x16_bf16 v[144:159], a[140:143], v[108:111], v[144:159]\n"
    "ds_read_b128 a[140:143], v5 offset:40960\n"
    "v_exp_f32 v32, v168\n"
    "v_add_f32 v18, v18, v39\n"
    "v_cvt_pk_bf16_f32 v211, v38, v39\n"
    "v_mfma_f32_32x32x16_bf16 v[128:143], a[144:147], v[80:83], v[128:143]\n"
    "v_exp_f32 v33, v169\n"
    "v_add_f32 v18, v18, v32\n"
    "v_mfma_f32_32x32x16_bf16 v[144:159], a[144:147], v[112:115], v[144:159]\n"
    "ds_read_b128 a[144:147], v6 offset:40960\n"
    "v_exp_f32 v34, v170\n"
    "v_add_f32 v18, v18, v33\n"
    "v_cvt_pk_bf16_f32 v212, v32, v33\n"
    "v_mfma_f32_32x32x16_bf16 v[128:143], a[148:151], v[84:87], v[128:143]\n"
    "buffer_load_dwordx4 v15, s[44:47], s68 offen offset:2048 lds\n"
    "v_exp_f32 v35, v171\n"
    "v_add_f32 v18, v18, v34\n"
    "v_exp_f32 v36, v172\n"
    "v_mfma_f32_32x32x16_bf16 v[144:159], a[148:151], v[116:119], v[144:159]\n"
    "ds_read_b128 a[148:151], v7 offset:40960\n"
    "v_add_f32 v18, v18, v35\n"
    "v_cvt_pk_bf16_f32 v213, v34, v35\n"
    "v_exp_f32 v37, v173\n"
    "v_mfma_f32_32x32x16_bf16 v[128:143], a[152:155], v[88:91], v[128:143]\n"
    "v_add_f32 v18, v18, v36\n"
    "v_exp_f32 v38, v174\n"
    "v_add_f32 v18, v18, v37\n"
    "v_mfma_f32_32x32x16_bf16 v[144:159], a[152:155], v[120:123], v[144:159]\n"
    "ds_read_b128 a[152:155], v8 offset:40960\n"
    "v_cvt_pk_bf16_f32 v214, v36, v37\n"
    "v_exp_f32 v39, v175\n"
    "v_add_f32 v18, v18, v38\n"
    "v_mfma_f32_32x32x16_bf16 v[128:143], a[156:159], v[92:95], v[128:143]\n"
    "buffer_load_dwordx4 v15, s[44:47], s68 offen offset:3072 lds\n"
    "v_exp_f32 v32, v176\n"
    "v_add_f32 v18, v18, v39\n"
    "v_cvt_pk_bf16_f32 v215, v38, v39\n"
    "v_mfma_f32_32x32x16_bf16 v[144:159], a[156:159], v[124:127], v[144:159]\n"
    "ds_read_b128 a[156:159], v9 offset:40960\n"
    "v_exp_f32 v33, v177\n"
    "s_add_u32 s68, s68, 0x4000\n"
    "v_mfma_f32_32x32x16_bf16 a[0:15], a[160:163], v[192:195], a[0:15]\n"
    "v_exp_f32 v34, v178\n"
    "v_add_f32 v19, v32, v33\n"
    "v_cvt_pk_bf16_f32 v216, v32, v33\n"
    "v_mfma_f32_32x32x16_bf16 a[16:31], a[160:163], v[200:203], a[16:31]\n"
    "ds_read_b64_tr_b16 a[160:161], v10 offset:24576\n"
    "ds_read_b64_tr_b16 a[162:163], v10 offset:26624\n"
    "v_exp_f32 v35, v179\n"
    "v_add_f32 v19, v19, v34\n"
    "v_mfma_f32_32x32x16_bf16 a[32:47], a[164:167], v[192:195], a[32:47]\n"
    "v_exp_f32 v36, v180\n"
    "v_add_f32 v19, v19, v35\n"
    "v_cvt_pk_bf16_f32 v217, v34, v35\n"
    "v_exp_f32 v37, v181\n"
    "v_mfma_f32_32x32x16_bf16 a[48:63], a[164:167], v[200:203], a[48:63]\n"
    "ds_read_b64_tr_b16 a[164:165], v10 offset:24832\n"
    "ds_read_b64_tr_b16 a[166:167], v10 offset:26880\n"
    "v_add_f32 v19, v19, v36\n"
    "v_exp_f32 v38, v182\n"
    "s_waitcnt lgkmcnt(15)\n"
    "v_mfma_f32_32x32x16_bf16 a[64:79], a[168:171], v[192:195], a[64:79]\n"
    "v_add_f32 v19, v19, v37\n"
    "v_cvt_pk_bf16_f32 v218, v36, v37\n"
    "v_exp_f32 v39, v183\n"
    "v_mfma_f32_32x32x16_bf16 a[80:95], a[168:171], v[200:203], a[80:95]\n"
    "ds_read_b64_tr_b16 a[168:169], v10 offset:25088\n"
    "ds_read_b64_tr_b16 a[170:171], v10 offset:27136\n"
    "v_add_f32 v19, v19, v38\n"
    "v_exp_f32 v32, v184\n"
    "v_mfma_f32_32x32x16_bf16 a[96:111], a[172:175], v[192:195], a[96:111]\n"
    "v_add_f32 v19, v19, v39\n"
    "v_cvt_pk_bf16_f32 v219, v38, v39\n"
    "v_exp_f32 v33, v185\n"
    "v_add_f32 v19, v19, v32\n"
    "v_mfma_f32_32x32x16_bf16 a[112:127], a[172:175], v[200:203], a[112:127]\n"
    "ds_read_b64_tr_b16 a[172:173], v10 offset:25344\n"
    "ds_read_b64_tr_b16 a[174:175], v10 offset:27392\n"
    "v_exp_f32 v34, v186\n"
    "v_add_f32 v19, v19, v33\n"
    "v_mfma_f32_32x32x16_bf16 a[0:15], a[176:179], v[196:199], a[0:15]\n"
    "v_cvt_pk_bf16_f32 v220, v32, v33\n"
    "v_exp_f32 v35, v187\n"
    "v_add_f32 v19, v19, v34\n"
    "v_mfma_f32_32x32x16_bf16 a[16:31], a[176:179], v[204:207], a[16:31]\n"
    "ds_read_b64_tr_b16 a[176:177], v10 offset:28672\n"
    "ds_read_b64_tr_b16 a[178:179], v10 offset:30720\n"
    "v_exp_f32 v36, v188\n"
    "v_add_f32 v19, v19, v35\n"
    "v_mfma_f32_32x32x16_bf16 a[32:47], a[180:183], v[196:199], a[32:47]\n"
    "v_cvt_pk_bf16_f32 v221, v34, v35\n"
    "v_exp_f32 v37, v189\n"
    "v_add_f32 v19, v19, v36\n"
    "v_mfma_f32_32x32x16_bf16 a[48:63], a[180:183], v[204:207], a[48:63]\n"
    "ds_read_b64_tr_b16 a[180:181], v10 offset:28928\n"
    "ds_read_b64_tr_b16 a[182:183], v10 offset:30976\n"
    "v_exp_f32 v38, v190\n"
    "v_add_f32 v19, v19, v37\n"
    "v_cvt_pk_bf16_f32 v222, v36, v37\n"
    "s_waitcnt lgkmcnt(15)\n"
    "v_mfma_f32_32x32x16_bf16 a[64:79], a[184:187], v[196:199], a[64:79]\n"
    "v_exp_f32 v39, v191\n"
    "v_add_f32 v19, v19, v38\n"
    "v_add_f32 v19, v19, v39\n"
    "v_mfma_f32_32x32x16_bf16 a[80:95], a[184:187], v[204:207], a[80:95]\n"
    "ds_read_b64_tr_b16 a[184:185], v10 offset:29184\n"
    "ds_read_b64_tr_b16 a[186:187], v10 offset:31232\n"
    "v_cvt_pk_bf16_f32 v223, v38, v39\n"
    "v_max_f32 v20, v18, v19\n"
    "v_cmp_lt_f32 vcc, s72, v20\n"
    "v_mfma_f32_32x32x16_bf16 a[96:111], a[188:191], v[196:199], a[96:111]\n"
    "v_mfma_f32_32x32x16_bf16 a[112:127], a[188:191], v[204:207], a[112:127]\n"
    "ds_read_b64_tr_b16 a[188:189], v10 offset:29440\n"
    "ds_read_b64_tr_b16 a[190:191], v10 offset:31488\n"
    "s_cbranch_vccnz .Lfb_stub3\n"
    ".Lfb_ret3:\n"
    "v_mfma_f32_32x32x16_bf16 v[160:175], a[128:131], v[64:67], v[224:239]\n"
    "s_waitcnt vmcnt(8)\n"
    "s_barrier\n"
    "v_add_f32 v16, v16, v18\n"
    "v_add_f32 v17, v17, v19\n"
    "v_mfma_f32_32x32x16_bf16 v[176:191], a[128:131], v[96:99], v[240:255]\n"
    "ds_read_b128 a[128:131], v2 offset:49152\n"
    "v_exp_f32 v32, v128\n"
    "v_exp_f32 v33, v129\n"
    "s_add_u32 m0, s69, 0x4000\n"
    "v_mfma_f32_32x32x16_bf16 v[160:175], a[132:135], v[68:71], v[160:175]\n"
    "buffer_load_dwordx4 v11, s[40:43], s67 offen offset:0 lds\n"
    "v_exp_f32 v34, v130\n"
    "v_add_f32 v18, v32, v33\n"
    "v_cvt_pk_bf16_f32 v192, v32, v33\n"
    "v_mfma_f32_32x32x16_bf16 v[176:191], a[132:135], v[100:103], v[176:191]\n"
    "ds_read_b128 a[132:135], v3 offset:49152\n"
    "v_exp_f32 v35, v131\n"
    "v_add_f32 v18, v18, v34\n"
    "v_mfma_f32_32x32x16_bf16 v[160:175], a[136:139], v[72:75], v[160:175]\n"
    "v_exp_f32 v36, v132\n"
    "v_add_f32 v18, v18, v35\n"
    "v_cvt_pk_bf16_f32 v193, v34, v35\n"
    "v_mfma_f32_32x32x16_bf16 v[176:191], a[136:139], v[104:107], v[176:191]\n"
    "ds_read_b128 a[136:139], v4 offset:49152\n"
    "v_exp_f32 v37, v133\n"
    "v_add_f32 v18, v18, v36\n"
    "v_exp_f32 v38, v134\n"
    "v_mfma_f32_32x32x16_bf16 v[160:175], a[140:143], v[76:79], v[160:175]\n"
    "buffer_load_dwordx4 v12, s[40:43], s67 offen offset:1024 lds\n"
    "v_add_f32 v18, v18, v37\n"
    "v_cvt_pk_bf16_f32 v194, v36, v37\n"
    "v_exp_f32 v39, v135\n"
    "v_mfma_f32_32x32x16_bf16 v[176:191], a[140:143], v[108:111], v[176:191]\n"
    "ds_read_b128 a[140:143], v5 offset:49152\n"
    "v_add_f32 v18, v18, v38\n"
    "v_exp_f32 v32, v136\n"
    "v_add_f32 v18, v18, v39\n"
    "v_mfma_f32_32x32x16_bf16 v[160:175], a[144:147], v[80:83], v[160:175]\n"
    "v_cvt_pk_bf16_f32 v195, v38, v39\n"
    "v_exp_f32 v33, v137\n"
    "v_add_f32 v18, v18, v32\n"
    "v_mfma_f32_32x32x16_bf16 v[176:191], a[144:147], v[112:115], v[176:191]\n"
    "ds_read_b128 a[144:147], v6 offset:49152\n"
    "v_exp_f32 v34, v138\n"
    "v_add_f32 v18, v18, v33\n"
    "v_cvt_pk_bf16_f32 v196, v32, v33\n"
    "s_waitcnt lgkmcnt(15)\n"
    "v_mfma_f32_32x32x16_bf16 v[160:175], a[148:151], v[84:87], v[160:175]\n"
    "buffer_load_dwordx4 v13, s[40:43], s67 offen offset:2048 lds\n"
    "v_exp_f32 v35, v139\n"
    "v_add_f32 v18, v18, v34\n"
    "v_mfma_f32_32x32x16_bf16 v[176:191], a[148:151], v[116:119], v[176:191]\n"
    "ds_read_b128 a[148:151], v7 offset:49152\n"
    "v_exp_f32 v36, v140\n"
    "v_add_f32 v18, v18, v35\n"
    "v_cvt_pk_bf16_f32 v197, v34, v35\n"
    "v_mfma_f32_32x32x16_bf16 v[160:175], a[152:155], v[88:91], v[160:175]\n"
    "v_exp_f32 v37, v141\n"
    "v_add_f32 v18, v18, v36\n"
    "v_exp_f32 v38, v142\n"
    "v_mfma_f32_32x32x16_bf16 v[176:191], a[152:155], v[120:123], v[176:191]\n"
    "ds_read_b128 a[152:155], v8 offset:49152\n"
    "v_add_f32 v18, v18, v37\n"
    "v_cvt_pk_bf16_f32 v198, v36, v37\n"
    "v_exp_f32 v39, v143\n"
    "v_mfma_f32_32x32x16_bf16 v[160:175], a[156:159], v[92:95], v[160:175]\n"
    "buffer_load_dwordx4 v14, s[40:43], s67 offen offset:3072 lds\n"
    "v_add_f32 v18, v18, v38\n"
    "v_exp_f32 v32, v144\n"
    "v_add_f32 v18, v18, v39\n"
    "v_mfma_f32_32x32x16_bf16 v[176:191], a[156:159], v[124:127], v[176:191]\n"
    "ds_read_b128 a[156:159], v9 offset:49152\n"
    "v_cvt_pk_bf16_f32 v199, v38, v39\n"
    "v_exp_f32 v33, v145\n"
    "s_add_u32 s67, s67, 0x4000\n"
    "v_mfma_f32_32x32x16_bf16 a[0:15], a[160:163], v[208:211], a[0:15]\n"
    "v_exp_f32 v34, v146\n"
    "v_add_f32 v19, v32, v33\n"
    "v_cvt_pk_bf16_f32 v200, v32, v33\n"
    "v_mfma_f32_32x32x16_bf16 a[16:31], a[160:163], v[216:219], a[16:31]\n"
    "ds_read_b64_tr_b16 a[160:161], v10 offset:32768\n"
    "ds_read_b64_tr_b16 a[162:163], v10 offset:34816\n"
    "v_exp_f32 v35, v147\n"
    "v_add_f32 v19, v19, v34\n"
    "v_mfma_f32_32x32x16_bf16 a[32:47], a[164:167], v[208:211], a[32:47]\n"
    "v_exp_f32 v36, v148\n"
    "v_add_f32 v19, v19, v35\n"
    "v_cvt_pk_bf16_f32 v201, v34, v35\n"
    "v_mfma_f32_32x32x16_bf16 a[48:63], a[164:167], v[216:219], a[48:63]\n"
    "ds_read_b64_tr_b16 a[164:165], v10 offset:33024\n"
    "ds_read_b64_tr_b16 a[166:167], v10 offset:35072\n"
    "v_exp_f32 v37, v149\n"
    "v_add_f32 v19, v19, v36\n"
    "v_mfma_f32_32x32x16_bf16 a[64:79], a[168:171], v[208:211], a[64:79]\n"
    "v_exp_f32 v38, v150\n"
    "v_add_f32 v19, v19, v37\n"
    "v_cvt_pk_bf16_f32 v202, v36, v37\n"
    "v_mfma_f32_32x32x16_bf16 a[80:95], a[168:171], v[216:219], a[80:95]\n"
    "ds_read_b64_tr_b16 a[168:169], v10 offset:33280\n"
    "ds_read_b64_tr_b16 a[170:171], v10 offset:35328\n"
    "v_exp_f32 v39, v151\n"
    "v_add_f32 v19, v19, v38\n"
    "s_waitcnt lgkmcnt(15)\n"
    "v_mfma_f32_32x32x16_bf16 a[96:111], a[172:175], v[208:211], a[96:111]\n"
    "v_exp_f32 v32, v152\n"
    "v_add_f32 v19, v19, v39\n"
    "v_cvt_pk_bf16_f32 v203, v38, v39\n"
    "v_exp_f32 v33, v153\n"
    "v_mfma_f32_32x32x16_bf16 a[112:127], a[172:175], v[216:219], a[112:127]\n"
    "ds_read_b64_tr_b16 a[172:173], v10 offset:33536\n"
    "ds_read_b64_tr_b16 a[174:175], v10 offset:35584\n"
    "v_add_f32 v19, v19, v32\n"
    "v_exp_f32 v34, v154\n"
    "v_mfma_f32_32x32x16_bf16 a[0:15], a[176:179], v[212:215], a[0:15]\n"
    "v_add_f32 v19, v19, v33\n"
    "v_cvt_pk_bf16_f32 v204, v32, v33\n"
    "v_exp_f32 v35, v155\n"
    "v_mfma_f32_32x32x16_bf16 a[16:31], a[176:179], v[220:223], a[16:31]\n"
    "ds_read_b64_tr_b16 a[176:177], v10 offset:36864\n"
    "ds_read_b64_tr_b16 a[178:179], v10 offset:38912\n"
    "v_add_f32 v19, v19, v34\n"
    "v_exp_f32 v36, v156\n"
    "v_mfma_f32_32x32x16_bf16 a[32:47], a[180:183], v[212:215], a[32:47]\n"
    "v_add_f32 v19, v19, v35\n"
    "v_cvt_pk_bf16_f32 v205, v34, v35\n"
    "v_exp_f32 v37, v157\n"
    "v_add_f32 v19, v19, v36\n"
    "v_mfma_f32_32x32x16_bf16 a[48:63], a[180:183], v[220:223], a[48:63]\n"
    "ds_read_b64_tr_b16 a[180:181], v10 offset:37120\n"
    "ds_read_b64_tr_b16 a[182:183], v10 offset:39168\n"
    "v_exp_f32 v38, v158\n"
    "v_add_f32 v19, v19, v37\n"
    "v_mfma_f32_32x32x16_bf16 a[64:79], a[184:187], v[212:215], a[64:79]\n"
    "v_cvt_pk_bf16_f32 v206, v36, v37\n"
    "v_exp_f32 v39, v159\n"
    "v_add_f32 v19, v19, v38\n"
    "v_mfma_f32_32x32x16_bf16 a[80:95], a[184:187], v[220:223], a[80:95]\n"
    "ds_read_b64_tr_b16 a[184:185], v10 offset:37376\n"
    "ds_read_b64_tr_b16 a[186:187], v10 offset:39424\n"
    "v_add_f32 v19, v19, v39\n"
    "v_cvt_pk_bf16_f32 v207, v38, v39\n"
    "v_max_f32 v20, v18, v19\n"
    "s_waitcnt lgkmcnt(15)\n"
    "v_mfma_f32_32x32x16_bf16 a[96:111], a[188:191], v[212:215], a[96:111]\n"
    "v_cmp_lt_f32 vcc, s72, v20\n"
    "v_mfma_f32_32x32x16_bf16 a[112:127], a[188:191], v[220:223], a[112:127]\n"
    "ds_read_b64_tr_b16 a[188:189], v10 offset:37632\n"
    "ds_read_b64_tr_b16 a[190:191], v10 offset:39680\n"
    "s_cbranch_vccnz .Lfb_stub4\n"
    ".Lfb_ret4:\n"
    "v_mfma_f32_32x32x16_bf16 v[128:143], a[128:131], v[64:67], v[224:239]\n"
    "v_add_f32 v16, v16, v18\n"
    "v_add_f32 v17, v17, v19\n"
    "v_exp_f32 v32, v160\n"
    "v_mfma_f32_32x32x16_bf16 v[144:159], a[128:131], v[96:99], v[240:255]\n"
    "ds_read_b128 a[128:131], v2 offset:57344\n"
    "v_exp_f32 v33, v161\n"
    "s_add_u32 m0, s69, 0x10000\n"
    "v_mfma_f32_32x32x16_bf16 v[128:143], a[132:135], v[68:71], v[128:143]\n"
    "buffer_load_dwordx4 v15, s[44:47], s68 offen offset:0 lds\n"
    "v_exp_f32 v34, v162\n"
    "v_add_f32 v18, v32, v33\n"
    "v_cvt_pk_bf16_f32 v208, v32, v33\n"
    "v_mfma_f32_32x32x16_bf16 v[144:159], a[132:135], v[100:103], v[144:159]\n"
    "ds_read_b128 a[132:135], v3 offset:57344\n"
    "v_exp_f32 v35, v163\n"
    "v_add_f32 v18, v18, v34\n"
    "v_exp_f32 v36, v164\n"
    "v_mfma_f32_32x32x16_bf16 v[128:143], a[136:139], v[72:75], v[128:143]\n"
    "v_add_f32 v18, v18, v35\n"
    "v_cvt_pk_bf16_f32 v209, v34, v35\n"
    "v_exp_f32 v37, v165\n"
    "v_mfma_f32_32x32x16_bf16 v[144:159], a[136:139], v[104:107], v[144:159]\n"
    "ds_read_b128 a[136:139], v4 offset:57344\n"
    "v_add_f32 v18, v18, v36\n"
    "v_exp_f32 v38, v166\n"
    "v_add_f32 v18, v18, v37\n"
    "v_mfma_f32_32x32x16_bf16 v[128:143], a[140:143], v[76:79], v[128:143]\n"
    "buffer_load_dwordx4 v15, s[44:47], s68 offen offset:1024 lds\n"
    "v_cvt_pk_bf16_f32 v210, v36, v37\n"
    "v_exp_f32 v39, v167\n"
    "v_add_f32 v18, v18, v38\n"
    "v_mfma_f32_32x32x16_bf16 v[144:159], a[140:143], v[108:111], v[144:159]\n"
    "ds_read_b128 a[140:143], v5 offset:57344\n"
    "v_exp_f32 v32, v168\n"
    "v_add_f32 v18, v18, v39\n"
    "v_cvt_pk_bf16_f32 v211, v38, v39\n"
    "v_mfma_f32_32x32x16_bf16 v[128:143], a[144:147], v[80:83], v[128:143]\n"
    "v_exp_f32 v33, v169\n"
    "v_add_f32 v18, v18, v32\n"
    "v_mfma_f32_32x32x16_bf16 v[144:159], a[144:147], v[112:115], v[144:159]\n"
    "ds_read_b128 a[144:147], v6 offset:57344\n"
    "v_exp_f32 v34, v170\n"
    "v_add_f32 v18, v18, v33\n"
    "v_cvt_pk_bf16_f32 v212, v32, v33\n"
    "v_mfma_f32_32x32x16_bf16 v[128:143], a[148:151], v[84:87], v[128:143]\n"
    "buffer_load_dwordx4 v15, s[44:47], s68 offen offset:2048 lds\n"
    "v_exp_f32 v35, v171\n"
    "v_add_f32 v18, v18, v34\n"
    "v_exp_f32 v36, v172\n"
    "v_mfma_f32_32x32x16_bf16 v[144:159], a[148:151], v[116:119], v[144:159]\n"
    "ds_read_b128 a[148:151], v7 offset:57344\n"
    "v_add_f32 v18, v18, v35\n"
    "v_cvt_pk_bf16_f32 v213, v34, v35\n"
    "v_exp_f32 v37, v173\n"
    "v_mfma_f32_32x32x16_bf16 v[128:143], a[152:155], v[88:91], v[128:143]\n"
    "v_add_f32 v18, v18, v36\n"
    "v_exp_f32 v38, v174\n"
    "v_add_f32 v18, v18, v37\n"
    "v_mfma_f32_32x32x16_bf16 v[144:159], a[152:155], v[120:123], v[144:159]\n"
    "ds_read_b128 a[152:155], v8 offset:57344\n"
    "v_cvt_pk_bf16_f32 v214, v36, v37\n"
    "v_exp_f32 v39, v175\n"
    "v_add_f32 v18, v18, v38\n"
    "s_waitcnt lgkmcnt(15)\n"
    "v_mfma_f32_32x32x16_bf16 v[128:143], a[156:159], v[92:95], v[128:143]\n"
    "buffer_load_dwordx4 v15, s[44:47], s68 offen offset:3072 lds\n"
    "v_exp_f32 v32, v176\n"
    "v_add_f32 v18, v18, v39\n"
    "v_cvt_pk_bf16_f32 v215, v38, v39\n"
    "v_mfma_f32_32x32x16_bf16 v[144:159], a[156:159], v[124:127], v[144:159]\n"
    "ds_read_b128 a[156:159], v9 offset:57344\n"
    "v_exp_f32 v33, v177\n"
    "s_add_u32 s68, s68, 0x4000\n"
    "v_mfma_f32_32x32x16_bf16 a[0:15], a[160:163], v[192:195], a[0:15]\n"
    "v_exp_f32 v34, v178\n"
    "v_add_f32 v19, v32, v33\n"
    "v_cvt_pk_bf16_f32 v216, v32, v33\n"
    "v_mfma_f32_32x32x16_bf16 a[16:31], a[160:163], v[200:203], a[16:31]\n"
    "ds_read_b64_tr_b16 a[160:161], v10 offset:40960\n"
    "ds_read_b64_tr_b16 a[162:163], v10 offset:43008\n"
    "v_exp_f32 v35, v179\n"
    "v_add_f32 v19, v19, v34\n"
    "v_mfma_f32_32x32x16_bf16 a[32:47], a[164:167], v[192:195], a[32:47]\n"
    "v_exp_f32 v36, v180\n"
    "v_add_f32 v19, v19, v35\n"
    "v_cvt_pk_bf16_f32 v217, v34, v35\n"
    "v_exp_f32 v37, v181\n"
    "v_mfma_f32_32x32x16_bf16 a[48:63], a[164:167], v[200:203], a[48:63]\n"
    "ds_read_b64_tr_b16 a[164:165], v10 offset:41216\n"
    "ds_read_b64_tr_b16 a[166:167], v10 offset:43264\n"
    "v_add_f32 v19, v19, v36\n"
    "v_exp_f32 v38, v182\n"
    "v_mfma_f32_32x32x16_bf16 a[64:79], a[168:171], v[192:195], a[64:79]\n"
    "v_add_f32 v19, v19, v37\n"
    "v_cvt_pk_bf16_f32 v218, v36, v37\n"
    "v_exp_f32 v39, v183\n"
    "v_mfma_f32_32x32x16_bf16 a[80:95], a[168:171], v[200:203], a[80:95]\n"
    "ds_read_b64_tr_b16 a[168:169], v10 offset:41472\n"
    "ds_read_b64_tr_b16 a[170:171], v10 offset:43520\n"
    "v_add_f32 v19, v19, v38\n"
    "v_exp_f32 v32, v184\n"
    "v_mfma_f32_32x32x16_bf16 a[96:111], a[172:175], v[192:195], a[96:111]\n"
    "v_add_f32 v19, v19, v39\n"
    "v_cvt_pk_bf16_f32 v219, v38, v39\n"
    "v_exp_f32 v33, v185\n"
    "v_add_f32 v19, v19, v32\n"
    "v_mfma_f32_32x32x16_bf16 a[112:127], a[172:175], v[200:203], a[112:127]\n"
    "ds_read_b64_tr_b16 a[172:173], v10 offset:41728\n"
    "ds_read_b64_tr_b16 a[174:175], v10 offset:43776\n"
    "v_exp_f32 v34, v186\n"
    "v_add_f32 v19, v19, v33\n"
    "s_waitcnt lgkmcnt(15)\n"
    "v_mfma_f32_32x32x16_bf16 a[0:15], a[176:179], v[196:199], a[0:15]\n"
    "v_cvt_pk_bf16_f32 v220, v32, v33\n"
    "v_exp_f32 v35, v187\n"
    "v_add_f32 v19, v19, v34\n"
    "v_mfma_f32_32x32x16_bf16 a[16:31], a[176:179], v[204:207], a[16:31]\n"
    "ds_read_b64_tr_b16 a[176:177], v10 offset:45056\n"
    "ds_read_b64_tr_b16 a[178:179], v10 offset:47104\n"
    "v_exp_f32 v36, v188\n"
    "v_add_f32 v19, v19, v35\n"
    "v_mfma_f32_32x32x16_bf16 a[32:47], a[180:183], v[196:199], a[32:47]\n"
    "v_cvt_pk_bf16_f32 v221, v34, v35\n"
    "v_exp_f32 v37, v189\n"
    "v_add_f32 v19, v19, v36\n"
    "v_mfma_f32_32x32x16_bf16 a[48:63], a[180:183], v[204:207], a[48:63]\n"
    "ds_read_b64_tr_b16 a[180:181], v10 offset:45312\n"
    "ds_read_b64_tr_b16 a[182:183], v10 offset:47360\n"
    "v_exp_f32 v38, v190\n"
    "v_add_f32 v19, v19, v37\n"
    "v_cvt_pk_bf16_f32 v222, v36, v37\n"
    "v_mfma_f32_32x32x16_bf16 a[64:79], a[184:187], v[196:199], a[64:79]\n"
    "v_exp_f32 v39, v191\n"
    "v_add_f32 v19, v19, v38\n"
    "v_add_f32 v19, v19, v39\n"
    "v_mfma_f32_32x32x16_bf16 a[80:95], a[184:187], v[204:207], a[80:95]\n"
    "ds_read_b64_tr_b16 a[184:185], v10 offset:45568\n"
    "ds_read_b64_tr_b16 a[186:187], v10 offset:47616\n"
    "v_cvt_pk_bf16_f32 v223, v38, v39\n"
    "v_max_f32 v20, v18, v19\n"
    "v_cmp_lt_f32 vcc, s72, v20\n"
    "v_mfma_f32_32x32x16_bf16 a[96:111], a[188:191], v[196:199], a[96:111]\n"
    "v_mfma_f32_32x32x16_bf16 a[112:127], a[188:191], v[204:207], a[112:127]\n"
    "ds_read_b64_tr_b16 a[188:189], v10 offset:45824\n"
    "ds_read_b64_tr_b16 a[190:191], v10 offset:47872\n"
    "s_cbranch_vccnz .Lfb_stub5\n"
    ".Lfb_ret5:\n"
    "v_mfma_f32_32x32x16_bf16 v[160:175], a[128:131], v[64:67], v[224:239]\n"
    "s_waitcnt vmcnt(8)\n"
    "s_barrier\n"
    "v_add_f32 v16, v16, v18\n"
    "v_add_f32 v17, v17, v19\n"
    "v_mfma_f32_32x32x16_bf16 v[176:191], a[128:131], v[96:99], v[240:255]\n"
    "ds_read_b128 a[128:131], v2 offset:0\n"
    "v_exp_f32 v32, v128\n"
    "v_exp_f32 v33, v129\n"
    "s_add_u32 m0, s69, 0x8000\n"
    "s_waitcnt lgkmcnt(15)\n"
    "v_mfma_f32_32x32x16_bf16 v[160:175], a[132:135], v[68:71], v[160:175]\n"
    "buffer_load_dwordx4 v11, s[40:43], s67 offen offset:0 lds\n"
    "v_exp_f32 v34, v130\n"
    "v_add_f32 v18, v32, v33\n"
    "v_cvt_pk_bf16_f32 v192, v32, v33\n"
    "v_mfma_f32_32x32x16_bf16 v[176:191], a[132:135], v[100:103], v[176:191]\n"
    "ds_read_b128 a[132:135], v3 offset:0\n"
    "v_exp_f32 v35, v131\n"
    "v_add_f32 v18, v18, v34\n"
    "v_mfma_f32_32x32x16_bf16 v[160:175], a[136:139], v[72:75], v[160:175]\n"
    "v_exp_f32 v36, v132\n"
    "v_add_f32 v18, v18, v35\n"
    "v_cvt_pk_bf16_f32 v193, v34, v35\n"
    "v_mfma_f32_32x32x16_bf16 v[176:191], a[136:139], v[104:107], v[176:191]\n"
    "ds_read_b128 a[136:139], v4 offset:0\n"
    "v_exp_f32 v37, v133\n"
    "v_add_f32 v18, v18, v36\n"
    "v_exp_f32 v38, v134\n"
    "v_mfma_f32_32x32x16_bf16 v[160:175], a[140:143], v[76:79], v[160:175]\n"
    "buffer_load_dwordx4 v12, s[40:43], s67 offen offset:1024 lds\n"
    "v_add_f32 v18, v18, v37\n"
    "v_cvt_pk_bf16_f32 v194, v36, v37\n"
    "v_exp_f32 v39, v135\n"
    "v_mfma_f32_32x32x16_bf16 v[176:191], a[140:143], v[108:111], v[176:191]\n"
    "ds_read_b128 a[140:143], v5 offset:0\n"
    "v_add_f32 v18, v18, v38\n"
    "v_exp_f32 v32, v136\n"
    "v_add_f32 v18, v18, v39\n"
    "v_mfma_f32_32x32x16_bf16 v[160:175], a[144:147], v[80:83], v[160:175]\n"
    "v_cvt_pk_bf16_f32 v195, v38, v39\n"
    "v_exp_f32 v33, v137\n"
    "v_add_f32 v18, v18, v32\n"
    "v_mfma_f32_32x32x16_bf16 v[176:191], a[144:147], v[112:115], v[176:191]\n"
    "ds_read_b128 a[144:147], v6 offset:0\n"
    "v_exp_f32 v34, v138\n"
    "v_add_f32 v18, v18, v33\n"
    "v_cvt_pk_bf16_f32 v196, v32, v33\n"
    "v_mfma_f32_32x32x16_bf16 v[160:175], a[148:151], v[84:87], v[160:175]\n"
    "buffer_load_dwordx4 v13, s[40:43], s67 offen offset:2048 lds\n"
    "v_exp_f32 v35, v139\n"
    "v_add_f32 v18, v18, v34\n"
    "v_mfma_f32_32x32x16_bf16 v[176:191], a[148:151], v[116:119], v[176:191]\n"
    "ds_read_b128 a[148:151], v7 offset:0\n"
    "v_exp_f32 v36, v140\n"
    "v_add_f32 v18, v18, v35\n"
    "v_cvt_pk_bf16_f32 v197, v34, v35\n"
    "v_mfma_f32_32x32x16_bf16 v[160:175], a[152:155], v[88:91], v[160:175]\n"
    "v_exp_f32 v37, v141\n"
    "v_add_f32 v18, v18, v36\n"
    "v_exp_f32 v38, v142\n"
    "v_mfma_f32_32x32x16_bf16 v[176:191], a[152:155], v[120:123], v[176:191]\n"
    "ds_read_b128 a[152:155], v8 offset:0\n"
    "v_add_f32 v18, v18, v37\n"
    "v_cvt_pk_bf16_f32 v198, v36, v37\n"
    "v_exp_f32 v39, v143\n"
    "v_mfma_f32_32x32x16_bf16 v[160:175], a[156:159], v[92:95], v[160:175]\n"
    "buffer_load_dwordx4 v14, s[40:43], s67 offen offset:3072 lds\n"
    "v_add_f32 v18, v18, v38\n"
    "v_exp_f32 v32, v144\n"
    "v_add_f32 v18, v18, v39\n"
    "v_mfma_f32_32x32x16_bf16 v[176:191], a[156:159], v[124:127], v[176:191]\n"
    "ds_read_b128 a[156:159], v9 offset:0\n"
    "v_cvt_pk_bf16_f32 v199, v38, v39\n"
    "v_exp_f32 v33, v145\n"
    "s_add_u32 s67, s67, 0x4000\n"
    "v_mfma_f32_32x32x16_bf16 a[0:15], a[160:163], v[208:211], a[0:15]\n"
    "v_exp_f32 v34, v146\n"
    "v_add_f32 v19, v32, v33\n"
    "v_cvt_pk_bf16_f32 v200, v32, v33\n"
    "v_mfma_f32_32x32x16_bf16 a[16:31], a[160:163], v[216:219], a[16:31]\n"
    "ds_read_b64_tr_b16 a[160:161], v10 offset:49152\n"
    "ds_read_b64_tr_b16 a[162:163], v10 offset:51200\n"
    "v_exp_f32 v35, v147\n"
    "v_add_f32 v19, v19, v34\n"
    "s_waitcnt lgkmcnt(15)\n"
    "v_mfma_f32_32x32x16_bf16 a[32:47], a[164:167], v[208:211], a[32:47]\n"
    "v_exp_f32 v36, v148\n"
    "v_add_f32 v19, v19, v35\n"
    "v_cvt_pk_bf16_f32 v201, v34, v35\n"
    "v_mfma_f32_32x32x16_bf16 a[48:63], a[164:167], v[216:219], a[48:63]\n"
    "ds_read_b64_tr_b16 a[164:165], v10 offset:49408\n"
    "ds_read_b64_tr_b16 a[166:167], v10 offset:51456\n"
    "v_exp_f32 v37, v149\n"
    "v_add_f32 v19, v19, v36\n"
    "v_mfma_f32_32x32x16_bf16 a[64:79], a[168:171], v[208:211], a[64:79]\n"
    "v_exp_f32 v38, v150\n"
    "v_add_f32 v19, v19, v37\n"
    "v_cvt_pk_bf16_f32 v202, v36, v37\n"
    "v_mfma_f32_32x32x16_bf16 a[80:95], a[168:171], v[216:219], a[80:95]\n"
    "ds_read_b64_tr_b16 a[168:169], v10 offset:49664\n"
    "ds_read_b64_tr_b16 a[170:171], v10 offset:51712\n"
    "v_exp_f32 v39, v151\n"
    "v_add_f32 v19, v19, v38\n"
    "v_mfma_f32_32x32x16_bf16 a[96:111], a[172:175], v[208:211], a[96:111]\n"
    "v_exp_f32 v32, v152\n"
    "v_add_f32 v19, v19, v39\n"
    "v_cvt_pk_bf16_f32 v203, v38, v39\n"
    "v_exp_f32 v33, v153\n"
    "v_mfma_f32_32x32x16_bf16 a[112:127], a[172:175], v[216:219], a[112:127]\n"
    "ds_read_b64_tr_b16 a[172:173], v10 offset:49920\n"
    "ds_read_b64_tr_b16 a[174:175], v10 offset:51968\n"
    "v_add_f32 v19, v19, v32\n"
    "v_exp_f32 v34, v154\n"
    "v_mfma_f32_32x32x16_bf16 a[0:15], a[176:179], v[212:215], a[0:15]\n"
    "v_add_f32 v19, v19, v33\n"
    "v_cvt_pk_bf16_f32 v204, v32, v33\n"
    "v_exp_f32 v35, v155\n"
    "v_mfma_f32_32x32x16_bf16 a[16:31], a[176:179], v[220:223], a[16:31]\n"
    "ds_read_b64_tr_b16 a[176:177], v10 offset:53248\n"
    "ds_read_b64_tr_b16 a[178:179], v10 offset:55296\n"
    "v_add_f32 v19, v19, v34\n"
    "v_exp_f32 v36, v156\n"
    "s_waitcnt lgkmcnt(15)\n"
    "v_mfma_f32_32x32x16_bf16 a[32:47], a[180:183], v[212:215], a[32:47]\n"
    "v_add_f32 v19, v19, v35\n"
    "v_cvt_pk_bf16_f32 v205, v34, v35\n"
    "v_exp_f32 v37, v157\n"
    "v_add_f32 v19, v19, v36\n"
    "v_mfma_f32_32x32x16_bf16 a[48:63], a[180:183], v[220:223], a[48:63]\n"
    "ds_read_b64_tr_b16 a[180:181], v10 offset:53504\n"
    "ds_read_b64_tr_b16 a[182:183], v10 offset:55552\n"
    "v_exp_f32 v38, v158\n"
    "v_add_f32 v19, v19, v37\n"
    "v_mfma_f32_32x32x16_bf16 a[64:79], a[184:187], v[212:215], a[64:79]\n"
    "v_cvt_pk_bf16_f32 v206, v36, v37\n"
    "v_exp_f32 v39, v159\n"
    "v_add_f32 v19, v19, v38\n"
    "v_mfma_f32_32x32x16_bf16 a[80:95], a[184:187], v[220:223], a[80:95]\n"
    "ds_read_b64_tr_b16 a[184:185], v10 offset:53760\n"
    "ds_read_b64_tr_b16 a[186:187], v10 offset:55808\n"
    "v_add_f32 v19, v19, v39\n"
    "v_cvt_pk_bf16_f32 v207, v38, v39\n"
    "v_max_f32 v20, v18, v19\n"
    "v_mfma_f32_32x32x16_bf16 a[96:111], a[188:191], v[212:215], a[96:111]\n"
    "v_cmp_lt_f32 vcc, s72, v20\n"
    "v_mfma_f32_32x32x16_bf16 a[112:127], a[188:191], v[220:223], a[112:127]\n"
    "ds_read_b64_tr_b16 a[188:189], v10 offset:54016\n"
    "ds_read_b64_tr_b16 a[190:191], v10 offset:56064\n"
    "s_cbranch_vccnz .Lfb_stub6\n"
    ".Lfb_ret6:\n"
    "v_mfma_f32_32x32x16_bf16 v[128:143], a[128:131], v[64:67], v[224:239]\n"
    "v_add_f32 v16, v16, v18\n"
    "v_add_f32 v17, v17, v19\n"
    "v_exp_f32 v32, v160\n"
    "v_mfma_f32_32x32x16_bf16 v[144:159], a[128:131], v[96:99], v[240:255]\n"
    "ds_read_b128 a[128:131], v2 offset:8192\n"
    "v_exp_f32 v33, v161\n"
    "s_add_u32 m0, s69, 0x14000\n"
    "v_mfma_f32_32x32x16_bf16 v[128:143], a[132:135], v[68:71], v[128:143]\n"
    "buffer_load_dwordx4 v15, s[44:47], s68 offen offset:0 lds\n"
    "v_exp_f32 v34, v162\n"
    "v_add_f32 v18, v32, v33\n"
    "v_cvt_pk_bf16_f32 v208, v32, v33\n"
    "v_mfma_f32_32x32x16_bf16 v[144:159], a[132:135], v[100:103], v[144:159]\n"
    "ds_read_b128 a[132:135], v3 offset:8192\n"
    "v_exp_f32 v35, v163\n"
    "v_add_f32 v18, v18, v34\n"
    "v_exp_f32 v36, v164\n"
    "v_mfma_f32_32x32x16_bf16 v[128:143], a[136:139], v[72:75], v[128:143]\n"
    "v_add_f32 v18, v18, v35\n"
    "v_cvt_pk_bf16_f32 v209, v34, v35\n"
    "v_exp_f32 v37, v165\n"
    "v_mfma_f32_32x32x16_bf16 v[144:159], a[136:139], v[104:107], v[144:159]\n"
    "ds_read_b128 a[136:139], v4 offset:8192\n"
    "v_add_f32 v18, v18, v36\n"
    "v_exp_f32 v38, v166\n"
    "v_add_f32 v18, v18, v37\n"
    "s_waitcnt lgkmcnt(15)\n"
    "v_mfma_f32_32x32x16_bf16 v[128:143], a[140:143], v[76:79], v[128:143]\n"
    "buffer_load_dwordx4 v15, s[44:47], s68 offen offset:1024 lds\n"
    "v_cvt_pk_bf16_f32 v210, v36, v37\n"
    "v_exp_f32 v39, v167\n"
    "v_add_f32 v18, v18, v38\n"
    "v_mfma_f32_32x32x16_bf16 v[144:159], a[140:143], v[108:111], v[144:159]\n"
    "ds_read_b128 a[140:143], v5 offset:8192\n"
    "v_exp_f32 v32, v168\n"
    "v_add_f32 v18, v18, v39\n"
    "v_cvt_pk_bf16_f32 v211, v38, v39\n"
    "v_mfma_f32_32x32x16_bf16 v[128:143], a[144:147], v[80:83], v[128:143]\n"
    "v_exp_f32 v33, v169\n"
    "v_add_f32 v18, v18, v32\n"
    "v_mfma_f32_32x32x16_bf16 v[144:159], a[144:147], v[112:115], v[144:159]\n"
    "ds_read_b128 a[144:147], v6 offset:8192\n"
    "v_exp_f32 v34, v170\n"
    "v_add_f32 v18, v18, v33\n"
    "v_cvt_pk_bf16_f32 v212, v32, v33\n"
    "v_mfma_f32_32x32x16_bf16 v[128:143], a[148:151], v[84:87], v[128:143]\n"
    "buffer_load_dwordx4 v15, s[44:47], s68 offen offset:2048 lds\n"
    "v_exp_f32 v35, v171\n"
    "v_add_f32 v18, v18, v34\n"
    "v_exp_f32 v36, v172\n"
    "v_mfma_f32_32x32x16_bf16 v[144:159], a[148:151], v[116:119], v[144:159]\n"
    "ds_read_b128 a[148:151], v7 offset:8192\n"
    "v_add_f32 v18, v18, v35\n"
    "v_cvt_pk_bf16_f32 v213, v34, v35\n"
    "v_exp_f32 v37, v173\n"
    "v_mfma_f32_32x32x16_bf16 v[128:143], a[152:155], v[88:91], v[128:143]\n"
    "v_add_f32 v18, v18, v36\n"
    "v_exp_f32 v38, v174\n"
    "v_add_f32 v18, v18, v37\n"
    "v_mfma_f32_32x32x16_bf16 v[144:159], a[152:155], v[120:123], v[144:159]\n"
    "ds_read_b128 a[152:155], v8 offset:8192\n"
    "v_cvt_pk_bf16_f32 v214, v36, v37\n"
    "v_exp_f32 v39, v175\n"
    "v_add_f32 v18, v18, v38\n"
    "v_mfma_f32_32x32x16_bf16 v[128:143], a[156:159], v[92:95], v[128:143]\n"
    "buffer_load_dwordx4 v15, s[44:47], s68 offen offset:3072 lds\n"
    "v_exp_f32 v32, v176\n"
    "v_add_f32 v18, v18, v39\n"
    "v_cvt_pk_bf16_f32 v215, v38, v39\n"
    "v_mfma_f32_32x32x16_bf16 v[144:159], a[156:159], v[124:127], v[144:159]\n"
    "ds_read_b128 a[156:159], v9 offset:8192\n"
    "v_exp_f32 v33, v177\n"
    "s_add_u32 s68, s68, 0x4000\n"
    "v_mfma_f32_32x32x16_bf16 a[0:15], a[160:163], v[192:195], a[0:15]\n"
    "v_exp_f32 v34, v178\n"
    "v_add_f32 v19, v32, v33\n"
    "v_cvt_pk_bf16_f32 v216, v32, v33\n"
    "v_mfma_f32_32x32x16_bf16 a[16:31], a[160:163], v[200:203], a[16:31]\n"
    "ds_read_b64_tr_b16 a[160:161], v10 offset:57344\n"
    "ds_read_b64_tr_b16 a[162:163], v10 offset:59392\n"
    "v_exp_f32 v35, v179\n"
    "v_add_f32 v19, v19, v34\n"
    "v_mfma_f32_32x32x16_bf16 a[32:47], a[164:167], v[192:195], a[32:47]\n"
    "v_exp_f32 v36, v180\n"
    "v_add_f32 v19, v19, v35\n"
    "v_cvt_pk_bf16_f32 v217, v34, v35\n"
    "v_exp_f32 v37, v181\n"
    "v_mfma_f32_32x32x16_bf16 a[48:63], a[164:167], v[200:203], a[48:63]\n"
    "ds_read_b64_tr_b16 a[164:165], v10 offset:57600\n"
    "ds_read_b64_tr_b16 a[166:167], v10 offset:59648\n"
    "v_add_f32 v19, v19, v36\n"
    "v_exp_f32 v38, v182\n"
    "s_waitcnt lgkmcnt(15)\n"
    "v_mfma_f32_32x32x16_bf16 a[64:79], a[168:171], v[192:195], a[64:79]\n"
    "v_add_f32 v19, v19, v37\n"
    "v_cvt_pk_bf16_f32 v218, v36, v37\n"
    "v_exp_f32 v39, v183\n"
    "v_mfma_f32_32x32x16_bf16 a[80:95], a[168:171], v[200:203], a[80:95]\n"
    "ds_read_b64_tr_b16 a[168:169], v10 offset:57856\n"
    "ds_read_b64_tr_b16 a[170:171], v10 offset:59904\n"
    "v_add_f32 v19, v19, v38\n"
    "v_exp_f32 v32, v184\n"
    "v_mfma_f32_32x32x16_bf16 a[96:111], a[172:175], v[192:195], a[96:111]\n"
    "v_add_f32 v19, v19, v39\n"
    "v_cvt_pk_bf16_f32 v219, v38, v39\n"
    "v_exp_f32 v33, v185\n"
    "v_add_f32 v19, v19, v32\n"
    "v_mfma_f32_32x32x16_bf16 a[112:127], a[172:175], v[200:203], a[112:127]\n"
    "ds_read_b64_tr_b16 a[172:173], v10 offset:58112\n"
    "ds_read_b64_tr_b16 a[174:175], v10 offset:60160\n"
    "v_exp_f32 v34, v186\n"
    "v_add_f32 v19, v19, v33\n"
    "v_mfma_f32_32x32x16_bf16 a[0:15], a[176:179], v[196:199], a[0:15]\n"
    "v_cvt_pk_bf16_f32 v220, v32, v33\n"
    "v_exp_f32 v35, v187\n"
    "v_add_f32 v19, v19, v34\n"
    "v_mfma_f32_32x32x16_bf16 a[16:31], a[176:179], v[204:207], a[16:31]\n"
    "ds_read_b64_tr_b16 a[176:177], v10 offset:61440\n"
    "ds_read_b64_tr_b16 a[178:179], v10 offset:63488\n"
    "v_exp_f32 v36, v188\n"
    "v_add_f32 v19, v19, v35\n"
    "v_mfma_f32_32x32x16_bf16 a[32:47], a[180:183], v[196:199], a[32:47]\n"
    "v_cvt_pk_bf16_f32 v221, v34, v35\n"
    "v_exp_f32 v37, v189\n"
    "v_add_f32 v19, v19, v36\n"
    "v_mfma_f32_32x32x16_bf16 a[48:63], a[180:183], v[204:207], a[48:63]\n"
    "ds_read_b64_tr_b16 a[180:181], v10 offset:61696\n"
    "ds_read_b64_tr_b16 a[182:183], v10 offset:63744\n"
    "v_exp_f32 v38, v190\n"
    "v_add_f32 v19, v19, v37\n"
    "v_cvt_pk_bf16_f32 v222, v36, v37\n"
    "s_waitcnt lgkmcnt(15)\n"
    "v_mfma_f32_32x32x16_bf16 a[64:79], a[184:187], v[196:199], a[64:79]\n"
    "v_exp_f32 v39, v191\n"
    "v_add_f32 v19, v19, v38\n"
    "v_add_f32 v19, v19, v39\n"
    "v_mfma_f32_32x32x16_bf16 a[80:95], a[184:187], v[204:207], a[80:95]\n"
    "ds_read_b64_tr_b16 a[184:185], v10 offset:61952\n"
    "ds_read_b64_tr_b16 a[186:187], v10 offset:64000\n"
    "v_cvt_pk_bf16_f32 v223, v38, v39\n"
    "v_max_f32 v20, v18, v19\n"
    "v_cmp_lt_f32 vcc, s72, v20\n"
    "v_mfma_f32_32x32x16_bf16 a[96:111], a[188:191], v[196:199], a[96:111]\n"
    "v_mfma_f32_32x32x16_bf16 a[112:127], a[188:191], v[204:207], a[112:127]\n"
    "ds_read_b64_tr_b16 a[188:189], v10 offset:62208\n"
    "ds_read_b64_tr_b16 a[190:191], v10 offset:64256\n"
    "s_cbranch_vccnz .Lfb_stub7\n"
    ".Lfb_ret7:\n"
    "v_mfma_f32_32x32x16_bf16 v[160:175], a[128:131], v[64:67], v[224:239]\n"
    "s_waitcnt vmcnt(8)\n"
    "s_barrier\n"
    "v_add_f32 v16, v16, v18\n"
    "v_add_f32 v17, v17, v19\n"
    "v_mfma_f32_32x32x16_bf16 v[176:191], a[128:131], v[96:99], v[240:255]\n"
    "ds_read_b128 a[128:131], v2 offset:16384\n"
    "v_exp_f32 v32, v128\n"
    "v_exp_f32 v33, v129\n"
    "s_add_u32 m0, s69, 0xc000\n"
    "v_mfma_f32_32x32x16_bf16 v[160:175], a[132:135], v[68:71], v[160:175]\n"
    "buffer_load_dwordx4 v11, s[40:43], s67 offen offset:0 lds\n"
    "v_exp_f32 v34, v130\n"
    "v_add_f32 v18, v32, v33\n"
    "v_cvt_pk_bf16_f32 v192, v32, v33\n"
    "v_mfma_f32_32x32x16_bf16 v[176:191], a[132:135], v[100:103], v[176:191]\n"
    "ds_read_b128 a[132:135], v3 offset:16384\n"
    "v_exp_f32 v35, v131\n"
    "v_add_f32 v18, v18, v34\n"
    "v_mfma_f32_32x32x16_bf16 v[160:175], a[136:139], v[72:75], v[160:175]\n"
    "v_exp_f32 v36, v132\n"
    "v_add_f32 v18, v18, v35\n"
    "v_cvt_pk_bf16_f32 v193, v34, v35\n"
    "v_mfma_f32_32x32x16_bf16 v[176:191], a[136:139], v[104:107], v[176:191]\n"
    "ds_read_b128 a[136:139], v4 offset:16384\n"
    "v_exp_f32 v37, v133\n"
    "v_add_f32 v18, v18, v36\n"
    "v_exp_f32 v38, v134\n"
    "v_mfma_f32_32x32x16_bf16 v[160:175], a[140:143], v[76:79], v[160:175]\n"
    "buffer_load_dwordx4 v12, s[40:43], s67 offen offset:1024 lds\n"
    "v_add_f32 v18, v18, v37\n"
    "v_cvt_pk_bf16_f32 v194, v36, v37\n"
    "v_exp_f32 v39, v135\n"
    "v_mfma_f32_32x32x16_bf16 v[176:191], a[140:143], v[108:111], v[176:191]\n"
    "ds_read_b128 a[140:143], v5 offset:16384\n"
    "v_add_f32 v18, v18, v38\n"
    "v_exp_f32 v32, v136\n"
    "v_add_f32 v18, v18, v39\n"
    "v_mfma_f32_32x32x16_bf16 v[160:175], a[144:147], v[80:83], v[160:175]\n"
    "v_cvt_pk_bf16_f32 v195, v38, v39\n"
    "v_exp_f32 v33, v137\n"
    "v_add_f32 v18, v18, v32\n"
    "v_mfma_f32_32x32x16_bf16 v[176:191], a[144:147], v[112:115], v[176:191]\n"
    "ds_read_b128 a[144:147], v6 offset:16384\n"
    "v_exp_f32 v34, v138\n"
    "v_add_f32 v18, v18, v33\n"
    "v_cvt_pk_bf16_f32 v196, v32, v33\n"
    "s_waitcnt lgkmcnt(15)\n"
    "v_mfma_f32_32x32x16_bf16 v[160:175], a[148:151], v[84:87], v[160:175]\n"
    "buffer_load_dwordx4 v13, s[40:43], s67 offen offset:2048 lds\n"
    "v_exp_f32 v35, v139\n"
    "v_add_f32 v18, v18, v34\n"
    "v_mfma_f32_32x32x16_bf16 v[176:191], a[148:151], v[116:119], v[176:191]\n"
    "ds_read_b128 a[148:151], v7 offset:16384\n"
    "v_exp_f32 v36, v140\n"
    "v_add_f32 v18, v18, v35\n"
    "v_cvt_pk_bf16_f32 v197, v34, v35\n"
    "v_mfma_f32_32x32x16_bf16 v[160:175], a[152:155], v[88:91], v[160:175]\n"
    "v_exp_f32 v37, v141\n"
    "v_add_f32 v18, v18, v36\n"
    "v_exp_f32 v38, v142\n"
    "v_mfma_f32_32x32x16_bf16 v[176:191], a[152:155], v[120:123], v[176:191]\n"
    "ds_read_b128 a[152:155], v8 offset:16384\n"
    "v_add_f32 v18, v18, v37\n"
    "v_cvt_pk_bf16_f32 v198, v36, v37\n"
    "v_exp_f32 v39, v143\n"
    "v_mfma_f32_32x32x16_bf16 v[160:175], a[156:159], v[92:95], v[160:175]\n"
    "buffer_load_dwordx4 v14, s[40:43], s67 offen offset:3072 lds\n"
    "v_add_f32 v18, v18, v38\n"
    "v_exp_f32 v32, v144\n"
    "v_add_f32 v18, v18, v39\n"
    "v_mfma_f32_32x32x16_bf16 v[176:191], a[156:159], v[124:127], v[176:191]\n"
    "ds_read_b128 a[156:159], v9 offset:16384\n"
    "v_cvt_pk_bf16_f32 v199, v38, v39\n"
    "v_exp_f32 v33, v145\n"
    "s_add_u32 s67, s67, 0x4000\n"
    "v_mfma_f32_32x32x16_bf16 a[0:15], a[160:163], v[208:211], a[0:15]\n"
    "v_exp_f32 v34, v146\n"
    "v_add_f32 v19, v32, v33\n"
    "v_cvt_pk_bf16_f32 v200, v32, v33\n"
    "v_mfma_f32_32x32x16_bf16 a[16:31], a[160:163], v[216:219], a[16:31]\n"
    "ds_read_b64_tr_b16 a[160:161], v10 offset:0\n"
    "ds_read_b64_tr_b16 a[162:163], v10 offset:2048\n"
    "v_exp_f32 v35, v147\n"
    "v_add_f32 v19, v19, v34\n"
    "v_mfma_f32_32x32x16_bf16 a[32:47], a[164:167], v[208:211], a[32:47]\n"
    "v_exp_f32 v36, v148\n"
    "v_add_f32 v19, v19, v35\n"
    "v_cvt_pk_bf16_f32 v201, v34, v35\n"
    "v_mfma_f32_32x32x16_bf16 a[48:63], a[164:167], v[216:219], a[48:63]\n"
    "ds_read_b64_tr_b16 a[164:165], v10 offset:256\n"
    "ds_read_b64_tr_b16 a[166:167], v10 offset:2304\n"
    "v_exp_f32 v37, v149\n"
    "v_add_f32 v19, v19, v36\n"
    "v_mfma_f32_32x32x16_bf16 a[64:79], a[168:171], v[208:211], a[64:79]\n"
    "v_exp_f32 v38, v150\n"
    "v_add_f32 v19, v19, v37\n"
    "v_cvt_pk_bf16_f32 v202, v36, v37\n"
    "v_mfma_f32_32x32x16_bf16 a[80:95], a[168:171], v[216:219], a[80:95]\n"
    "ds_read_b64_tr_b16 a[168:169], v10 offset:512\n"
    "ds_read_b64_tr_b16 a[170:171], v10 offset:2560\n"
    "v_exp_f32 v39, v151\n"
    "v_add_f32 v19, v19, v38\n"
    "s_waitcnt lgkmcnt(15)\n"
    "v_mfma_f32_32x32x16_bf16 a[96:111], a[172:175], v[208:211], a[96:111]\n"
    "v_exp_f32 v32, v152\n"
    "v_add_f32 v19, v19, v39\n"
    "v_cvt_pk_bf16_f32 v203, v38, v39\n"
    "v_exp_f32 v33, v153\n"
    "v_mfma_f32_32x32x16_bf16 a[112:127], a[172:175], v[216:219], a[112:127]\n"
    "ds_read_b64_tr_b16 a[172:173], v10 offset:768\n"
    "ds_read_b64_tr_b16 a[174:175], v10 offset:2816\n"
    "v_add_f32 v19, v19, v32\n"
    "v_exp_f32 v34, v154\n"
    "v_mfma_f32_32x32x16_bf16 a[0:15], a[176:179], v[212:215], a[0:15]\n"
    "v_add_f32 v19, v19, v33\n"
    "v_cvt_pk_bf16_f32 v204, v32, v33\n"
    "v_exp_f32 v35, v155\n"
    "v_mfma_f32_32x32x16_bf16 a[16:31], a[176:179], v[220:223], a[16:31]\n"
    "ds_read_b64_tr_b16 a[176:177], v10 offset:4096\n"
    "ds_read_b64_tr_b16 a[178:179], v10 offset:6144\n"
    "v_add_f32 v19, v19, v34\n"
    "v_exp_f32 v36, v156\n"
    "v_mfma_f32_32x32x16_bf16 a[32:47], a[180:183], v[212:215], a[32:47]\n"
    "v_add_f32 v19, v19, v35\n"
    "v_cvt_pk_bf16_f32 v205, v34, v35\n"
    "v_exp_f32 v37, v157\n"
    "v_add_f32 v19, v19, v36\n"
    "v_mfma_f32_32x32x16_bf16 a[48:63], a[180:183], v[220:223], a[48:63]\n"
    "ds_read_b64_tr_b16 a[180:181], v10 offset:4352\n"
    "ds_read_b64_tr_b16 a[182:183], v10 offset:6400\n"
    "v_exp_f32 v38, v158\n"
    "v_add_f32 v19, v19, v37\n"
    "v_mfma_f32_32x32x16_bf16 a[64:79], a[184:187], v[212:215], a[64:79]\n"
    "v_cvt_pk_bf16_f32 v206, v36, v37\n"
    "v_exp_f32 v39, v159\n"
    "v_add_f32 v19, v19, v38\n"
    "v_mfma_f32_32x32x16_bf16 a[80:95], a[184:187], v[220:223], a[80:95]\n"
    "ds_read_b64_tr_b16 a[184:185], v10 offset:4608\n"
    "ds_read_b64_tr_b16 a[186:187], v10 offset:6656\n"
    "v_add_f32 v19, v19, v39\n"
    "v_cvt_pk_bf16_f32 v207, v38, v39\n"
    "v_max_f32 v20, v18, v19\n"
    "s_waitcnt lgkmcnt(15)\n"
    "v_mfma_f32_32x32x16_bf16 a[96:111], a[188:191], v[212:215], a[96:111]\n"
    "v_cmp_lt_f32 vcc, s72, v20\n"
    "v_mfma_f32_32x32x16_bf16 a[112:127], a[188:191], v[220:223], a[112:127]\n"
    "ds_read_b64_tr_b16 a[188:189], v10 offset:4864\n"
    "ds_read_b64_tr_b16 a[190:191], v10 offset:6912\n"
    "s_cbranch_vccnz .Lfb_stub8\n"
    ".Lfb_ret8:\n"
    "v_mfma_f32_32x32x16_bf16 v[128:143], a[128:131], v[64:67], v[224:239]\n"
    "v_add_f32 v16, v16, v18\n"
    "v_add_f32 v17, v17, v19\n"
    "v_exp_f32 v32, v160\n"
    "v_mfma_f32_32x32x16_bf16 v[144:159], a[128:131], v[96:99], v[240:255]\n"
    "ds_read_b128 a[128:131], v2 offset:24576\n"
    "v_exp_f32 v33, v161\n"
    "s_add_u32 m0, s69, 0x18000\n"
    "v_mfma_f32_32x32x16_bf16 v[128:143], a[132:135], v[68:71], v[128:143]\n"
    "buffer_load_dwordx4 v15, s[44:47], s68 offen offset:0 lds\n"
    "v_exp_f32 v34, v162\n"
    "v_add_f32 v18, v32, v33\n"
    "v_cvt_pk_bf16_f32 v208, v32, v33\n"
    "v_mfma_f32_32x32x16_bf16 v[144:159], a[132:135], v[100:103], v[144:159]\n"
    "ds_read_b128 a[132:135], v3 offset:24576\n"
    "v_exp_f32 v35, v163\n"
    "v_add_f32 v18, v18, v34\n"
    "v_exp_f32 v36, v164\n"
    "v_mfma_f32_32x32x16_bf16 v[128:143], a[136:139], v[72:75], v[128:143]\n"
    "v_add_f32 v18, v18, v35\n"
    "v_cvt_pk_bf16_f32 v209, v34, v35\n"
    "v_exp_f32 v37, v165\n"
    "v_mfma_f32_32x32x16_bf16 v[144:159], a[136:139], v[104:107], v[144:159]\n"
    "ds_read_b128 a[136:139], v4 offset:24576\n"
    "v_add_f32 v18, v18, v36\n"
    "v_exp_f32 v38, v166\n"
    "v_add_f32 v18, v18, v37\n"
    "v_mfma_f32_32x32x16_bf16 v[128:143], a[140:143], v[76:79], v[128:143]\n"
    "buffer_load_dwordx4 v15, s[44:47], s68 offen offset:1024 lds\n"
    "v_cvt_pk_bf16_f32 v210, v36, v37\n"
    "v_exp_f32 v39, v167\n"
    "v_add_f32 v18, v18, v38\n"
    "v_mfma_f32_32x32x16_bf16 v[144:159], a[140:143], v[108:111], v[144:159]\n"
    "ds_read_b128 a[140:143], v5 offset:24576\n"
    "v_exp_f32 v32, v168\n"
    "v_add_f32 v18, v18, v39\n"
    "v_cvt_pk_bf16_f32 v211, v38, v39\n"
    "v_mfma_f32_32x32x16_bf16 v[128:143], a[144:147], v[80:83], v[128:143]\n"
    "v_exp_f32 v33, v169\n"
    "v_add_f32 v18, v18, v32\n"
    "v_mfma_f32_32x32x16_bf16 v[144:159], a[144:147], v[112:115], v[144:159]\n"
    "ds_read_b128 a[144:147], v6 offset:24576\n"
    "v_exp_f32 v34, v170\n"
    "v_add_f32 v18, v18, v33\n"
    "v_cvt_pk_bf16_f32 v212, v32, v33\n"
    "v_mfma_f32_32x32x16_bf16 v[128:143], a[148:151], v[84:87], v[128:143]\n"
    "buffer_load_dwordx4 v15, s[44:47], s68 offen offset:2048 lds\n"
    "v_exp_f32 v35, v171\n"
    "v_add_f32 v18, v18, v34\n"
    "v_exp_f32 v36, v172\n"
    "v_mfma_f32_32x32x16_bf16 v[144:159], a[148:151], v[116:119], v[144:159]\n"
    "ds_read_b128 a[148:151], v7 offset:24576\n"
    "v_add_f32 v18, v18, v35\n"
    "v_cvt_pk_bf16_f32 v213, v34, v35\n"
    "v_exp_f32 v37, v173\n"
    "v_mfma_f32_32x32x16_bf16 v[128:143], a[152:155], v[88:91], v[128:143]\n"
    "v_add_f32 v18, v18, v36\n"
    "v_exp_f32 v38, v174\n"
    "v_add_f32 v18, v18, v37\n"
    "v_mfma_f32_32x32x16_bf16 v[144:159], a[152:155], v[120:123], v[144:159]\n"
    "ds_read_b128 a[152:155], v8 offset:24576\n"
    "v_cvt_pk_bf16_f32 v214, v36, v37\n"
    "v_exp_f32 v39, v175\n"
    "v_add_f32 v18, v18, v38\n"
    "s_waitcnt lgkmcnt(15)\n"
    "v_mfma_f32_32x32x16_bf16 v[128:143], a[156:159], v[92:95], v[128:143]\n"
    "buffer_load_dwordx4 v15, s[44:47], s68 offen offset:3072 lds\n"
    "v_exp_f32 v32, v176\n"
    "v_add_f32 v18, v18, v39\n"
    "v_cvt_pk_bf16_f32 v215, v38, v39\n"
    "v_mfma_f32_32x32x16_bf16 v[144:159], a[156:159], v[124:127], v[144:159]\n"
    "ds_read_b128 a[156:159], v9 offset:24576\n"
    "v_exp_f32 v33, v177\n"
    "s_add_u32 s68, s68, 0x4000\n"
    "v_mfma_f32_32x32x16_bf16 a[0:15], a[160:163], v[192:195], a[0:15]\n"
    "v_exp_f32 v34, v178\n"
    "v_add_f32 v19, v32, v33\n"
    "v_cvt_pk_bf16_f32 v216, v32, v33\n"
    "v_mfma_f32_32x32x16_bf16 a[16:31], a[160:163], v[200:203], a[16:31]\n"
    "ds_read_b64_tr_b16 a[160:161], v10 offset:8192\n"
    "ds_read_b64_tr_b16 a[162:163], v10 offset:10240\n"
    "v_exp_f32 v35, v179\n"
    "v_add_f32 v19, v19, v34\n"
    "v_mfma_f32_32x32x16_bf16 a[32:47], a[164:167], v[192:195], a[32:47]\n"
    "v_exp_f32 v36, v180\n"
    "v_add_f32 v19, v19, v35\n"
    "v_cvt_pk_bf16_f32 v217, v34, v35\n"
    "v_exp_f32 v37, v181\n"
    "v_mfma_f32_32x32x16_bf16 a[48:63], a[164:167], v[200:203], a[48:63]\n"
    "ds_read_b64_tr_b16 a[164:165], v10 offset:8448\n"
    "ds_read_b64_tr_b16 a[166:167], v10 offset:10496\n"
    "v_add_f32 v19, v19, v36\n"
    "v_exp_f32 v38, v182\n"
    "v_mfma_f32_32x32x16_bf16 a[64:79], a[168:171], v[192:195], a[64:79]\n"
    "v_add_f32 v19, v19, v37\n"
    "v_cvt_pk_bf16_f32 v218, v36, v37\n"
    "v_exp_f32 v39, v183\n"
    "v_mfma_f32_32x32x16_bf16 a[80:95], a[168:171], v[200:203], a[80:95]\n"
    "ds_read_b64_tr_b16 a[168:169], v10 offset:8704\n"
    "ds_read_b64_tr_b16 a[170:171], v10 offset:10752\n"
    "v_add_f32 v19, v19, v38\n"
    "v_exp_f32 v32, v184\n"
    "v_mfma_f32_32x32x16_bf16 a[96:111], a[172:175], v[192:195], a[96:111]\n"
    "v_add_f32 v19, v19, v39\n"
    "v_cvt_pk_bf16_f32 v219, v38, v39\n"
    "v_exp_f32 v33, v185\n"
    "v_add_f32 v19, v19, v32\n"
    "v_mfma_f32_32x32x16_bf16 a[112:127], a[172:175], v[200:203], a[112:127]\n"
    "ds_read_b64_tr_b16 a[172:173], v10 offset:8960\n"
    "ds_read_b64_tr_b16 a[174:175], v10 offset:11008\n"
    "v_exp_f32 v34, v186\n"
    "v_add_f32 v19, v19, v33\n"
    "s_waitcnt lgkmcnt(15)\n"
    "v_mfma_f32_32x32x16_bf16 a[0:15], a[176:179], v[196:199], a[0:15]\n"
    "v_cvt_pk_bf16_f32 v220, v32, v33\n"
    "v_exp_f32 v35, v187\n"
    "v_add_f32 v19, v19, v34\n"
    "v_mfma_f32_32x32x16_bf16 a[16:31], a[176:179], v[204:207], a[16:31]\n"
    "ds_read_b64_tr_b16 a[176:177], v10 offset:12288\n"
    "ds_read_b64_tr_b16 a[178:179], v10 offset:14336\n"
    "v_exp_f32 v36, v188\n"
    "v_add_f32 v19, v19, v35\n"
    "v_mfma_f32_32x32x16_bf16 a[32:47], a[180:183], v[196:199], a[32:47]\n"
    "v_cvt_pk_bf16_f32 v221, v34, v35\n"
    "v_exp_f32 v37, v189\n"
    "v_add_f32 v19, v19, v36\n"
    "v_mfma_f32_32x32x16_bf16 a[48:63], a[180:183], v[204:207], a[48:63]\n"
    "ds_read_b64_tr_b16 a[180:181], v10 offset:12544\n"
    "ds_read_b64_tr_b16 a[182:183], v10 offset:14592\n"
    "v_exp_f32 v38, v190\n"
    "v_add_f32 v19, v19, v37\n"
    "v_cvt_pk_bf16_f32 v222, v36, v37\n"
    "v_mfma_f32_32x32x16_bf16 a[64:79], a[184:187], v[196:199], a[64:79]\n"
    "v_exp_f32 v39, v191\n"
    "v_add_f32 v19, v19, v38\n"
    "v_add_f32 v19, v19, v39\n"
    "v_mfma_f32_32x32x16_bf16 a[80:95], a[184:187], v[204:207], a[80:95]\n"
    "ds_read_b64_tr_b16 a[184:185], v10 offset:12800\n"
    "ds_read_b64_tr_b16 a[186:187], v10 offset:14848\n"
    "v_cvt_pk_bf16_f32 v223, v38, v39\n"
    "v_max_f32 v20, v18, v19\n"
    "v_cmp_lt_f32 vcc, s72, v20\n"
    "v_mfma_f32_32x32x16_bf16 a[96:111], a[188:191], v[196:199], a[96:111]\n"
    "v_mfma_f32_32x32x16_bf16 a[112:127], a[188:191], v[204:207], a[112:127]\n"
    "ds_read_b64_tr_b16 a[188:189], v10 offset:13056\n"
    "ds_read_b64_tr_b16 a[190:191], v10 offset:15104\n"
    "s_cbranch_vccnz .Lfb_stub9\n"
    ".Lfb_ret9:\n"
    "s_sub_u32 s71, s71, 1\n"
    "s_cmp_lg_u32 s71, 0\n"
    "s_cbranch_scc1 .Lattn_loop\n"
    "v_lshlrev_b32 v31, 4, v1\n"
    "v_lshrrev_b32 v56, 5, v1\n"
    "v_lshlrev_b32 v56, 8, v56\n"
    "v_and_b32 v57, 31, v1\n"
    "v_lshl_add_u32 v56, v57, 2, v56\n"
    "v_add_u32 v56, 0x100, v56\n"
    "v_add_u32 v57, 0x200, v56\n"
    "v_add_u32 v58, 0x400, v56\n"
    "v_add_u32 v59, 0x600, v56\n"
    "global_load_dword v60, v56, s[86:87] sc1\n"
    "global_load_dword v61, v57, s[86:87] sc1\n"
    "global_load_dword v62, v58, s[86:87] sc1\n"
    "global_load_dword v63, v59, s[86:87] sc1\n"
    "v_mfma_f32_32x32x16_bf16 v[160:175], a[128:131], v[64:67], v[224:239]\n"
    "s_waitcnt vmcnt(12)\n"
    "s_barrier\n"
    "v_add_f32 v16, v16, v18\n"
    "v_add_f32 v17, v17, v19\n"
    "v_exp_f32 v32, v128\n"
    "v_mfma_f32_32x32x16_bf16 v[176:191], a[128:131], v[96:99], v[240:255]\n"
    "ds_read_b128 a[128:131], v2 offset:32768\n"
    "v_exp_f32 v33, v129\n"
    "v_exp_f32 v34, v130\n"
    "v_add_f32 v18, v32, v33\n"
    "s_waitcnt lgkmcnt(15)\n"
    "v_mfma_f32_32x32x16_bf16 v[160:175], a[132:135], v[68:71], v[160:175]\n"
    "s_and_b32 s74, s98, 7\n"
    "s_lshl_b32 s74, s74, 5\n"
    "s_lshr_b32 s75, s98, 3\n"
    "s_add_u32 s74, s74, s75\n"
    "s_mul_i32 s74, s74, 0x4000\n"
    "s_mov_b64 s[48:49], %4\n"
    "s_and_b32 s49, s49, 0xffff\n"
    "s_mov_b32 s50, 0x400000\n"
    "s_mov_b32 s51, 0x20000\n"
    "s_add_u32 m0, s69, 0x20000\n"
    "s_add_u32 s78, s74, s69\n"
    "v_mfma_f32_32x32x16_bf16 v[176:191], a[132:135], v[100:103], v[176:191]\n"
    "ds_read_b128 a[132:135], v3 offset:32768\n"
    "buffer_load_dwordx4 v31, s[48:51], s78 offen offset:0 lds\n"
    "v_cvt_pk_bf16_f32 v192, v32, v33\n"
    "v_exp_f32 v35, v131\n"
    "v_mfma_f32_32x32x16_bf16 v[160:175], a[136:139], v[72:75], v[160:175]\n"
    "v_add_f32 v18, v18, v34\n"
    "v_exp_f32 v36, v132\n"
    "v_add_f32 v18, v18, v35\n"
    "v_cvt_pk_bf16_f32 v193, v34, v35\n"
    "v_mfma_f32_32x32x16_bf16 v[176:191], a[136:139], v[104:107], v[176:191]\n"
    "ds_read_b128 a[136:139], v4 offset:32768\n"
    "buffer_load_dwordx4 v31, s[48:51], s78 offen offset:1024 lds\n"
    "v_exp_f32 v37, v133\n"
    "v_add_f32 v18, v18, v36\n"
    "v_mfma_f32_32x32x16_bf16 v[160:175], a[140:143], v[76:79], v[160:175]\n"
    "v_exp_f32 v38, v134\n"
    "v_add_f32 v18, v18, v37\n"
    "v_cvt_pk_bf16_f32 v194, v36, v37\n"
    "v_exp_f32 v39, v135\n"
    "v_mfma_f32_32x32x16_bf16 v[176:191], a[140:143], v[108:111], v[176:191]\n"
    "ds_read_b128 a[140:143], v5 offset:32768\n"
    "v_add_f32 v18, v18, v38\n"
    "v_exp_f32 v32, v136\n"
    "v_add_f32 v18, v18, v39\n"
    "v_cvt_pk_bf16_f32 v195, v38, v39\n"
    "v_mfma_f32_32x32x16_bf16 v[160:175], a[144:147], v[80:83], v[160:175]\n"
    "buffer_load_dwordx4 v31, s[48:51], s78 offen offset:2048 lds\n"
    "v_exp_f32 v33, v137\n"
    "v_add_f32 v18, v18, v32\n"
    "v_exp_f32 v34, v138\n"
    "v_mfma_f32_32x32x16_bf16 v[176:191], a[144:147], v[112:115], v[176:191]\n"
    "ds_read_b128 a[144:147], v6 offset:32768\n"
    "v_add_f32 v18, v18, v33\n"
    "v_cvt_pk_bf16_f32 v196, v32, v33\n"
    "v_exp_f32 v35, v139\n"
    "v_mfma_f32_32x32x16_bf16 v[160:175], a[148:151], v[84:87], v[160:175]\n"
    "v_add_f32 v18, v18, v34\n"
    "v_exp_f32 v36, v140\n"
    "v_add_f32 v18, v18, v35\n"
    "v_cvt_pk_bf16_f32 v197, v34, v35\n"
    "v_mfma_f32_32x32x16_bf16 v[176:191], a[148:151], v[116:119], v[176:191]\n"
    "ds_read_b128 a[148:151], v7 offset:32768\n"
    "buffer_load_dwordx4 v31, s[48:51], s78 offen offset:3072 lds\n"
    "v_exp_f32 v37, v141\n"
    "v_add_f32 v18, v18, v36\n"
    "v_mfma_f32_32x32x16_bf16 v[160:175], a[152:155], v[88:91], v[160:175]\n"
    "v_exp_f32 v38, v142\n"
    "v_add_f32 v18, v18, v37\n"
    "v_cvt_pk_bf16_f32 v198, v36, v37\n"
    "v_exp_f32 v39, v143\n"
    "v_mfma_f32_32x32x16_bf16 v[176:191], a[152:155], v[120:123], v[176:191]\n"
    "ds_read_b128 a[152:155], v8 offset:32768\n"
    "s_and_b32 s74, s98, 7\n"
    "s_lshl_b32 s74, s74, 5\n"
    "s_lshr_b32 s75, s98, 3\n"
    "s_add_u32 s74, s74, s75\n"
    "s_mul_i32 s74, s74, 0x1c00\n"
    "s_mov_b64 s[48:49], %5\n"
    "s_and_b32 s49, s49, 0xffff\n"
    "s_mov_b32 s50, 0x1c0000\n"
    "s_mov_b32 s51, 0x20000\n"
    "s_add_u32 m0, s69, 0x20000\n"
    "s_lshl_b32 s75, s64, 11\n"
    "s_add_u32 s78, s74, s75\n"
    "v_mfma_f32_32x32x16_bf16 v[160:175], a[156:159], v[92:95], v[160:175]\n"
    "buffer_load_dwordx4 v31, s[48:51], s78 offen offset:0 lds\n"
    "v_add_f32 v18, v18, v38\n"
    "v_exp_f32 v32, v144\n"
    "v_add_f32 v18, v18, v39\n"
    "v_cvt_pk_bf16_f32 v199, v38, v39\n"
    "v_mfma_f32_32x32x16_bf16 v[176:191], a[156:159], v[124:127], v[176:191]\n"
    "ds_read_b128 a[156:159], v9 offset:32768\n"
    "v_exp_f32 v33, v145\n"
    "v_exp_f32 v34, v146\n"
    "v_add_f32 v19, v32, v33\n"
    "v_mfma_f32_32x32x16_bf16 a[0:15], a[160:163], v[208:211], a[0:15]\n"
    "v_cvt_pk_bf16_f32 v200, v32, v33\n"
    "v_exp_f32 v35, v147\n"
    "v_add_f32 v19, v19, v34\n"
    "v_mfma_f32_32x32x16_bf16 a[16:31], a[160:163], v[216:219], a[16:31]\n"
    "ds_read_b64_tr_b16 a[160:161], v10 offset:16384\n"
    "ds_read_b64_tr_b16 a[162:163], v10 offset:18432\n"
    "buffer_load_dwordx4 v31, s[48:51], s78 offen offset:1024 lds\n"
    "v_exp_f32 v36, v148\n"
    "v_add_f32 v19, v19, v35\n"
    "s_waitcnt lgkmcnt(15)\n"
    "v_mfma_f32_32x32x16_bf16 a[32:47], a[164:167], v[208:211], a[32:47]\n"
    "v_cvt_pk_bf16_f32 v201, v34, v35\n"
    "v_exp_f32 v37, v149\n"
    "v_add_f32 v19, v19, v36\n"
    "v_exp_f32 v38, v150\n"
    "v_mfma_f32_32x32x16_bf16 a[48:63], a[164:167], v[216:219], a[48:63]\n"
    "ds_read_b64_tr_b16 a[164:165], v10 offset:16640\n"
    "ds_read_b64_tr_b16 a[166:167], v10 offset:18688\n"
    "s_and_b32 s74, s98, 7\n"
    "s_lshl_b32 s74, s74, 5\n"
    "s_lshr_b32 s75, s98, 3\n"
    "s_add_u32 s74, s74, s75\n"
    "s_mul_i32 s74, s74, 0x1c00\n"
    "s_mov_b64 s[48:49], %6\n"
    "s_and_b32 s49, s49, 0xffff\n"
    "s_mov_b32 s50, 0x1c0000\n"
    "s_mov_b32 s51, 0x20000\n"
    "s_add_u32 m0, s69, 0x20000\n"
    "s_lshl_b32 s75, s64, 11\n"
    "s_add_u32 s78, s74, s75\n"
    "v_mfma_f32_32x32x16_bf16 a[64:79], a[168:171], v[208:211], a[64:79]\n"
    "buffer_load_dwordx4 v31, s[48:51], s78 offen offset:0 lds\n"
    "v_add_f32 v19, v19, v37\n"
    "v_cvt_pk_bf16_f32 v202, v36, v37\n"
    "v_exp_f32 v39, v151\n"
    "v_add_f32 v19, v19, v38\n"
    "v_mfma_f32_32x32x16_bf16 a[80:95], a[168:171], v[216:219], a[80:95]\n"
    "ds_read_b64_tr_b16 a[168:169], v10 offset:16896\n"
    "ds_read_b64_tr_b16 a[170:171], v10 offset:18944\n"
    "v_exp_f32 v32, v152\n"
    "v_add_f32 v19, v19, v39\n"
    "v_mfma_f32_32x32x16_bf16 a[96:111], a[172:175], v[208:211], a[96:111]\n"
    "v_cvt_pk_bf16_f32 v203, v38, v39\n"
    "v_exp_f32 v33, v153\n"
    "v_add_f32 v19, v19, v32\n"
    "v_exp_f32 v34, v154\n"
    "v_mfma_f32_32x32x16_bf16 a[112:127], a[172:175], v[216:219], a[112:127]\n"
    "ds_read_b64_tr_b16 a[172:173], v10 offset:17152\n"
    "ds_read_b64_tr_b16 a[174:175], v10 offset:19200\n"
    "buffer_load_dwordx4 v31, s[48:51], s78 offen offset:1024 lds\n"
    "v_add_f32 v19, v19, v33\n"
    "v_cvt_pk_bf16_f32 v204, v32, v33\n"
    "v_mfma_f32_32x32x16_bf16 a[0:15], a[176:179], v[212:215], a[0:15]\n"
    "v_exp_f32 v35, v155\n"
    "v_add_f32 v19, v19, v34\n"
    "v_exp_f32 v36, v156\n"
    "v_add_f32 v19, v19, v35\n"
    "v_mfma_f32_32x32x16_bf16 a[16:31], a[176:179], v[220:223], a[16:31]\n"
    "ds_read_b64_tr_b16 a[176:177], v10 offset:20480\n"
    "ds_read_b64_tr_b16 a[178:179], v10 offset:22528\n"
    "s_and_b32 s74, s98, 7\n"
    "s_lshl_b32 s74, s74, 5\n"
    "s_lshr_b32 s75, s98, 3\n"
    "s_add_u32 s74, s74, s75\n"
    "s_mul_i32 s74, s74, 0x1c00\n"
    "s_mov_b64 s[48:49], %7\n"
    "s_and_b32 s49, s49, 0xffff\n"
    "s_mov_b32 s50, 0x1c0000\n"
    "s_mov_b32 s51, 0x20000\n"
    "s_add_u32 m0, s69, 0x20000\n"
    "s_lshl_b32 s75, s64, 11\n"
    "s_add_u32 s78, s74, s75\n"
    "s_waitcnt lgkmcnt(15)\n"
    "v_mfma_f32_32x32x16_bf16 a[32:47], a[180:183], v[212:215], a[32:47]\n"
    "buffer_load_dwordx4 v31, s[48:51], s78 offen offset:0 lds\n"
    "v_cvt_pk_bf16_f32 v205, v34, v35\n"
    "v_exp_f32 v37, v157\n"
    "v_add_f32 v19, v19, v36\n"
    "v_mfma_f32_32x32x16_bf16 a[48:63], a[180:183], v[220:223], a[48:63]\n"
    "ds_read_b64_tr_b16 a[180:181], v10 offset:20736\n"
    "ds_read_b64_tr_b16 a[182:183], v10 offset:22784\n"
    "s_waitcnt vmcnt(12)\n"
    "v_xor_b32 v60, s88, v60\n"
    "s_waitcnt vmcnt(11)\n"
    "v_xor_b32 v61, s88, v61\n"
    "s_waitcnt vmcnt(10)\n"
    "v_xor_b32 v62, s88, v62\n"
    "s_waitcnt vmcnt(9)\n"
    "v_xor_b32 v63, s88, v63\n"
    "v_or_b32 v60, v60, v61\n"
    "v_or_b32 v62, v62, v63\n"
    "v_or_b32 v60, v60, v62\n"
    "v_cmp_ne_u32 vcc, 0, v60\n"
    "v_mfma_f32_32x32x16_bf16 a[64:79], a[184:187], v[212:215], a[64:79]\n"
    "v_exp_f32 v38, v158\n"
    "v_add_f32 v19, v19, v37\n"
    "v_cvt_pk_bf16_f32 v206, v36, v37\n"
    "v_exp_f32 v39, v159\n"
    "v_mfma_f32_32x32x16_bf16 a[80:95], a[184:187], v[220:223], a[80:95]\n"
    "ds_read_b64_tr_b16 a[184:185], v10 offset:20992\n"
    "ds_read_b64_tr_b16 a[186:187], v10 offset:23040\n"
    "buffer_load_dwordx4 v31, s[48:51], s78 offen offset:1024 lds\n"
    "v_add_f32 v19, v19, v38\n"
    "v_add_f32 v19, v19, v39\n"
    "v_mfma_f32_32x32x16_bf16 a[96:111], a[188:191], v[212:215], a[96:111]\n"
    "v_cvt_pk_bf16_f32 v207, v38, v39\n"
    "v_max_f32 v20, v18, v19\n"
    "v_cmp_lt_f32 vcc, s72, v20\n"
    "s_cbranch_vccnz .Lci6\n"
    ".Lci6r:\n"
    "v_mfma_f32_32x32x16_bf16 a[112:127], a[188:191], v[220:223], a[112:127]\n"
    "ds_read_b64_tr_b16 a[188:189], v10 offset:21248\n"
    "ds_read_b64_tr_b16 a[190:191], v10 offset:23296\n"
    "s_cbranch_vccnz .Lfb_stub10\n"
    ".Lfb_ret10:\n"
    "s_mov_b64 s[48:49], %4\n"
    "s_lshr_b32 s73, s79, 2\n"
    "s_lshl_b32 s73, s73, 20\n"
    "s_add_u32 s48, s48, s73\n"
    "s_addc_u32 s49, s49, 0\n"
    "s_and_b32 s73, s79, 3\n"
    "s_lshl_b32 s73, s73, 8\n"
    "s_add_u32 s48, s48, s73\n"
    "s_addc_u32 s49, s49, 0\n"
    "s_and_b32 s49, s49, 0xffff\n"
    "s_mov_b32 s50, 0xffd00\n"
    "s_mov_b32 s51, 0x20000\n"
    "v_bfe_u32 v22, v1, 2, 2\n"
    "v_lshlrev_b32 v22, 10, v22\n"
    "v_lshrrev_b32 v23, 4, v1\n"
    "v_lshl_add_u32 v22, v23, 6, v22\n"
    "v_and_b32 v23, 3, v1\n"
    "v_lshl_add_u32 v26, v23, 4, v22\n"
    "v_add_u32 v27, 0xc00, v26\n"
    "v_add_u32 v28, 0x1800, v26\n"
    "v_add_u32 v29, 0x2400, v26\n"
    "v_mfma_f32_32x32x16_bf16 v[128:143], a[128:131], v[64:67], v[224:239]\n"
    "v_add_f32 v16, v16, v18\n"
    "v_add_f32 v17, v17, v19\n"
    "v_exp_f32 v32, v160\n"
    "v_mfma_f32_32x32x16_bf16 v[144:159], a[128:131], v[96:99], v[240:255]\n"
    "ds_read_b128 a[128:131], v2 offset:40960\n"
    "v_exp_f32 v33, v161\n"
    "v_exp_f32 v34, v162\n"
    "s_add_u32 m0, s69, 0x1c000\n"
    "v_mfma_f32_32x32x16_bf16 v[128:143], a[132:135], v[68:71], v[128:143]\n"
    "buffer_load_dwordx4 v15, s[44:47], s68 offen offset:0 lds\n"
    "v_add_f32 v18, v32, v33\n"
    "v_cvt_pk_bf16_f32 v208, v32, v33\n"
    "v_exp_f32 v35, v163\n"
    "v_mfma_f32_32x32x16_bf16 v[144:159], a[132:135], v[100:103], v[144:159]\n"
    "ds_read_b128 a[132:135], v3 offset:40960\n"
    "v_add_f32 v18, v18, v34\n"
    "v_exp_f32 v36, v164\n"
    "v_add_f32 v18, v18, v35\n"
    "v_mfma_f32_32x32x16_bf16 v[128:143], a[136:139], v[72:75], v[128:143]\n"
    "v_cvt_pk_bf16_f32 v209, v34, v35\n"
    "v_exp_f32 v37, v165\n"
    "v_add_f32 v18, v18, v36\n"
    "v_mfma_f32_32x32x16_bf16 v[144:159], a[136:139], v[104:107], v[144:159]\n"
    "ds_read_b128 a[136:139], v4 offset:40960\n"
    "v_exp_f32 v38, v166\n"
    "v_add_f32 v18, v18, v37\n"
    "v_cvt_pk_bf16_f32 v210, v36, v37\n"
    "s_waitcnt lgkmcnt(15)\n"
    "v_mfma_f32_32x32x16_bf16 v[128:143], a[140:143], v[76:79], v[128:143]\n"
    "buffer_load_dwordx4 v15, s[44:47], s68 offen offset:1024 lds\n"
    "v_exp_f32 v39, v167\n"
    "v_add_f32 v18, v18, v38\n"
    "v_exp_f32 v32, v168\n"
    "v_mfma_f32_32x32x16_bf16 v[144:159], a[140:143], v[108:111], v[144:159]\n"
    "ds_read_b128 a[140:143], v5 offset:40960\n"
    "v_add_f32 v18, v18, v39\n"
    "v_cvt_pk_bf16_f32 v211, v38, v39\n"
    "v_exp_f32 v33, v169\n"
    "v_mfma_f32_32x32x16_bf16 v[128:143], a[144:147], v[80:83], v[128:143]\n"
    "v_add_f32 v18, v18, v32\n"
    "v_exp_f32 v34, v170\n"
    "v_add_f32 v18, v18, v33\n"
    "v_cvt_pk_bf16_f32 v212, v32, v33\n"
    "v_mfma_f32_32x32x16_bf16 v[144:159], a[144:147], v[112:115], v[144:159]\n"
    "ds_read_b128 a[144:147], v6 offset:40960\n"
    "v_exp_f32 v35, v171\n"
    "v_add_f32 v18, v18, v34\n"
    "v_mfma_f32_32x32x16_bf16 v[128:143], a[148:151], v[84:87], v[128:143]\n"
    "buffer_load_dwordx4 v15, s[44:47], s68 offen offset:2048 lds\n"
    "v_exp_f32 v36, v172\n"
    "v_add_f32 v18, v18, v35\n"
    "v_cvt_pk_bf16_f32 v213, v34, v35\n"
    "v_mfma_f32_32x32x16_bf16 v[144:159], a[148:151], v[116:119], v[144:159]\n"
    "ds_read_b128 a[148:151], v7 offset:40960\n"
    "v_exp_f32 v37, v173\n"
    "v_add_f32 v18, v18, v36\n"
    "v_exp_f32 v38, v174\n"
    "v_mfma_f32_32x32x16_bf16 v[128:143], a[152:155], v[88:91], v[128:143]\n"
    "v_add_f32 v18, v18, v37\n"
    "v_cvt_pk_bf16_f32 v214, v36, v37\n"
    "v_exp_f32 v39, v175\n"
    "v_add_f32 v18, v18, v38\n"
    "v_mfma_f32_32x32x16_bf16 v[144:159], a[152:155], v[120:123], v[144:159]\n"
    "ds_read_b128 a[152:155], v8 offset:40960\n"
    "v_exp_f32 v32, v176\n"
    "v_add_f32 v18, v18, v39\n"
    "v_cvt_pk_bf16_f32 v215, v38, v39\n"
    "v_mfma_f32_32x32x16_bf16 v[128:143], a[156:159], v[92:95], v[128:143]\n"
    "buffer_load_dwordx4 v15, s[44:47], s68 offen offset:3072 lds\n"
    "v_exp_f32 v33, v177\n"
    "v_exp_f32 v34, v178\n"
    "v_mfma_f32_32x32x16_bf16 v[144:159], a[156:159], v[124:127], v[144:159]\n"
    "ds_read_b128 a[156:159], v9 offset:40960\n"
    "v_add_f32 v19, v32, v33\n"
    "v_cvt_pk_bf16_f32 v216, v32, v33\n"
    "s_add_u32 s68, s68, 0x4000\n"
    "v_mfma_f32_32x32x16_bf16 a[0:15], a[160:163], v[192:195], a[0:15]\n"
    "v_exp_f32 v35, v179\n"
    "v_add_f32 v19, v19, v34\n"
    "s_add_u32 m0, s69, 0\n"
    "s_lshl_b32 s78, s64, 14\n"
    "s_add_u32 s78, s78, 0x20000\n"
    "v_mfma_f32_32x32x16_bf16 a[16:31], a[160:163], v[200:203], a[16:31]\n"
    "ds_read_b64_tr_b16 a[160:161], v10 offset:24576\n"
    "ds_read_b64_tr_b16 a[162:163], v10 offset:26624\n"
    "buffer_load_dwordx4 v26, s[48:51], s78 offen offset:0 lds\n"
    "v_exp_f32 v36, v180\n"
    "v_mfma_f32_32x32x16_bf16 a[32:47], a[164:167], v[192:195], a[32:47]\n"
    "v_add_f32 v19, v19, v35\n"
    "v_cvt_pk_bf16_f32 v217, v34, v35\n"
    "v_exp_f32 v37, v181\n"
    "v_add_f32 v19, v19, v36\n"
    "v_mfma_f32_32x32x16_bf16 a[48:63], a[164:167], v[200:203], a[48:63]\n"
    "ds_read_b64_tr_b16 a[164:165], v10 offset:24832\n"
    "ds_read_b64_tr_b16 a[166:167], v10 offset:26880\n"
    "v_exp_f32 v38, v182\n"
    "v_add_f32 v19, v19, v37\n"
    "s_waitcnt lgkmcnt(15)\n"
    "v_mfma_f32_32x32x16_bf16 a[64:79], a[168:171], v[192:195], a[64:79]\n"
    "buffer_load_dwordx4 v27, s[48:51], s78 offen offset:1024 lds\n"
    "v_cvt_pk_bf16_f32 v218, v36, v37\n"
    "v_exp_f32 v39, v183\n"
    "v_add_f32 v19, v19, v38\n"
    "v_mfma_f32_32x32x16_bf16 a[80:95], a[168:171], v[200:203], a[80:95]\n"
    "ds_read_b64_tr_b16 a[168:169], v10 offset:25088\n"
    "ds_read_b64_tr_b16 a[170:171], v10 offset:27136\n"
    "v_exp_f32 v32, v184\n"
    "v_add_f32 v19, v19, v39\n"
    "v_mfma_f32_32x32x16_bf16 a[96:111], a[172:175], v[192:195], a[96:111]\n"
    "v_cvt_pk_bf16_f32 v219, v38, v39\n"
    "v_exp_f32 v33, v185\n"
    "v_add_f32 v19, v19, v32\n"
    "v_mfma_f32_32x32x16_bf16 a[112:127], a[172:175], v[200:203], a[112:127]\n"
    "ds_read_b64_tr_b16 a[172:173], v10 offset:25344\n"
    "ds_read_b64_tr_b16 a[174:175], v10 offset:27392\n"
    "buffer_load_dwordx4 v28, s[48:51], s78 offen offset:2048 lds\n"
    "v_exp_f32 v34, v186\n"
    "v_add_f32 v19, v19, v33\n"
    "v_mfma_f32_32x32x16_bf16 a[0:15], a[176:179], v[196:199], a[0:15]\n"
    "v_cvt_pk_bf16_f32 v220, v32, v33\n"
    "v_exp_f32 v35, v187\n"
    "v_add_f32 v19, v19, v34\n"
    "v_mfma_f32_32x32x16_bf16 a[16:31], a[176:179], v[204:207], a[16:31]\n"
    "ds_read_b64_tr_b16 a[176:177], v10 offset:28672\n"
    "ds_read_b64_tr_b16 a[178:179], v10 offset:30720\n"
    "v_exp_f32 v36, v188\n"
    "v_add_f32 v19, v19, v35\n"
    "v_cvt_pk_bf16_f32 v221, v34, v35\n"
    "v_mfma_f32_32x32x16_bf16 a[32:47], a[180:183], v[196:199], a[32:47]\n"
    "buffer_load_dwordx4 v29, s[48:51], s78 offen offset:3072 lds\n"
    "v_exp_f32 v37, v189\n"
    "v_add_f32 v19, v19, v36\n"
    "v_mfma_f32_32x32x16_bf16 a[48:63], a[180:183], v[204:207], a[48:63]\n"
    "ds_read_b64_tr_b16 a[180:181], v10 offset:28928\n"
    "ds_read_b64_tr_b16 a[182:183], v10 offset:30976\n"
    "v_exp_f32 v38, v190\n"
    "v_add_f32 v19, v19, v37\n"
    "v_cvt_pk_bf16_f32 v222, v36, v37\n"
    "s_waitcnt lgkmcnt(15)\n"
    "v_mfma_f32_32x32x16_bf16 a[64:79], a[184:187], v[196:199], a[64:79]\n"
    "v_exp_f32 v39, v191\n"
    "v_add_f32 v19, v19, v38\n"
    "v_add_f32 v19, v19, v39\n"
    "v_cvt_pk_bf16_f32 v223, v38, v39\n"
    "v_mfma_f32_32x32x16_bf16 a[80:95], a[184:187], v[204:207], a[80:95]\n"
    "ds_read_b64_tr_b16 a[184:185], v10 offset:29184\n"
    "ds_read_b64_tr_b16 a[186:187], v10 offset:31232\n"
    "v_max_f32 v20, v18, v19\n"
    "v_cmp_lt_f32 vcc, s72, v20\n"
    "v_mfma_f32_32x32x16_bf16 a[96:111], a[188:191], v[196:199], a[96:111]\n"
    "v_mfma_f32_32x32x16_bf16 a[112:127], a[188:191], v[204:207], a[112:127]\n"
    "ds_read_b64_tr_b16 a[188:189], v10 offset:29440\n"
    "ds_read_b64_tr_b16 a[190:191], v10 offset:31488\n"
    "s_cbranch_vccnz .Lfb_stub11\n"
    ".Lfb_ret11:\n"
    "v_mfma_f32_32x32x16_bf16 v[160:175], a[128:131], v[64:67], v[224:239]\n"
    "s_barrier\n"
    "v_add_f32 v16, v16, v18\n"
    "v_add_f32 v17, v17, v19\n"
    "v_exp_f32 v32, v128\n"
    "v_mfma_f32_32x32x16_bf16 v[176:191], a[128:131], v[96:99], v[240:255]\n"
    "ds_read_b128 a[128:131], v2 offset:49152\n"
    "v_exp_f32 v33, v129\n"
    "v_exp_f32 v34, v130\n"
    "v_mfma_f32_32x32x16_bf16 v[160:175], a[132:135], v[68:71], v[160:175]\n"
    "v_add_f32 v18, v32, v33\n"
    "v_cvt_pk_bf16_f32 v192, v32, v33\n"
    "s_add_u32 m0, s69, 0x20000\n"
    "s_lshl_b32 s78, s64, 14\n"
    "s_add_u32 s78, s78, 0\n"
    "v_mfma_f32_32x32x16_bf16 v[176:191], a[132:135], v[100:103], v[176:191]\n"
    "ds_read_b128 a[132:135], v3 offset:49152\n"
    "buffer_load_dwordx4 v26, s[48:51], s78 offen offset:0 lds\n"
    "v_exp_f32 v35, v131\n"
    "v_add_f32 v18, v18, v34\n"
    "v_mfma_f32_32x32x16_bf16 v[160:175], a[136:139], v[72:75], v[160:175]\n"
    "v_exp_f32 v36, v132\n"
    "v_add_f32 v18, v18, v35\n"
    "v_cvt_pk_bf16_f32 v193, v34, v35\n"
    "v_mfma_f32_32x32x16_bf16 v[176:191], a[136:139], v[104:107], v[176:191]\n"
    "ds_read_b128 a[136:139], v4 offset:49152\n"
    "v_exp_f32 v37, v133\n"
    "v_add_f32 v18, v18, v36\n"
    "v_exp_f32 v38, v134\n"
    "v_mfma_f32_32x32x16_bf16 v[160:175], a[140:143], v[76:79], v[160:175]\n"
    "buffer_load_dwordx4 v27, s[48:51], s78 offen offset:1024 lds\n"
    "v_add_f32 v18, v18, v37\n"
    "v_cvt_pk_bf16_f32 v194, v36, v37\n"
    "v_exp_f32 v39, v135\n"
    "v_mfma_f32_32x32x16_bf16 v[176:191], a[140:143], v[108:111], v[176:191]\n"
    "ds_read_b128 a[140:143], v5 offset:49152\n"
    "v_add_f32 v18, v18, v38\n"
    "v_exp_f32 v32, v136\n"
    "v_add_f32 v18, v18, v39\n"
    "v_mfma_f32_32x32x16_bf16 v[160:175], a[144:147], v[80:83], v[160:175]\n"
    "v_cvt_pk_bf16_f32 v195, v38, v39\n"
    "v_exp_f32 v33, v137\n"
    "v_add_f32 v18, v18, v32\n"
    "v_mfma_f32_32x32x16_bf16 v[176:191], a[144:147], v[112:115], v[176:191]\n"
    "ds_read_b128 a[144:147], v6 offset:49152\n"
    "buffer_load_dwordx4 v28, s[48:51], s78 offen offset:2048 lds\n"
    "v_exp_f32 v34, v138\n"
    "v_add_f32 v18, v18, v33\n"
    "v_cvt_pk_bf16_f32 v196, v32, v33\n"
    "s_waitcnt lgkmcnt(15)\n"
    "v_mfma_f32_32x32x16_bf16 v[160:175], a[148:151], v[84:87], v[160:175]\n"
    "v_exp_f32 v35, v139\n"
    "v_add_f32 v18, v18, v34\n"
    "v_exp_f32 v36, v140\n"
    "v_mfma_f32_32x32x16_bf16 v[176:191], a[148:151], v[116:119], v[176:191]\n"
    "ds_read_b128 a[148:151], v7 offset:49152\n"
    "v_add_f32 v18, v18, v35\n"
    "v_cvt_pk_bf16_f32 v197, v34, v35\n"
    "v_exp_f32 v37, v141\n"
    "v_mfma_f32_32x32x16_bf16 v[160:175], a[152:155], v[88:91], v[160:175]\n"
    "buffer_load_dwordx4 v29, s[48:51], s78 offen offset:3072 lds\n"
    "v_add_f32 v18, v18, v36\n"
    "v_exp_f32 v38, v142\n"
    "v_add_f32 v18, v18, v37\n"
    "v_mfma_f32_32x32x16_bf16 v[176:191], a[152:155], v[120:123], v[176:191]\n"
    "ds_read_b128 a[152:155], v8 offset:49152\n"
    "v_cvt_pk_bf16_f32 v198, v36, v37\n"
    "v_exp_f32 v39, v143\n"
    "v_add_f32 v18, v18, v38\n"
    "v_mfma_f32_32x32x16_bf16 v[160:175], a[156:159], v[92:95], v[160:175]\n"
    "v_exp_f32 v32, v144\n"
    "v_add_f32 v18, v18, v39\n"
    "v_cvt_pk_bf16_f32 v199, v38, v39\n"
    "v_mfma_f32_32x32x16_bf16 v[176:191], a[156:159], v[124:127], v[176:191]\n"
    "ds_read_b128 a[156:159], v9 offset:49152\n"
    "v_exp_f32 v33, v145\n"
    "v_exp_f32 v34, v146\n"
    "v_add_f32 v19, v32, v33\n"
    "v_mfma_f32_32x32x16_bf16 a[0:15], a[160:163], v[208:211], a[0:15]\n"
    "v_cvt_pk_bf16_f32 v200, v32, v33\n"
    "s_add_u32 m0, s69, 0x24000\n"
    "s_lshl_b32 s78, s64, 14\n"
    "s_add_u32 s78, s78, 0x10000\n"
    "v_mfma_f32_32x32x16_bf16 a[16:31], a[160:163], v[216:219], a[16:31]\n"
    "ds_read_b64_tr_b16 a[160:161], v10 offset:32768\n"
    "ds_read_b64_tr_b16 a[162:163], v10 offset:34816\n"
    "buffer_load_dwordx4 v26, s[48:51], s78 offen offset:0 lds\n"
    "v_exp_f32 v35, v147\n"
    "v_add_f32 v19, v19, v34\n"
    "v_mfma_f32_32x32x16_bf16 a[32:47], a[164:167], v[208:211], a[32:47]\n"
    "v_exp_f32 v36, v148\n"
    "v_add_f32 v19, v19, v35\n"
    "v_cvt_pk_bf16_f32 v201, v34, v35\n"
    "v_mfma_f32_32x32x16_bf16 a[48:63], a[164:167], v[216:219], a[48:63]\n"
    "ds_read_b64_tr_b16 a[164:165], v10 offset:33024\n"
    "ds_read_b64_tr_b16 a[166:167], v10 offset:35072\n"
    "v_exp_f32 v37, v149\n"
    "v_add_f32 v19, v19, v36\n"
    "v_mfma_f32_32x32x16_bf16 a[64:79], a[168:171], v[208:211], a[64:79]\n"
    "buffer_load_dwordx4 v27, s[48:51], s78 offen offset:1024 lds\n"
    "v_exp_f32 v38, v150\n"
    "v_add_f32 v19, v19, v37\n"
    "v_cvt_pk_bf16_f32 v202, v36, v37\n"
    "v_mfma_f32_32x32x16_bf16 a[80:95], a[168:171], v[216:219], a[80:95]\n"
    "ds_read_b64_tr_b16 a[168:169], v10 offset:33280\n"
    "ds_read_b64_tr_b16 a[170:171], v10 offset:35328\n"
    "v_exp_f32 v39, v151\n"
    "v_add_f32 v19, v19, v38\n"
    "s_waitcnt lgkmcnt(15)\n"
    "v_mfma_f32_32x32x16_bf16 a[96:111], a[172:175], v[208:211], a[96:111]\n"
    "v_exp_f32 v32, v152\n"
    "v_add_f32 v19, v19, v39\n"
    "v_cvt_pk_bf16_f32 v203, v38, v39\n"
    "v_exp_f32 v33, v153\n"
    "v_mfma_f32_32x32x16_bf16 a[112:127], a[172:175], v[216:219], a[112:127]\n"
    "ds_read_b64_tr_b16 a[172:173], v10 offset:33536\n"
    "ds_read_b64_tr_b16 a[174:175], v10 offset:35584\n"
    "buffer_load_dwordx4 v28, s[48:51], s78 offen offset:2048 lds\n"
    "v_add_f32 v19, v19, v32\n"
    "v_mfma_f32_32x32x16_bf16 a[0:15], a[176:179], v[212:215], a[0:15]\n"
    "v_exp_f32 v34, v154\n"
    "v_add_f32 v19, v19, v33\n"
    "v_cvt_pk_bf16_f32 v204, v32, v33\n"
    "v_exp_f32 v35, v155\n"
    "v_mfma_f32_32x32x16_bf16 a[16:31], a[176:179], v[220:223], a[16:31]\n"
    "ds_read_b64_tr_b16 a[176:177], v10 offset:36864\n"
    "ds_read_b64_tr_b16 a[178:179], v10 offset:38912\n"
    "v_add_f32 v19, v19, v34\n"
    "v_exp_f32 v36, v156\n"
    "v_mfma_f32_32x32x16_bf16 a[32:47], a[180:183], v[212:215], a[32:47]\n"
    "buffer_load_dwordx4 v29, s[48:51], s78 offen offset:3072 lds\n"
    "v_add_f32 v19, v19, v35\n"
    "v_cvt_pk_bf16_f32 v205, v34, v35\n"
    "v_exp_f32 v37, v157\n"
    "v_mfma_f32_32x32x16_bf16 a[48:63], a[180:183], v[220:223], a[48:63]\n"
    "ds_read_b64_tr_b16 a[180:181], v10 offset:37120\n"
    "ds_read_b64_tr_b16 a[182:183], v10 offset:39168\n"
    "v_add_f32 v19, v19, v36\n"
    "v_exp_f32 v38, v158\n"
    "v_mfma_f32_32x32x16_bf16 a[64:79], a[184:187], v[212:215], a[64:79]\n"
    "v_add_f32 v19, v19, v37\n"
    "v_cvt_pk_bf16_f32 v206, v36, v37\n"
    "v_exp_f32 v39, v159\n"
    "v_add_f32 v19, v19, v38\n"
    "v_mfma_f32_32x32x16_bf16 a[80:95], a[184:187], v[220:223], a[80:95]\n"
    "ds_read_b64_tr_b16 a[184:185], v10 offset:37376\n"
    "ds_read_b64_tr_b16 a[186:187], v10 offset:39424\n"
    "v_add_f32 v19, v19, v39\n"
    "v_cvt_pk_bf16_f32 v207, v38, v39\n"
    "v_max_f32 v20, v18, v19\n"
    "s_waitcnt lgkmcnt(15)\n"
    "v_mfma_f32_32x32x16_bf16 a[96:111], a[188:191], v[212:215], a[96:111]\n"
    "v_cmp_lt_f32 vcc, s72, v20\n"
    "v_mfma_f32_32x32x16_bf16 a[112:127], a[188:191], v[220:223], a[112:127]\n"
    "ds_read_b64_tr_b16 a[188:189], v10 offset:37632\n"
    "ds_read_b64_tr_b16 a[190:191], v10 offset:39680\n"
    "s_cbranch_vccnz .Lfb_stub12\n"
    ".Lfb_ret12:\n"
    "v_mfma_f32_32x32x16_bf16 v[128:143], a[128:131], v[64:67], v[224:239]\n"
    "v_add_f32 v16, v16, v18\n"
    "v_add_f32 v17, v17, v19\n"
    "v_exp_f32 v32, v160\n"
    "v_mfma_f32_32x32x16_bf16 v[144:159], a[128:131], v[96:99], v[240:255]\n"
    "ds_read_b128 a[128:131], v2 offset:57344\n"
    "v_exp_f32 v33, v161\n"
    "v_exp_f32 v34, v162\n"
    "v_add_f32 v18, v32, v33\n"
    "v_mfma_f32_32x32x16_bf16 v[128:143], a[132:135], v[68:71], v[128:143]\n"
    "v_cvt_pk_bf16_f32 v208, v32, v33\n"
    "s_add_u32 m0, s69, 0x4000\n"
    "s_lshl_b32 s78, s64, 14\n"
    "s_add_u32 s78, s78, 0x30000\n"
    "v_mfma_f32_32x32x16_bf16 v[144:159], a[132:135], v[100:103], v[144:159]\n"
    "ds_read_b128 a[132:135], v3 offset:57344\n"
    "buffer_load_dwordx4 v26, s[48:51], s78 offen offset:0 lds\n"
    "v_exp_f32 v35, v163\n"
    "v_add_f32 v18, v18, v34\n"
    "v_mfma_f32_32x32x16_bf16 v[128:143], a[136:139], v[72:75], v[128:143]\n"
    "v_exp_f32 v36, v164\n"
    "v_add_f32 v18, v18, v35\n"
    "v_cvt_pk_bf16_f32 v209, v34, v35\n"
    "v_exp_f32 v37, v165\n"
    "v_mfma_f32_32x32x16_bf16 v[144:159], a[136:139], v[104:107], v[144:159]\n"
    "ds_read_b128 a[136:139], v4 offset:57344\n"
    "v_add_f32 v18, v18, v36\n"
    "v_exp_f32 v38, v166\n"
    "v_add_f32 v18, v18, v37\n"
    "v_mfma_f32_32x32x16_bf16 v[128:143], a[140:143], v[76:79], v[128:143]\n"
    "buffer_load_dwordx4 v27, s[48:51], s78 offen offset:1024 lds\n"
    "v_cvt_pk_bf16_f32 v210, v36, v37\n"
    "v_exp_f32 v39, v167\n"
    "v_add_f32 v18, v18, v38\n"
    "v_mfma_f32_32x32x16_bf16 v[144:159], a[140:143], v[108:111], v[144:159]\n"
    "ds_read_b128 a[140:143], v5 offset:57344\n"
    "v_exp_f32 v32, v168\n"
    "v_add_f32 v18, v18, v39\n"
    "v_cvt_pk_bf16_f32 v211, v38, v39\n"
    "v_mfma_f32_32x32x16_bf16 v[128:143], a[144:147], v[80:83], v[128:143]\n"
    "v_exp_f32 v33, v169\n"
    "v_add_f32 v18, v18, v32\n"
    "v_exp_f32 v34, v170\n"
    "v_mfma_f32_32x32x16_bf16 v[144:159], a[144:147], v[112:115], v[144:159]\n"
    "ds_read_b128 a[144:147], v6 offset:57344\n"
    "buffer_load_dwordx4 v28, s[48:51], s78 offen offset:2048 lds\n"
    "v_add_f32 v18, v18, v33\n"
    "v_cvt_pk_bf16_f32 v212, v32, v33\n"
    "v_mfma_f32_32x32x16_bf16 v[128:143], a[148:151], v[84:87], v[128:143]\n"
    "v_exp_f32 v35, v171\n"
    "v_add_f32 v18, v18, v34\n"
    "v_exp_f32 v36, v172\n"
    "v_add_f32 v18, v18, v35\n"
    "v_mfma_f32_32x32x16_bf16 v[144:159], a[148:151], v[116:119], v[144:159]\n"
    "ds_read_b128 a[148:151], v7 offset:57344\n"
    "v_cvt_pk_bf16_f32 v213, v34, v35\n"
    "v_exp_f32 v37, v173\n"
    "v_add_f32 v18, v18, v36\n"
    "v_mfma_f32_32x32x16_bf16 v[128:143], a[152:155], v[88:91], v[128:143]\n"
    "buffer_load_dwordx4 v29, s[48:51], s78 offen offset:3072 lds\n"
    "v_exp_f32 v38, v174\n"
    "v_add_f32 v18, v18, v37\n"
    "v_cvt_pk_bf16_f32 v214, v36, v37\n"
    "v_mfma_f32_32x32x16_bf16 v[144:159], a[152:155], v[120:123], v[144:159]\n"
    "ds_read_b128 a[152:155], v8 offset:57344\n"
    "v_exp_f32 v39, v175\n"
    "v_add_f32 v18, v18, v38\n"
    "s_waitcnt lgkmcnt(15)\n"
    "v_mfma_f32_32x32x16_bf16 v[128:143], a[156:159], v[92:95], v[128:143]\n"
    "v_exp_f32 v32, v176\n"
    "v_add_f32 v18, v18, v39\n"
    "v_cvt_pk_bf16_f32 v215, v38, v39\n"
    "v_exp_f32 v33, v177\n"
    "v_mfma_f32_32x32x16_bf16 v[144:159], a[156:159], v[124:127], v[144:159]\n"
    "ds_read_b128 a[156:159], v9 offset:57344\n"
    "v_exp_f32 v34, v178\n"
    "v_add_f32 v19, v32, v33\n"
    "v_cvt_pk_bf16_f32 v216, v32, v33\n"
    "v_mfma_f32_32x32x16_bf16 a[0:15], a[160:163], v[192:195], a[0:15]\n"
    "v_exp_f32 v35, v179\n"
    "s_add_u32 m0, s69, 0x10000\n"
    "s_lshl_b32 s78, s64, 14\n"
    "s_add_u32 s78, s78, 0x40000\n"
    "v_mfma_f32_32x32x16_bf16 a[16:31], a[160:163], v[200:203], a[16:31]\n"
    "ds_read_b64_tr_b16 a[160:161], v10 offset:40960\n"
    "ds_read_b64_tr_b16 a[162:163], v10 offset:43008\n"
    "buffer_load_dwordx4 v26, s[48:51], s78 offen offset:0 lds\n"
    "v_add_f32 v19, v19, v34\n"
    "v_mfma_f32_32x32x16_bf16 a[32:47], a[164:167], v[192:195], a[32:47]\n"
    "v_exp_f32 v36, v180\n"
    "v_add_f32 v19, v19, v35\n"
    "v_cvt_pk_bf16_f32 v217, v34, v35\n"
    "v_exp_f32 v37, v181\n"
    "v_mfma_f32_32x32x16_bf16 a[48:63], a[164:167], v[200:203], a[48:63]\n"
    "ds_read_b64_tr_b16 a[164:165], v10 offset:41216\n"
    "ds_read_b64_tr_b16 a[166:167], v10 offset:43264\n"
    "v_add_f32 v19, v19, v36\n"
    "v_exp_f32 v38, v182\n"
    "v_mfma_f32_32x32x16_bf16 a[64:79], a[168:171], v[192:195], a[64:79]\n"
    "buffer_load_dwordx4 v27, s[48:51], s78 offen offset:1024 lds\n"
    "v_add_f32 v19, v19, v37\n"
    "v_cvt_pk_bf16_f32 v218, v36, v37\n"
    "v_exp_f32 v39, v183\n"
    "v_mfma_f32_32x32x16_bf16 a[80:95], a[168:171], v[200:203], a[80:95]\n"
    "ds_read_b64_tr_b16 a[168:169], v10 offset:41472\n"
    "ds_read_b64_tr_b16 a[170:171], v10 offset:43520\n"
    "v_add_f32 v19, v19, v38\n"
    "v_exp_f32 v32, v184\n"
    "v_mfma_f32_32x32x16_bf16 a[96:111], a[172:175], v[192:195], a[96:111]\n"
    "v_add_f32 v19, v19, v39\n"
    "v_cvt_pk_bf16_f32 v219, v38, v39\n"
    "v_exp_f32 v33, v185\n"
    "v_add_f32 v19, v19, v32\n"
    "v_mfma_f32_32x32x16_bf16 a[112:127], a[172:175], v[200:203], a[112:127]\n"
    "ds_read_b64_tr_b16 a[172:173], v10 offset:41728\n"
    "ds_read_b64_tr_b16 a[174:175], v10 offset:43776\n"
    "buffer_load_dwordx4 v28, s[48:51], s78 offen offset:2048 lds\n"
    "v_exp_f32 v34, v186\n"
    "s_waitcnt lgkmcnt(15)\n"
    "v_mfma_f32_32x32x16_bf16 a[0:15], a[176:179], v[196:199], a[0:15]\n"
    "v_add_f32 v19, v19, v33\n"
    "v_cvt_pk_bf16_f32 v220, v32, v33\n"
    "v_exp_f32 v35, v187\n"
    "v_add_f32 v19, v19, v34\n"
    "v_mfma_f32_32x32x16_bf16 a[16:31], a[176:179], v[204:207], a[16:31]\n"
    "ds_read_b64_tr_b16 a[176:177], v10 offset:45056\n"
    "ds_read_b64_tr_b16 a[178:179], v10 offset:47104\n"
    "v_exp_f32 v36, v188\n"
    "v_add_f32 v19, v19, v35\n"
    "v_mfma_f32_32x32x16_bf16 a[32:47], a[180:183], v[196:199], a[32:47]\n"
    "buffer_load_dwordx4 v29, s[48:51], s78 offen offset:3072 lds\n"
    "v_cvt_pk_bf16_f32 v221, v34, v35\n"
    "v_exp_f32 v37, v189\n"
    "v_add_f32 v19, v19, v36\n"
    "v_mfma_f32_32x32x16_bf16 a[48:63], a[180:183], v[204:207], a[48:63]\n"
    "ds_read_b64_tr_b16 a[180:181], v10 offset:45312\n"
    "ds_read_b64_tr_b16 a[182:183], v10 offset:47360\n"
    "v_exp_f32 v38, v190\n"
    "v_add_f32 v19, v19, v37\n"
    "v_mfma_f32_32x32x16_bf16 a[64:79], a[184:187], v[196:199], a[64:79]\n"
    "v_cvt_pk_bf16_f32 v222, v36, v37\n"
    "v_exp_f32 v39, v191\n"
    "v_add_f32 v19, v19, v38\n"
    "v_add_f32 v19, v19, v39\n"
    "v_mfma_f32_32x32x16_bf16 a[80:95], a[184:187], v[204:207], a[80:95]\n"
    "ds_read_b64_tr_b16 a[184:185], v10 offset:45568\n"
    "ds_read_b64_tr_b16 a[186:187], v10 offset:47616\n"
    "v_cvt_pk_bf16_f32 v223, v38, v39\n"
    "v_max_f32 v20, v18, v19\n"
    "v_cmp_lt_f32 vcc, s72, v20\n"
    "v_mfma_f32_32x32x16_bf16 a[96:111], a[188:191], v[196:199], a[96:111]\n"
    "v_mfma_f32_32x32x16_bf16 a[112:127], a[188:191], v[204:207], a[112:127]\n"
    "ds_read_b64_tr_b16 a[188:189], v10 offset:45824\n"
    "ds_read_b64_tr_b16 a[190:191], v10 offset:47872\n"
    "s_cbranch_vccnz .Lfb_stub13\n"
    ".Lfb_ret13:\n"
    "v_mfma_f32_32x32x16_bf16 v[160:175], a[128:131], v[64:67], v[224:239]\n"
    "s_waitcnt vmcnt(20)\n"
    "s_barrier\n"
    "v_add_f32 v16, v16, v18\n"
    "v_add_f32 v17, v17, v19\n"
    "v_mfma_f32_32x32x16_bf16 v[176:191], a[128:131], v[96:99], v[240:255]\n"
    "v_exp_f32 v32, v128\n"
    "v_exp_f32 v33, v129\n"
    "v_exp_f32 v34, v130\n"
    "s_waitcnt lgkmcnt(15)\n"
    "v_mfma_f32_32x32x16_bf16 v[160:175], a[132:135], v[68:71], v[160:175]\n"
    "v_add_f32 v18, v32, v33\n"
    "v_cvt_pk_bf16_f32 v192, v32, v33\n"
    "s_add_u32 m0, s69, 0x8000\n"
    "s_lshl_b32 s78, s64, 14\n"
    "s_add_u32 s78, s78, 0x50000\n"
    "v_mfma_f32_32x32x16_bf16 v[176:191], a[132:135], v[100:103], v[176:191]\n"
    "buffer_load_dwordx4 v26, s[48:51], s78 offen offset:0 lds\n"
    "v_exp_f32 v35, v131\n"
    "v_add_f32 v18, v18, v34\n"
    "v_mfma_f32_32x32x16_bf16 v[160:175], a[136:139], v[72:75], v[160:175]\n"
    "v_exp_f32 v36, v132\n"
    "v_add_f32 v18, v18, v35\n"
    "v_cvt_pk_bf16_f32 v193, v34, v35\n"
    "v_mfma_f32_32x32x16_bf16 v[176:191], a[136:139], v[104:107], v[176:191]\n"
    "v_exp_f32 v37, v133\n"
    "v_add_f32 v18, v18, v36\n"
    "v_exp_f32 v38, v134\n"
    "v_add_f32 v18, v18, v37\n"
    "v_mfma_f32_32x32x16_bf16 v[160:175], a[140:143], v[76:79], v[160:175]\n"
    "buffer_load_dwordx4 v27, s[48:51], s78 offen offset:1024 lds\n"
    "v_cvt_pk_bf16_f32 v194, v36, v37\n"
    "v_exp_f32 v39, v135\n"
    "v_add_f32 v18, v18, v38\n"
    "v_mfma_f32_32x32x16_bf16 v[176:191], a[140:143], v[108:111], v[176:191]\n"
    "v_exp_f32 v32, v136\n"
    "v_add_f32 v18, v18, v39\n"
    "v_cvt_pk_bf16_f32 v195, v38, v39\n"
    "v_mfma_f32_32x32x16_bf16 v[160:175], a[144:147], v[80:83], v[160:175]\n"
    "v_exp_f32 v33, v137\n"
    "v_add_f32 v18, v18, v32\n"
    "v_exp_f32 v34, v138\n"
    "v_mfma_f32_32x32x16_bf16 v[176:191], a[144:147], v[112:115], v[176:191]\n"
    "buffer_load_dwordx4 v28, s[48:51], s78 offen offset:2048 lds\n"
    "v_add_f32 v18, v18, v33\n"
    "v_cvt_pk_bf16_f32 v196, v32, v33\n"
    "v_exp_f32 v35, v139\n"
    "v_mfma_f32_32x32x16_bf16 v[160:175], a[148:151], v[84:87], v[160:175]\n"
    "v_add_f32 v18, v18, v34\n"
    "v_exp_f32 v36, v140\n"
    "v_add_f32 v18, v18, v35\n"
    "v_cvt_pk_bf16_f32 v197, v34, v35\n"
    "v_mfma_f32_32x32x16_bf16 v[176:191], a[148:151], v[116:119], v[176:191]\n"
    "v_exp_f32 v37, v141\n"
    "v_add_f32 v18, v18, v36\n"
    "v_exp_f32 v38, v142\n"
    "v_mfma_f32_32x32x16_bf16 v[160:175], a[152:155], v[88:91], v[160:175]\n"
    "buffer_load_dwordx4 v29, s[48:51], s78 offen offset:3072 lds\n"
    "v_add_f32 v18, v18, v37\n"
    "v_cvt_pk_bf16_f32 v198, v36, v37\n"
    "v_mfma_f32_32x32x16_bf16 v[176:191], a[152:155], v[120:123], v[176:191]\n"
    "v_exp_f32 v39, v143\n"
    "v_add_f32 v18, v18, v38\n"
    "v_exp_f32 v32, v144\n"
    "v_add_f32 v18, v18, v39\n"
    "v_mfma_f32_32x32x16_bf16 v[160:175], a[156:159], v[92:95], v[160:175]\n"
    "v_cvt_pk_bf16_f32 v199, v38, v39\n"
    "v_exp_f32 v33, v145\n"
    "v_exp_f32 v34, v146\n"
    "v_mfma_f32_32x32x16_bf16 v[176:191], a[156:159], v[124:127], v[176:191]\n"
    "v_add_f32 v19, v32, v33\n"
    "v_cvt_pk_bf16_f32 v200, v32, v33\n"
    "v_exp_f32 v35, v147\n"
    "v_add_f32 v19, v19, v34\n"
    "s_waitcnt lgkmcnt(14)\n"
    "v_mfma_f32_32x32x16_bf16 a[0:15], a[160:163], v[208:211], a[0:15]\n"
    "s_add_u32 m0, s69, 0x14000\n"
    "s_lshl_b32 s78, s64, 14\n"
    "s_add_u32 s78, s78, 0x60000\n"
    "v_mfma_f32_32x32x16_bf16 a[16:31], a[160:163], v[216:219], a[16:31]\n"
    "ds_read_b64_tr_b16 a[160:161], v10 offset:49152\n"
    "ds_read_b64_tr_b16 a[162:163], v10 offset:51200\n"
    "buffer_load_dwordx4 v26, s[48:51], s78 offen offset:0 lds\n"
    "v_exp_f32 v36, v148\n"
    "v_add_f32 v19, v19, v35\n"
    "s_waitcnt lgkmcnt(14)\n"
    "v_mfma_f32_32x32x16_bf16 a[32:47], a[164:167], v[208:211], a[32:47]\n"
    "v_cvt_pk_bf16_f32 v201, v34, v35\n"
    "v_exp_f32 v37, v149\n"
    "v_add_f32 v19, v19, v36\n"
    "v_mfma_f32_32x32x16_bf16 a[48:63], a[164:167], v[216:219], a[48:63]\n"
    "ds_read_b64_tr_b16 a[164:165], v10 offset:49408\n"
    "ds_read_b64_tr_b16 a[166:167], v10 offset:51456\n"
    "v_exp_f32 v38, v150\n"
    "v_add_f32 v19, v19, v37\n"
    "v_cvt_pk_bf16_f32 v202, v36, v37\n"
    "s_waitcnt lgkmcnt(14)\n"
    "v_mfma_f32_32x32x16_bf16 a[64:79], a[168:171], v[208:211], a[64:79]\n"
    "buffer_load_dwordx4 v27, s[48:51], s78 offen offset:1024 lds\n"
    "v_exp_f32 v39, v151\n"
    "v_add_f32 v19, v19, v38\n"
    "v_mfma_f32_32x32x16_bf16 a[80:95], a[168:171], v[216:219], a[80:95]\n"
    "ds_read_b64_tr_b16 a[168:169], v10 offset:49664\n"
    "ds_read_b64_tr_b16 a[170:171], v10 offset:51712\n"
    "v_exp_f32 v32, v152\n"
    "v_add_f32 v19, v19, v39\n"
    "s_waitcnt lgkmcnt(14)\n"
    "v_mfma_f32_32x32x16_bf16 a[96:111], a[172:175], v[208:211], a[96:111]\n"
    "v_cvt_pk_bf16_f32 v203, v38, v39\n"
    "v_exp_f32 v33, v153\n"
    "v_add_f32 v19, v19, v32\n"
    "v_mfma_f32_32x32x16_bf16 a[112:127], a[172:175], v[216:219], a[112:127]\n"
    "ds_read_b64_tr_b16 a[172:173], v10 offset:49920\n"
    "ds_read_b64_tr_b16 a[174:175], v10 offset:51968\n"
    "buffer_load_dwordx4 v28, s[48:51], s78 offen offset:2048 lds\n"
    "v_exp_f32 v34, v154\n"
    "v_add_f32 v19, v19, v33\n"
    "s_waitcnt lgkmcnt(14)\n"
    "v_mfma_f32_32x32x16_bf16 a[0:15], a[176:179], v[212:215], a[0:15]\n"
    "v_cvt_pk_bf16_f32 v204, v32, v33\n"
    "v_exp_f32 v35, v155\n"
    "v_add_f32 v19, v19, v34\n"
    "v_mfma_f32_32x32x16_bf16 a[16:31], a[176:179], v[220:223], a[16:31]\n"
    "ds_read_b64_tr_b16 a[176:177], v10 offset:53248\n"
    "ds_read_b64_tr_b16 a[178:179], v10 offset:55296\n"
    "v_exp_f32 v36, v156\n"
    "v_add_f32 v19, v19, v35\n"
    "s_waitcnt lgkmcnt(14)\n"
    "v_mfma_f32_32x32x16_bf16 a[32:47], a[180:183], v[212:215], a[32:47]\n"
    "buffer_load_dwordx4 v29, s[48:51], s78 offen offset:3072 lds\n"
    "v_cvt_pk_bf16_f32 v205, v34, v35\n"
    "v_exp_f32 v37, v157\n"
    "v_add_f32 v19, v19, v36\n"
    "v_mfma_f32_32x32x16_bf16 a[48:63], a[180:183], v[220:223], a[48:63]\n"
    "ds_read_b64_tr_b16 a[180:181], v10 offset:53504\n"
    "ds_read_b64_tr_b16 a[182:183], v10 offset:55552\n"
    "v_exp_f32 v38, v158\n"
    "v_add_f32 v19, v19, v37\n"
    "s_waitcnt lgkmcnt(14)\n"
    "v_mfma_f32_32x32x16_bf16 a[64:79], a[184:187], v[212:215], a[64:79]\n"
    "v_cvt_pk_bf16_f32 v206, v36, v37\n"
    "v_exp_f32 v39, v159\n"
    "v_add_f32 v19, v19, v38\n"
    "v_mfma_f32_32x32x16_bf16 a[80:95], a[184:187], v[220:223], a[80:95]\n"
    "ds_read_b64_tr_b16 a[184:185], v10 offset:53760\n"
    "ds_read_b64_tr_b16 a[186:187], v10 offset:55808\n"
    "v_add_f32 v19, v19, v39\n"
    "v_cvt_pk_bf16_f32 v207, v38, v39\n"
    "v_max_f32 v20, v18, v19\n"
    "s_waitcnt lgkmcnt(14)\n"
    "v_mfma_f32_32x32x16_bf16 a[96:111], a[188:191], v[212:215], a[96:111]\n"
    "v_cmp_lt_f32 vcc, s72, v20\n"
    "v_mfma_f32_32x32x16_bf16 a[112:127], a[188:191], v[220:223], a[112:127]\n"
    "ds_read_b64_tr_b16 a[188:189], v10 offset:54016\n"
    "ds_read_b64_tr_b16 a[190:191], v10 offset:56064\n"
    "s_cbranch_vccnz .Lfb_stub14\n"
    ".Lfb_ret14:\n"
    "s_waitcnt lgkmcnt(14)\n"
    "v_mfma_f32_32x32x16_bf16 a[0:15], a[160:163], v[192:195], a[0:15]\n"
    "v_add_f32 v16, v16, v18\n"
    "v_add_f32 v17, v17, v19\n"
    "v_exp_f32 v32, v160\n"
    "v_exp_f32 v33, v161\n"
    "v_mfma_f32_32x32x16_bf16 a[16:31], a[160:163], v[200:203], a[16:31]\n"
    "ds_read_b64_tr_b16 a[160:161], v10 offset:57344\n"
    "ds_read_b64_tr_b16 a[162:163], v10 offset:59392\n"
    "v_exp_f32 v34, v162\n"
    "v_add_f32 v18, v32, v33\n"
    "v_cvt_pk_bf16_f32 v208, v32, v33\n"
    "s_waitcnt lgkmcnt(14)\n"
    "v_mfma_f32_32x32x16_bf16 a[32:47], a[164:167], v[192:195], a[32:47]\n"
    "v_exp_f32 v35, v163\n"
    "v_add_f32 v18, v18, v34\n"
    "v_exp_f32 v36, v164\n"
    "v_add_f32 v18, v18, v35\n"
    "v_mfma_f32_32x32x16_bf16 a[48:63], a[164:167], v[200:203], a[48:63]\n"
    "ds_read_b64_tr_b16 a[164:165], v10 offset:57600\n"
    "ds_read_b64_tr_b16 a[166:167], v10 offset:59648\n"
    "v_cvt_pk_bf16_f32 v209, v34, v35\n"
    "v_exp_f32 v37, v165\n"
    "v_add_f32 v18, v18, v36\n"
    "s_waitcnt lgkmcnt(14)\n"
    "v_mfma_f32_32x32x16_bf16 a[64:79], a[168:171], v[192:195], a[64:79]\n"
    "v_exp_f32 v38, v166\n"
    "v_add_f32 v18, v18, v37\n"
    "v_cvt_pk_bf16_f32 v210, v36, v37\n"
    "v_exp_f32 v39, v167\n"
    "v_mfma_f32_32x32x16_bf16 a[80:95], a[168:171], v[200:203], a[80:95]\n"
    "ds_read_b64_tr_b16 a[168:169], v10 offset:57856\n"
    "ds_read_b64_tr_b16 a[170:171], v10 offset:59904\n"
    "v_add_f32 v18, v18, v38\n"
    "v_exp_f32 v32, v168\n"
    "v_add_f32 v18, v18, v39\n"
    "s_waitcnt lgkmcnt(14)\n"
    "v_mfma_f32_32x32x16_bf16 a[96:111], a[172:175], v[192:195], a[96:111]\n"
    "v_cvt_pk_bf16_f32 v211, v38, v39\n"
    "v_exp_f32 v33, v169\n"
    "v_add_f32 v18, v18, v32\n"
    "v_exp_f32 v34, v170\n"
    "v_mfma_f32_32x32x16_bf16 a[112:127], a[172:175], v[200:203], a[112:127]\n"
    "ds_read_b64_tr_b16 a[172:173], v10 offset:58112\n"
    "ds_read_b64_tr_b16 a[174:175], v10 offset:60160\n"
    "v_add_f32 v18, v18, v33\n"
    "v_cvt_pk_bf16_f32 v212, v32, v33\n"
    "v_exp_f32 v35, v171\n"
    "s_waitcnt lgkmcnt(14)\n"
    "v_mfma_f32_32x32x16_bf16 a[0:15], a[176:179], v[196:199], a[0:15]\n"
    "v_add_f32 v18, v18, v34\n"
    "v_exp_f32 v36, v172\n"
    "v_add_f32 v18, v18, v35\n"
    "v_cvt_pk_bf16_f32 v213, v34, v35\n"
    "v_mfma_f32_32x32x16_bf16 a[16:31], a[176:179], v[204:207], a[16:31]\n"
    "ds_read_b64_tr_b16 a[176:177], v10 offset:61440\n"
    "ds_read_b64_tr_b16 a[178:179], v10 offset:63488\n"
    "v_exp_f32 v37, v173\n"
    "v_add_f32 v18, v18, v36\n"
    "s_waitcnt lgkmcnt(14)\n"
    "v_mfma_f32_32x32x16_bf16 a[32:47], a[180:183], v[196:199], a[32:47]\n"
    "v_exp_f32 v38, v174\n"
    "v_add_f32 v18, v18, v37\n"
    "v_cvt_pk_bf16_f32 v214, v36, v37\n"
    "v_exp_f32 v39, v175\n"
    "v_mfma_f32_32x32x16_bf16 a[48:63], a[180:183], v[204:207], a[48:63]\n"
    "ds_read_b64_tr_b16 a[180:181], v10 offset:61696\n"
    "ds_read_b64_tr_b16 a[182:183], v10 offset:63744\n"
    "v_add_f32 v18, v18, v38\n"
    "v_exp_f32 v32, v176\n"
    "v_add_f32 v18, v18, v39\n"
    "s_waitcnt lgkmcnt(14)\n"
    "v_mfma_f32_32x32x16_bf16 a[64:79], a[184:187], v[196:199], a[64:79]\n"
    "v_cvt_pk_bf16_f32 v215, v38, v39\n"
    "v_exp_f32 v33, v177\n"
    "v_exp_f32 v34, v178\n"
    "v_add_f32 v19, v32, v33\n"
    "v_mfma_f32_32x32x16_bf16 a[80:95], a[184:187], v[204:207], a[80:95]\n"
    "ds_read_b64_tr_b16 a[184:185], v10 offset:61952\n"
    "ds_read_b64_tr_b16 a[186:187], v10 offset:64000\n"
    "v_cvt_pk_bf16_f32 v216, v32, v33\n"
    "v_exp_f32 v35, v179\n"
    "v_add_f32 v19, v19, v34\n"
    "s_waitcnt lgkmcnt(14)\n"
    "v_mfma_f32_32x32x16_bf16 a[96:111], a[188:191], v[196:199], a[96:111]\n"
    "v_exp_f32 v36, v180\n"
    "v_add_f32 v19, v19, v35\n"
    "v_cvt_pk_bf16_f32 v217, v34, v35\n"
    "v_exp_f32 v37, v181\n"
    "v_mfma_f32_32x32x16_bf16 a[112:127], a[188:191], v[204:207], a[112:127]\n"
    "ds_read_b64_tr_b16 a[188:189], v10 offset:62208\n"
    "ds_read_b64_tr_b16 a[190:191], v10 offset:64256\n"
    "v_add_f32 v19, v19, v36\n"
    "v_exp_f32 v38, v182\n"
    "v_add_f32 v19, v19, v37\n"
    "v_cvt_pk_bf16_f32 v218, v36, v37\n"
    "v_exp_f32 v39, v183\n"
    "v_add_f32 v19, v19, v38\n"
    "v_exp_f32 v32, v184\n"
    "v_add_f32 v19, v19, v39\n"
    "v_cvt_pk_bf16_f32 v219, v38, v39\n"
    "v_exp_f32 v33, v185\n"
    "v_add_f32 v19, v19, v32\n"
    "v_exp_f32 v34, v186\n"
    "v_add_f32 v19, v19, v33\n"
    "v_cvt_pk_bf16_f32 v220, v32, v33\n"
    "v_exp_f32 v35, v187\n"
    "v_add_f32 v19, v19, v34\n"
    "v_exp_f32 v36, v188\n"
    "v_add_f32 v19, v19, v35\n"
    "v_cvt_pk_bf16_f32 v221, v34, v35\n"
    "v_exp_f32 v37, v189\n"
    "v_add_f32 v19, v19, v36\n"
    "v_exp_f32 v38, v190\n"
    "v_add_f32 v19, v19, v37\n"
    "v_cvt_pk_bf16_f32 v222, v36, v37\n"
    "v_exp_f32 v39, v191\n"
    "v_add_f32 v19, v19, v38\n"
    "v_add_f32 v19, v19, v39\n"
    "v_cvt_pk_bf16_f32 v223, v38, v39\n"
    "v_max_f32 v20, v18, v19\n"
    "v_cmp_lt_f32 vcc, s72, v20\n"
    "s_cbranch_vccnz .Lfb_stub15\n"
    ".Lfb_ret15:\n"
    "s_waitcnt lgkmcnt(14)\n"
    "v_mfma_f32_32x32x16_bf16 a[0:15], a[160:163], v[208:211], a[0:15]\n"
    "v_add_f32 v16, v16, v18\n"
    "v_add_f32 v17, v17, v19\n"
    "v_mov_b32 v22, v16\n"
    "v_mov_b32 v23, v17\n"
    "s_nop 1\n"
    "v_permlane32_swap_b32 v16, v22\n"
    "s_waitcnt lgkmcnt(6)\n"
    "v_mfma_f32_32x32x16_bf16 a[0:15], a[176:179], v[212:215], a[0:15]\n"
    "v_permlane32_swap_b32 v17, v23\n"
    "v_add_f32 v16, v16, v22\n"
    "v_add_f32 v17, v17, v23\n"
    "v_rcp_f32 v20, v16\n"
    "v_mfma_f32_32x32x16_bf16 a[16:31], a[160:163], v[216:219], a[16:31]\n"
    "v_rcp_f32 v21, v17\n"
    "v_lshlrev_b32 v24, 4, v1\n"
    "v_add_u32 v25, 0x40000, v24\n"
    "v_mfma_f32_32x32x16_bf16 a[16:31], a[176:179], v[220:223], a[16:31]\n"
    "v_mfma_f32_32x32x16_bf16 a[32:47], a[164:167], v[208:211], a[32:47]\n"
    "s_waitcnt lgkmcnt(4)\n"
    "v_mfma_f32_32x32x16_bf16 a[32:47], a[180:183], v[212:215], a[32:47]\n"
    "v_accvgpr_read_b32 v32, a0\n"
    "v_accvgpr_read_b32 v33, a1\n"
    "v_accvgpr_read_b32 v34, a2\n"
    "v_accvgpr_read_b32 v35, a3\n"
    "v_accvgpr_read_b32 v36, a4\n"
    "v_accvgpr_read_b32 v37, a5\n"
    "v_mfma_f32_32x32x16_bf16 a[48:63], a[164:167], v[216:219], a[48:63]\n"
    "v_accvgpr_read_b32 v38, a6\n"
    "v_accvgpr_read_b32 v39, a7\n"
    "v_mul_f32 v32, v32, v20\n"
    "v_mul_f32 v33, v33, v20\n"
    "v_mul_f32 v34, v34, v20\n"
    "v_mul_f32 v35, v35, v20\n"
    "v_mfma_f32_32x32x16_bf16 a[48:63], a[180:183], v[220:223], a[48:63]\n"
    "v_mul_f32 v36, v36, v20\n"
    "v_mul_f32 v37, v37, v20\n"
    "v_mul_f32 v38, v38, v20\n"
    "v_mul_f32 v39, v39, v20\n"
    "v_cvt_pk_bf16_f32 v48, v32, v33\n"
    "v_cvt_pk_bf16_f32 v49, v34, v35\n"
    "v_mfma_f32_32x32x16_bf16 a[64:79], a[168:171], v[208:211], a[64:79]\n"
    "v_cvt_pk_bf16_f32 v50, v36, v37\n"
    "v_cvt_pk_bf16_f32 v51, v38, v39\n"
    "buffer_store_dwordx4 v[48:51], v24, s[56:59], 0 offen offset:0 sc1\n"
    "v_accvgpr_read_b32 v40, a8\n"
    "v_accvgpr_read_b32 v41, a9\n"
    "v_accvgpr_read_b32 v42, a10\n"
    "s_waitcnt lgkmcnt(2)\n"
    "v_mfma_f32_32x32x16_bf16 a[64:79], a[184:187], v[212:215], a[64:79]\n"
    "v_accvgpr_read_b32 v43, a11\n"
    "v_accvgpr_read_b32 v44, a12\n"
    "v_accvgpr_read_b32 v45, a13\n"
    "v_accvgpr_read_b32 v46, a14\n"
    "v_accvgpr_read_b32 v47, a15\n"
    "v_mul_f32 v40, v40, v20\n"
    "v_mfma_f32_32x32x16_bf16 a[80:95], a[168:171], v[216:219], a[80:95]\n"
    "v_mul_f32 v41, v41, v20\n"
    "v_mul_f32 v42, v42, v20\n"
    "v_mul_f32 v43, v43, v20\n"
    "v_mul_f32 v44, v44, v20\n"
    "v_mul_f32 v45, v45, v20\n"
    "v_mul_f32 v46, v46, v20\n"
    "v_mfma_f32_32x32x16_bf16 a[80:95], a[184:187], v[220:223], a[80:95]\n"
    "v_mul_f32 v47, v47, v20\n"
    "v_cvt_pk_bf16_f32 v52, v40, v41\n"
    "v_cvt_pk_bf16_f32 v53, v42, v43\n"
    "v_cvt_pk_bf16_f32 v54, v44, v45\n"
    "v_cvt_pk_bf16_f32 v55, v46, v47\n"
    "buffer_store_dwordx4 v[52:55], v24, s[56:59], 0 offen offset:1024 sc1\n"
    "v_mfma_f32_32x32x16_bf16 a[96:111], a[172:175], v[208:211], a[96:111]\n"
    "v_accvgpr_read_b32 v32, a16\n"
    "v_accvgpr_read_b32 v33, a17\n"
    "v_accvgpr_read_b32 v34, a18\n"
    "v_accvgpr_read_b32 v35, a19\n"
    "v_accvgpr_read_b32 v36, a20\n"
    "v_accvgpr_read_b32 v37, a21\n"
    "s_waitcnt lgkmcnt(0)\n"
    "v_mfma_f32_32x32x16_bf16 a[96:111], a[188:191], v[212:215], a[96:111]\n"
    "v_accvgpr_read_b32 v38, a22\n"
    "v_accvgpr_read_b32 v39, a23\n"
    "v_mul_f32 v32, v32, v21\n"
    "v_mul_f32 v33, v33, v21\n"
    "v_mul_f32 v34, v34, v21\n"
    "v_mul_f32 v35, v35, v21\n"
    "v_mfma_f32_32x32x16_bf16 a[112:127], a[172:175], v[216:219], a[112:127]\n"
    "v_mul_f32 v36, v36, v21\n"
    "v_mul_f32 v37, v37, v21\n"
    "v_mul_f32 v38, v38, v21\n"
    "v_mul_f32 v39, v39, v21\n"
    "v_cvt_pk_bf16_f32 v56, v32, v33\n"
    "v_cvt_pk_bf16_f32 v57, v34, v35\n"
    "v_mfma_f32_32x32x16_bf16 a[112:127], a[188:191], v[220:223], a[112:127]\n"
    "v_cvt_pk_bf16_f32 v58, v36, v37\n"
    "v_cvt_pk_bf16_f32 v59, v38, v39\n"
    "buffer_store_dwordx4 v[56:59], v25, s[56:59], 0 offen offset:0 sc1\n"
    "v_accvgpr_read_b32 v40, a24\n"
    "v_accvgpr_read_b32 v41, a25\n"
    "v_accvgpr_read_b32 v42, a26\n"
    "s_barrier\n"
    "s_nop 7\n"
    "v_accvgpr_read_b32 v43, a27\n"
    "v_accvgpr_read_b32 v44, a28\n"
    "v_accvgpr_read_b32 v45, a29\n"
    "v_accvgpr_read_b32 v46, a30\n"
    "v_accvgpr_read_b32 v47, a31\n"
    "v_mul_f32 v40, v40, v21\n"
    "v_mul_f32 v41, v41, v21\n"
    "v_mul_f32 v42, v42, v21\n"
    "v_mul_f32 v43, v43, v21\n"
    "v_mul_f32 v44, v44, v21\n"
    "v_mul_f32 v45, v45, v21\n"
    "v_mul_f32 v46, v46, v21\n"
    "v_mul_f32 v47, v47, v21\n"
    "v_cvt_pk_bf16_f32 v60, v40, v41\n"
    "v_cvt_pk_bf16_f32 v61, v42, v43\n"
    "v_cvt_pk_bf16_f32 v62, v44, v45\n"
    "v_cvt_pk_bf16_f32 v63, v46, v47\n"
    "buffer_store_dwordx4 v[60:63], v25, s[56:59], 0 offen offset:1024 sc1\n"
    "v_accvgpr_read_b32 v32, a32\n"
    "v_accvgpr_read_b32 v33, a33\n"
    "v_accvgpr_read_b32 v34, a34\n"
    "v_accvgpr_read_b32 v35, a35\n"
    "v_accvgpr_read_b32 v36, a36\n"
    "v_accvgpr_read_b32 v37, a37\n"
    "v_accvgpr_read_b32 v38, a38\n"
    "v_accvgpr_read_b32 v39, a39\n"
    "v_mul_f32 v32, v32, v20\n"
    "v_mul_f32 v33, v33, v20\n"
    "v_mul_f32 v34, v34, v20\n"
    "v_mul_f32 v35, v35, v20\n"
    "v_mul_f32 v36, v36, v20\n"
    "v_mul_f32 v37, v37, v20\n"
    "v_mul_f32 v38, v38, v20\n"
    "v_mul_f32 v39, v39, v20\n"
    "v_cvt_pk_bf16_f32 v48, v32, v33\n"
    "v_cvt_pk_bf16_f32 v49, v34, v35\n"
    "v_cvt_pk_bf16_f32 v50, v36, v37\n"
    "v_cvt_pk_bf16_f32 v51, v38, v39\n"
    "buffer_store_dwordx4 v[48:51], v24, s[56:59], 0 offen offset:2048 sc1\n"
    "v_accvgpr_read_b32 v40, a40\n"
    "v_accvgpr_read_b32 v41, a41\n"
    "v_accvgpr_read_b32 v42, a42\n"
    "v_accvgpr_read_b32 v43, a43\n"
    "v_accvgpr_read_b32 v44, a44\n"
    "v_accvgpr_read_b32 v45, a45\n"
    "v_accvgpr_read_b32 v46, a46\n"
    "v_accvgpr_read_b32 v47, a47\n"
    "v_mul_f32 v40, v40, v20\n"
    "v_mul_f32 v41, v41, v20\n"
    "v_mul_f32 v42, v42, v20\n"
    "v_mul_f32 v43, v43, v20\n"
    "v_mul_f32 v44, v44, v20\n"
    "v_mul_f32 v45, v45, v20\n"
    "v_mul_f32 v46, v46, v20\n"
    "v_mul_f32 v47, v47, v20\n"
    "v_cvt_pk_bf16_f32 v52, v40, v41\n"
    "v_cvt_pk_bf16_f32 v53, v42, v43\n"
    "v_cvt_pk_bf16_f32 v54, v44, v45\n"
    "v_cvt_pk_bf16_f32 v55, v46, v47\n"
    "buffer_store_dwordx4 v[52:55], v24, s[56:59], 0 offen offset:3072 sc1\n"
    "v_accvgpr_read_b32 v32, a48\n"
    "v_accvgpr_read_b32 v33, a49\n"
    "v_accvgpr_read_b32 v34, a50\n"
    "v_accvgpr_read_b32 v35, a51\n"
    "v_accvgpr_read_b32 v36, a52\n"
    "v_accvgpr_read_b32 v37, a53\n"
    "v_accvgpr_read_b32 v38, a54\n"
    "v_accvgpr_read_b32 v39, a55\n"
    "v_mul_f32 v32, v32, v21\n"
    "v_mul_f32 v33, v33, v21\n"
    "v_mul_f32 v34, v34, v21\n"
    "v_mul_f32 v35, v35, v21\n"
    "v_mul_f32 v36, v36, v21\n"
    "v_mul_f32 v37, v37, v21\n"
    "v_mul_f32 v38, v38, v21\n"
    "v_mul_f32 v39, v39, v21\n"
    "v_cvt_pk_bf16_f32 v56, v32, v33\n"
    "v_cvt_pk_bf16_f32 v57, v34, v35\n"
    "v_cvt_pk_bf16_f32 v58, v36, v37\n"
    "v_cvt_pk_bf16_f32 v59, v38, v39\n"
    "buffer_store_dwordx4 v[56:59], v25, s[56:59], 0 offen offset:2048 sc1\n"
    "v_accvgpr_read_b32 v40, a56\n"
    "v_accvgpr_read_b32 v41, a57\n"
    "v_accvgpr_read_b32 v42, a58\n"
    "v_accvgpr_read_b32 v43, a59\n"
    "v_accvgpr_read_b32 v44, a60\n"
    "v_accvgpr_read_b32 v45, a61\n"
    "v_accvgpr_read_b32 v46, a62\n"
    "v_accvgpr_read_b32 v47, a63\n"
    "v_mul_f32 v40, v40, v21\n"
    "v_mul_f32 v41, v41, v21\n"
    "v_mul_f32 v42, v42, v21\n"
    "v_mul_f32 v43, v43, v21\n"
    "v_mul_f32 v44, v44, v21\n"
    "v_mul_f32 v45, v45, v21\n"
    "v_mul_f32 v46, v46, v21\n"
    "v_mul_f32 v47, v47, v21\n"
    "v_cvt_pk_bf16_f32 v60, v40, v41\n"
    "v_cvt_pk_bf16_f32 v61, v42, v43\n"
    "v_cvt_pk_bf16_f32 v62, v44, v45\n"
    "v_cvt_pk_bf16_f32 v63, v46, v47\n"
    "buffer_store_dwordx4 v[60:63], v25, s[56:59], 0 offen offset:3072 sc1\n"
    "v_accvgpr_read_b32 v32, a64\n"
    "v_accvgpr_read_b32 v33, a65\n"
    "v_accvgpr_read_b32 v34, a66\n"
    "v_accvgpr_read_b32 v35, a67\n"
    "v_accvgpr_read_b32 v36, a68\n"
    "v_accvgpr_read_b32 v37, a69\n"
    "v_accvgpr_read_b32 v38, a70\n"
    "v_accvgpr_read_b32 v39, a71\n"
    "v_mul_f32 v32, v32, v20\n"
    "v_mul_f32 v33, v33, v20\n"
    "v_mul_f32 v34, v34, v20\n"
    "v_mul_f32 v35, v35, v20\n"
    "v_mul_f32 v36, v36, v20\n"
    "v_mul_f32 v37, v37, v20\n"
    "v_mul_f32 v38, v38, v20\n"
    "v_mul_f32 v39, v39, v20\n"
    "v_cvt_pk_bf16_f32 v48, v32, v33\n"
    "v_cvt_pk_bf16_f32 v49, v34, v35\n"
    "v_cvt_pk_bf16_f32 v50, v36, v37\n"
    "v_cvt_pk_bf16_f32 v51, v38, v39\n"
    "buffer_store_dwordx4 v[48:51], v24, s[56:59], s76 offen offset:0 sc1\n"
    "v_accvgpr_read_b32 v40, a72\n"
    "v_accvgpr_read_b32 v41, a73\n"
    "v_accvgpr_read_b32 v42, a74\n"
    "v_accvgpr_read_b32 v43, a75\n"
    "v_accvgpr_read_b32 v44, a76\n"
    "v_accvgpr_read_b32 v45, a77\n"
    "v_accvgpr_read_b32 v46, a78\n"
    "v_accvgpr_read_b32 v47, a79\n"
    "v_mul_f32 v40, v40, v20\n"
    "v_mul_f32 v41, v41, v20\n"
    "v_mul_f32 v42, v42, v20\n"
    "v_mul_f32 v43, v43, v20\n"
    "v_mul_f32 v44, v44, v20\n"
    "v_mul_f32 v45, v45, v20\n"
    "v_mul_f32 v46, v46, v20\n"
    "v_mul_f32 v47, v47, v20\n"
    "v_cvt_pk_bf16_f32 v52, v40, v41\n"
    "v_cvt_pk_bf16_f32 v53, v42, v43\n"
    "v_cvt_pk_bf16_f32 v54, v44, v45\n"
    "v_cvt_pk_bf16_f32 v55, v46, v47\n"
    "buffer_store_dwordx4 v[52:55], v24, s[56:59], s76 offen offset:1024 sc1\n"
    "v_accvgpr_read_b32 v32, a80\n"
    "v_accvgpr_read_b32 v33, a81\n"
    "v_accvgpr_read_b32 v34, a82\n"
    "v_accvgpr_read_b32 v35, a83\n"
    "v_accvgpr_read_b32 v36, a84\n"
    "v_accvgpr_read_b32 v37, a85\n"
    "v_accvgpr_read_b32 v38, a86\n"
    "v_accvgpr_read_b32 v39, a87\n"
    "v_mul_f32 v32, v32, v21\n"
    "v_mul_f32 v33, v33, v21\n"
    "v_mul_f32 v34, v34, v21\n"
    "v_mul_f32 v35, v35, v21\n"
    "v_mul_f32 v36, v36, v21\n"
    "v_mul_f32 v37, v37, v21\n"
    "v_mul_f32 v38, v38, v21\n"
    "v_mul_f32 v39, v39, v21\n"
    "v_cvt_pk_bf16_f32 v56, v32, v33\n"
    "v_cvt_pk_bf16_f32 v57, v34, v35\n"
    "v_cvt_pk_bf16_f32 v58, v36, v37\n"
    "v_cvt_pk_bf16_f32 v59, v38, v39\n"
    "buffer_store_dwordx4 v[56:59], v25, s[56:59], s76 offen offset:0 sc1\n"
    "v_accvgpr_read_b32 v40, a88\n"
    "v_accvgpr_read_b32 v41, a89\n"
    "v_accvgpr_read_b32 v42, a90\n"
    "v_accvgpr_read_b32 v43, a91\n"
    "v_accvgpr_read_b32 v44, a92\n"
    "v_accvgpr_read_b32 v45, a93\n"
    "v_accvgpr_read_b32 v46, a94\n"
    "v_accvgpr_read_b32 v47, a95\n"
    "v_mul_f32 v40, v40, v21\n"
    "v_mul_f32 v41, v41, v21\n"
    "v_mul_f32 v42, v42, v21\n"
    "v_mul_f32 v43, v43, v21\n"
    "v_mul_f32 v44, v44, v21\n"
    "v_mul_f32 v45, v45, v21\n"
    "v_mul_f32 v46, v46, v21\n"
    "v_mul_f32 v47, v47, v21\n"
    "v_cvt_pk_bf16_f32 v60, v40, v41\n"
    "v_cvt_pk_bf16_f32 v61, v42, v43\n"
    "v_cvt_pk_bf16_f32 v62, v44, v45\n"
    "v_cvt_pk_bf16_f32 v63, v46, v47\n"
    "buffer_store_dwordx4 v[60:63], v25, s[56:59], s76 offen offset:1024 sc1\n"
    "v_accvgpr_read_b32 v32, a96\n"
    "v_accvgpr_read_b32 v33, a97\n"
    "v_accvgpr_read_b32 v34, a98\n"
    "v_accvgpr_read_b32 v35, a99\n"
    "v_accvgpr_read_b32 v36, a100\n"
    "v_accvgpr_read_b32 v37, a101\n"
    "v_accvgpr_read_b32 v38, a102\n"
    "v_accvgpr_read_b32 v39, a103\n"
    "v_mul_f32 v32, v32, v20\n"
    "v_mul_f32 v33, v33, v20\n"
    "v_mul_f32 v34, v34, v20\n"
    "v_mul_f32 v35, v35, v20\n"
    "v_mul_f32 v36, v36, v20\n"
    "v_mul_f32 v37, v37, v20\n"
    "v_mul_f32 v38, v38, v20\n"
    "v_mul_f32 v39, v39, v20\n"
    "v_cvt_pk_bf16_f32 v48, v32, v33\n"
    "v_cvt_pk_bf16_f32 v49, v34, v35\n"
    "v_cvt_pk_bf16_f32 v50, v36, v37\n"
    "v_cvt_pk_bf16_f32 v51, v38, v39\n"
    "buffer_store_dwordx4 v[48:51], v24, s[56:59], s76 offen offset:2048 sc1\n"
    "v_accvgpr_read_b32 v40, a104\n"
    "v_accvgpr_read_b32 v41, a105\n"
    "v_accvgpr_read_b32 v42, a106\n"
    "v_accvgpr_read_b32 v43, a107\n"
    "v_accvgpr_read_b32 v44, a108\n"
    "v_accvgpr_read_b32 v45, a109\n"
    "v_accvgpr_read_b32 v46, a110\n"
    "v_accvgpr_read_b32 v47, a111\n"
    "v_mul_f32 v40, v40, v20\n"
    "v_mul_f32 v41, v41, v20\n"
    "v_mul_f32 v42, v42, v20\n"
    "v_mul_f32 v43, v43, v20\n"
    "v_mul_f32 v44, v44, v20\n"
    "v_mul_f32 v45, v45, v20\n"
    "v_mul_f32 v46, v46, v20\n"
    "v_mul_f32 v47, v47, v20\n"
    "v_cvt_pk_bf16_f32 v52, v40, v41\n"
    "v_cvt_pk_bf16_f32 v53, v42, v43\n"
    "v_cvt_pk_bf16_f32 v54, v44, v45\n"
    "v_cvt_pk_bf16_f32 v55, v46, v47\n"
    "buffer_store_dwordx4 v[52:55], v24, s[56:59], s76 offen offset:3072 sc1\n"
    "v_accvgpr_read_b32 v32, a112\n"
    "v_accvgpr_read_b32 v33, a113\n"
    "v_accvgpr_read_b32 v34, a114\n"
    "v_accvgpr_read_b32 v35, a115\n"
    "v_accvgpr_read_b32 v36, a116\n"
    "v_accvgpr_read_b32 v37, a117\n"
    "v_accvgpr_read_b32 v38, a118\n"
    "v_accvgpr_read_b32 v39, a119\n"
    "v_mul_f32 v32, v32, v21\n"
    "v_mul_f32 v33, v33, v21\n"
    "v_mul_f32 v34, v34, v21\n"
    "v_mul_f32 v35, v35, v21\n"
    "v_mul_f32 v36, v36, v21\n"
    "v_mul_f32 v37, v37, v21\n"
    "v_mul_f32 v38, v38, v21\n"
    "v_mul_f32 v39, v39, v21\n"
    "v_cvt_pk_bf16_f32 v56, v32, v33\n"
    "v_cvt_pk_bf16_f32 v57, v34, v35\n"
    "v_cvt_pk_bf16_f32 v58, v36, v37\n"
    "v_cvt_pk_bf16_f32 v59, v38, v39\n"
    "buffer_store_dwordx4 v[56:59], v25, s[56:59], s76 offen offset:2048 sc1\n"
    "v_accvgpr_read_b32 v40, a120\n"
    "v_accvgpr_read_b32 v41, a121\n"
    "v_accvgpr_read_b32 v42, a122\n"
    "v_accvgpr_read_b32 v43, a123\n"
    "v_accvgpr_read_b32 v44, a124\n"
    "v_accvgpr_read_b32 v45, a125\n"
    "v_accvgpr_read_b32 v46, a126\n"
    "v_accvgpr_read_b32 v47, a127\n"
    "v_mul_f32 v40, v40, v21\n"
    "v_mul_f32 v41, v41, v21\n"
    "v_mul_f32 v42, v42, v21\n"
    "v_mul_f32 v43, v43, v21\n"
    "v_mul_f32 v44, v44, v21\n"
    "v_mul_f32 v45, v45, v21\n"
    "v_mul_f32 v46, v46, v21\n"
    "v_mul_f32 v47, v47, v21\n"
    "v_cvt_pk_bf16_f32 v60, v40, v41\n"
    "v_cvt_pk_bf16_f32 v61, v42, v43\n"
    "v_cvt_pk_bf16_f32 v62, v44, v45\n"
    "v_cvt_pk_bf16_f32 v63, v46, v47\n"
    "buffer_store_dwordx4 v[60:63], v25, s[56:59], s76 offen offset:3072 sc1\n"
    "s_lshl_b32 s73, s66, 2\n"
    "s_lshr_b32 s74, s65, 3\n"
    "s_add_u32 s73, s73, s74\n"
    "s_add_u32 s92, s86, 0xb00\n"
    "s_addc_u32 s93, s87, 0\n"
    "s_lshl_b32 s99, s73, 8\n"
    "s_add_u32 s92, s92, s99\n"
    "s_addc_u32 s93, s93, 0\n"
    "s_waitcnt vmcnt(0)\n"
    "s_barrier\n"
    "s_cmp_lg_u32 s64, 0\n"
    "s_cbranch_scc1 .Lsig7\n"
    "v_mov_b32 v13, 0\n"
    "v_mov_b32 v14, 1\n"
    "s_mov_b64 exec, 1\n"
    "global_atomic_add v13, v14, s[92:93]\n"
    "s_mov_b64 exec, -1\n"
    ".Lsig7:\n"
    "v_lshlrev_b32 v2, 4, v1\n"
    "v_lshrrev_b32 v11, 5, v1\n"
    "v_lshlrev_b32 v11, 10, v11\n"
    "v_bfe_u32 v12, v1, 2, 2\n"
    "v_lshl_add_u32 v11, v12, 6, v11\n"
    "v_bfe_u32 v12, v1, 4, 1\n"
    "v_lshl_add_u32 v11, v12, 5, v11\n"
    "v_and_b32 v12, 3, v1\n"
    "v_lshl_add_u32 v8, v12, 3, v11\n"
    "v_add_u32 v9, 0x10000, v8\n"
    "v_add_u32 v10, 0x20000, v8\n"
    "s_lshr_b32 s73, s70, 1\n"
    "s_lshl_b32 s73, s73, 2\n"
    "s_lshr_b32 s74, s79, 2\n"
    "s_add_u32 s73, s73, s74\n"
    "s_mov_b32 s65, s73\n"
    "s_add_u32 m0, s69, 0xc000\n"
    "s_lshl_b32 s78, s64, 14\n"
    "s_add_u32 s78, s78, 0x70000\n"
    "s_nop 0\n"
    "buffer_load_dwordx4 v26, s[48:51], s78 offen offset:0 lds\n"
    "buffer_load_dwordx4 v27, s[48:51], s78 offen offset:1024 lds\n"
    "buffer_load_dwordx4 v28, s[48:51], s78 offen offset:2048 lds\n"
    "buffer_load_dwordx4 v29, s[48:51], s78 offen offset:3072 lds\n"
    "s_add_u32 m0, s69, 0x18000\n"
    "s_lshl_b32 s78, s64, 14\n"
    "s_add_u32 s78, s78, 0x80000\n"
    "s_nop 0\n"
    "buffer_load_dwordx4 v26, s[48:51], s78 offen offset:0 lds\n"
    "buffer_load_dwordx4 v27, s[48:51], s78 offen offset:1024 lds\n"
    "buffer_load_dwordx4 v28, s[48:51], s78 offen offset:2048 lds\n"
    "buffer_load_dwordx4 v29, s[48:51], s78 offen offset:3072 lds\n"
    "s_add_u32 m0, s69, 0x1c000\n"
    "s_lshl_b32 s78, s64, 14\n"
    "s_add_u32 s78, s78, 0x90000\n"
    "s_nop 0\n"
    "buffer_load_dwordx4 v26, s[48:51], s78 offen offset:0 lds\n"
    "buffer_load_dwordx4 v27, s[48:51], s78 offen offset:1024 lds\n"
    "buffer_load_dwordx4 v28, s[48:51], s78 offen offset:2048 lds\n"
    "buffer_load_dwordx4 v29, s[48:51], s78 offen offset:3072 lds\n"
    "s_add_u32 s92, s86, 0xb00\n"
    "s_addc_u32 s93, s87, 0\n"
    "s_lshl_b32 s99, s65, 8\n"
    "s_add_u32 s92, s92, s99\n"
    "s_addc_u32 s93, s93, 0\n"
    "s_cmp_lg_u32 s64, 0\n"
    "s_cbranch_scc1 .Lpoll8d\n"
    "s_mov_b32 s94, 0\n"
    ".Lpoll8:\n"
    "s_load_dword s95, s[92:93], 0x0 glc\n"
    "s_waitcnt lgkmcnt(0)\n"
    "s_cmp_ge_u32 s95, 8\n"
    "s_cbranch_scc1 .Lpoll8d\n"
    "s_add_u32 s94, s94, 1\n"
    "s_cmp_gt_u32 s94, 400000\n"
    "s_cbranch_scc1 .Lpoll8d\n"
    "s_sleep 1\n"
    "s_branch .Lpoll8\n"
    ".Lpoll8d:\n"
    "s_barrier\n"
    "s_lshl_b32 s76, s70, 2\n"
    "s_add_u32 s76, s76, s64\n"
    "s_mov_b64 s[40:41], %0\n"
    "s_add_u32 s40, s40, 0x220000\n"
    "s_addc_u32 s41, s41, 0\n"
    "s_lshl_b32 s73, s76, 18\n"
    "s_add_u32 s40, s40, s73\n"
    "s_addc_u32 s41, s41, 0\n"
    "s_lshr_b32 s73, s79, 2\n"
    "s_lshl_b32 s73, s73, 16\n"
    "s_add_u32 s40, s40, s73\n"
    "s_addc_u32 s41, s41, 0\n"
    "s_and_b32 s41, s41, 0xffff\n"
    "s_mov_b32 s42, 0x10000\n"
    "s_mov_b32 s43, 0x20000\n"
    "s_mov_b64 s[44:45], %0\n"
    "s_add_u32 s44, s44, 0x2220000\n"
    "s_addc_u32 s45, s45, 0\n"
    "s_lshr_b32 s73, s79, 2\n"
    "s_lshl_b32 s73, s73, 21\n"
    "s_add_u32 s44, s44, s73\n"
    "s_addc_u32 s45, s45, 0\n"
    "s_lshl_b32 s73, s76, 15\n"
    "s_add_u32 s44, s44, s73\n"
    "s_addc_u32 s45, s45, 0\n"
    "s_and_b32 s73, s79, 3\n"
    "s_lshl_b32 s73, s73, 13\n"
    "s_add_u32 s44, s44, s73\n"
    "s_addc_u32 s45, s45, 0\n"
    "s_and_b32 s45, s45, 0xffff\n"
    "s_mov_b32 s46, 0x2000\n"
    "s_mov_b32 s47, 0x20000\n"
    "s_mov_b32 s77, 0\n"
    "buffer_load_dwordx4 v[32:35], v2, s[40:43], s77 offen offset:0 sc1\n"
    "s_mov_b32 s77, 0\n"
    "buffer_load_dwordx4 v[36:39], v2, s[40:43], s77 offen offset:1024 sc1\n"
    "s_mov_b32 s77, 0\n"
    "buffer_load_dwordx4 v[40:43], v2, s[40:43], s77 offen offset:2048 sc1\n"
    "s_mov_b32 s77, 0\n"
    "buffer_load_dwordx4 v[44:47], v2, s[40:43], s77 offen offset:3072 sc1\n"
    "s_mov_b32 s77, 0x1000\n"
    "buffer_load_dwordx4 v[48:51], v2, s[40:43], s77 offen offset:0 sc1\n"
    "s_mov_b32 s77, 0x1000\n"
    "buffer_load_dwordx4 v[52:55], v2, s[40:43], s77 offen offset:1024 sc1\n"
    "s_mov_b32 s77, 0x1000\n"
    "buffer_load_dwordx4 v[56:59], v2, s[40:43], s77 offen offset:2048 sc1\n"
    "s_mov_b32 s77, 0x1000\n"
    "buffer_load_dwordx4 v[60:63], v2, s[40:43], s77 offen offset:3072 sc1\n"
    "s_mov_b32 s77, 0x2000\n"
    "buffer_load_dwordx4 v[64:67], v2, s[40:43], s77 offen offset:0 sc1\n"
    "s_mov_b32 s77, 0x2000\n"
    "buffer_load_dwordx4 v[68:71], v2, s[40:43], s77 offen offset:1024 sc1\n"
    "s_mov_b32 s77, 0x2000\n"
    "buffer_load_dwordx4 v[72:75], v2, s[40:43], s77 offen offset:2048 sc1\n"
    "s_mov_b32 s77, 0x2000\n"
    "buffer_load_dwordx4 v[76:79], v2, s[40:43], s77 offen offset:3072 sc1\n"
    "s_mov_b32 s77, 0x3000\n"
    "buffer_load_dwordx4 v[80:83], v2, s[40:43], s77 offen offset:0 sc1\n"
    "s_mov_b32 s77, 0x3000\n"
    "buffer_load_dwordx4 v[84:87], v2, s[40:43], s77 offen offset:1024 sc1\n"
    "s_mov_b32 s77, 0x3000\n"
    "buffer_load_dwordx4 v[88:91], v2, s[40:43], s77 offen offset:2048 sc1\n"
    "s_mul_i32 s73, s79, 0xe0\n"
    "s_mov_b64 s[52:53], %5\n"
    "s_add_u32 s52, s52, s73\n"
    "s_addc_u32 s53, s53, 0\n"
    "s_and_b32 s53, s53, 0xffff\n"
    "s_sub_u32 s54, 0x1c0000, s73\n"
    "s_mov_b32 s55, 0x20000\n"
    "s_mov_b64 s[56:57], %6\n"
    "s_add_u32 s56, s56, s73\n"
    "s_addc_u32 s57, s57, 0\n"
    "s_and_b32 s57, s57, 0xffff\n"
    "s_sub_u32 s58, 0x1c0000, s73\n"
    "s_mov_b32 s59, 0x20000\n"
    "v_lshrrev_b32 v11, 4, v1\n"
    "v_and_b32 v12, 15, v1\n"
    "v_lshlrev_b32 v13, 1, v11\n"
    "v_xor_b32 v12, v12, v13\n"
    "s_mov_b32 s74, 0xe00\n"
    "v_mul_lo_u32 v11, v11, s74\n"
    "v_xor_b32 v13, 0, v12\n"
    "v_lshl_add_u32 v4, v13, 4, v11\n"
    "v_add_u32 v5, 0x3400, v4\n"
    "v_add_u32 v6, 0x6800, v4\n"
    "v_add_u32 v7, 0x9c00, v4\n"
    "v_xor_b32 v13, 8, v12\n"
    "v_lshl_add_u32 v16, v13, 4, v11\n"
    "v_add_u32 v17, 0x3400, v16\n"
    "v_add_u32 v18, 0x6800, v16\n"
    "v_add_u32 v19, 0x9c00, v16\n"
    "ds_read_b64_tr_b16 a[0:1], v10 offset:0\n"
    "ds_read_b64_tr_b16 a[2:3], v10 offset:2048\n"
    "ds_read_b64_tr_b16 a[4:5], v10 offset:256\n"
    "ds_read_b64_tr_b16 a[6:7], v10 offset:2304\n"
    "ds_read_b64_tr_b16 a[8:9], v10 offset:512\n"
    "ds_read_b64_tr_b16 a[10:11], v10 offset:2560\n"
    "ds_read_b64_tr_b16 a[12:13], v10 offset:768\n"
    "ds_read_b64_tr_b16 a[14:15], v10 offset:2816\n"
    "s_waitcnt lgkmcnt(6)\n"
    "s_waitcnt vmcnt(14)\n"
    "v_mfma_f32_32x32x16_bf16 v[128:143], a[0:3], v[32:35], 0\n"
    "ds_read_b64_tr_b16 a[16:17], v10 offset:4096\n"
    "ds_read_b64_tr_b16 a[18:19], v10 offset:6144\n"
    "s_waitcnt lgkmcnt(6)\n"
    "v_mfma_f32_32x32x16_bf16 v[144:159], a[4:7], v[32:35], 0\n"
    "ds_read_b64_tr_b16 a[20:21], v10 offset:4352\n"
    "ds_read_b64_tr_b16 a[22:23], v10 offset:6400\n"
    "s_waitcnt lgkmcnt(6)\n"
    "v_mfma_f32_32x32x16_bf16 v[160:175], a[8:11], v[32:35], 0\n"
    "ds_read_b64_tr_b16 a[24:25], v10 offset:4608\n"
    "ds_read_b64_tr_b16 a[26:27], v10 offset:6656\n"
    "s_mov_b32 s77, 0x3000\n"
    "buffer_load_dwordx4 v[92:95], v2, s[40:43], s77 offen offset:3072 sc1\n"
    "s_waitcnt lgkmcnt(6)\n"
    "v_mfma_f32_32x32x16_bf16 v[176:191], a[12:15], v[32:35], 0\n"
    "ds_read_b64_tr_b16 a[28:29], v10 offset:4864\n"
    "ds_read_b64_tr_b16 a[30:31], v10 offset:6912\n"
    "s_waitcnt lgkmcnt(6)\n"
    "s_waitcnt vmcnt(14)\n"
    "v_mfma_f32_32x32x16_bf16 v[128:143], a[16:19], v[36:39], v[128:143]\n"
    "ds_read_b64_tr_b16 a[0:1], v10 offset:8192\n"
    "ds_read_b64_tr_b16 a[2:3], v10 offset:10240\n"
    "s_waitcnt lgkmcnt(6)\n"
    "v_mfma_f32_32x32x16_bf16 v[144:159], a[20:23], v[36:39], v[144:159]\n"
    "ds_read_b64_tr_b16 a[4:5], v10 offset:8448\n"
    "ds_read_b64_tr_b16 a[6:7], v10 offset:10496\n"
    "s_waitcnt lgkmcnt(6)\n"
    "v_mfma_f32_32x32x16_bf16 v[160:175], a[24:27], v[36:39], v[160:175]\n"
    "ds_read_b64_tr_b16 a[8:9], v10 offset:8704\n"
    "ds_read_b64_tr_b16 a[10:11], v10 offset:10752\n"
    "s_mov_b32 s77, 0x4000\n"
    "buffer_load_dwordx4 v[32:35], v2, s[40:43], s77 offen offset:0 sc1\n"
    "s_waitcnt lgkmcnt(6)\n"
    "v_mfma_f32_32x32x16_bf16 v[176:191], a[28:31], v[36:39], v[176:191]\n"
    "ds_read_b64_tr_b16 a[12:13], v10 offset:8960\n"
    "ds_read_b64_tr_b16 a[14:15], v10 offset:11008\n"
    "s_waitcnt lgkmcnt(6)\n"
    "s_waitcnt vmcnt(14)\n"
    "v_mfma_f32_32x32x16_bf16 v[128:143], a[0:3], v[40:43], v[128:143]\n"
    "ds_read_b64_tr_b16 a[16:17], v10 offset:12288\n"
    "ds_read_b64_tr_b16 a[18:19], v10 offset:14336\n"
    "s_waitcnt lgkmcnt(6)\n"
    "v_mfma_f32_32x32x16_bf16 v[144:159], a[4:7], v[40:43], v[144:159]\n"
    "ds_read_b64_tr_b16 a[20:21], v10 offset:12544\n"
    "ds_read_b64_tr_b16 a[22:23], v10 offset:14592\n"
    "s_waitcnt lgkmcnt(6)\n"
    "v_mfma_f32_32x32x16_bf16 v[160:175], a[8:11], v[40:43], v[160:175]\n"
    "ds_read_b64_tr_b16 a[24:25], v10 offset:12800\n"
    "ds_read_b64_tr_b16 a[26:27], v10 offset:14848\n"
    "s_mov_b32 s77, 0x4000\n"
    "buffer_load_dwordx4 v[36:39], v2, s[40:43], s77 offen offset:1024 sc1\n"
    "s_waitcnt lgkmcnt(6)\n"
    "v_mfma_f32_32x32x16_bf16 v[176:191], a[12:15], v[40:43], v[176:191]\n"
    "ds_read_b64_tr_b16 a[28:29], v10 offset:13056\n"
    "ds_read_b64_tr_b16 a[30:31], v10 offset:15104\n"
    "s_waitcnt lgkmcnt(6)\n"
    "s_waitcnt vmcnt(14)\n"
    "v_mfma_f32_32x32x16_bf16 v[128:143], a[16:19], v[44:47], v[128:143]\n"
    "s_barrier\n"
    "ds_read_b64_tr_b16 a[0:1], v10 offset:16384\n"
    "ds_read_b64_tr_b16 a[2:3], v10 offset:18432\n"
    "s_waitcnt lgkmcnt(6)\n"
    "v_mfma_f32_32x32x16_bf16 v[144:159], a[20:23], v[44:47], v[144:159]\n"
    "ds_read_b64_tr_b16 a[4:5], v10 offset:16640\n"
    "ds_read_b64_tr_b16 a[6:7], v10 offset:18688\n"
    "s_waitcnt lgkmcnt(6)\n"
    "v_mfma_f32_32x32x16_bf16 v[160:175], a[24:27], v[44:47], v[160:175]\n"
    "ds_read_b64_tr_b16 a[8:9], v10 offset:16896\n"
    "ds_read_b64_tr_b16 a[10:11], v10 offset:18944\n"
    "s_mov_b32 s77, 0x4000\n"
    "buffer_load_dwordx4 v[40:43], v2, s[40:43], s77 offen offset:2048 sc1\n"
    "s_waitcnt lgkmcnt(6)\n"
    "v_mfma_f32_32x32x16_bf16 v[176:191], a[28:31], v[44:47], v[176:191]\n"
    "ds_read_b64_tr_b16 a[12:13], v10 offset:17152\n"
    "ds_read_b64_tr_b16 a[14:15], v10 offset:19200\n"
    "s_waitcnt lgkmcnt(6)\n"
    "s_waitcnt vmcnt(14)\n"
    "v_mfma_f32_32x32x16_bf16 v[128:143], a[0:3], v[48:51], v[128:143]\n"
    "ds_read_b64_tr_b16 a[16:17], v10 offset:20480\n"
    "ds_read_b64_tr_b16 a[18:19], v10 offset:22528\n"
    "s_waitcnt lgkmcnt(6)\n"
    "v_mfma_f32_32x32x16_bf16 v[144:159], a[4:7], v[48:51], v[144:159]\n"
    "ds_read_b64_tr_b16 a[20:21], v10 offset:20736\n"
    "ds_read_b64_tr_b16 a[22:23], v10 offset:22784\n"
    "s_waitcnt lgkmcnt(6)\n"
    "v_mfma_f32_32x32x16_bf16 v[160:175], a[8:11], v[48:51], v[160:175]\n"
    "ds_read_b64_tr_b16 a[24:25], v10 offset:20992\n"
    "ds_read_b64_tr_b16 a[26:27], v10 offset:23040\n"
    "s_mov_b32 s77, 0x4000\n"
    "buffer_load_dwordx4 v[44:47], v2, s[40:43], s77 offen offset:3072 sc1\n"
    "s_waitcnt lgkmcnt(6)\n"
    "v_mfma_f32_32x32x16_bf16 v[176:191], a[12:15], v[48:51], v[176:191]\n"
    "ds_read_b64_tr_b16 a[28:29], v10 offset:21248\n"
    "ds_read_b64_tr_b16 a[30:31], v10 offset:23296\n"
    "s_waitcnt lgkmcnt(6)\n"
    "s_waitcnt vmcnt(14)\n"
    "v_mfma_f32_32x32x16_bf16 v[128:143], a[16:19], v[52:55], v[128:143]\n"
    "ds_read_b64_tr_b16 a[0:1], v10 offset:24576\n"
    "ds_read_b64_tr_b16 a[2:3], v10 offset:26624\n"
    "s_waitcnt lgkmcnt(6)\n"
    "v_mfma_f32_32x32x16_bf16 v[144:159], a[20:23], v[52:55], v[144:159]\n"
    "ds_read_b64_tr_b16 a[4:5], v10 offset:24832\n"
    "ds_read_b64_tr_b16 a[6:7], v10 offset:26880\n"
    "s_waitcnt lgkmcnt(6)\n"
    "v_mfma_f32_32x32x16_bf16 v[160:175], a[24:27], v[52:55], v[160:175]\n"
    "ds_read_b64_tr_b16 a[8:9], v10 offset:25088\n"
    "ds_read_b64_tr_b16 a[10:11], v10 offset:27136\n"
    "s_mov_b32 s77, 0x5000\n"
    "buffer_load_dwordx4 v[48:51], v2, s[40:43], s77 offen offset:0 sc1\n"
    "s_waitcnt lgkmcnt(6)\n"
    "v_mfma_f32_32x32x16_bf16 v[176:191], a[28:31], v[52:55], v[176:191]\n"
    "ds_read_b64_tr_b16 a[12:13], v10 offset:25344\n"
    "ds_read_b64_tr_b16 a[14:15], v10 offset:27392\n"
    "s_waitcnt lgkmcnt(6)\n"
    "s_waitcnt vmcnt(14)\n"
    "v_mfma_f32_32x32x16_bf16 v[128:143], a[0:3], v[56:59], v[128:143]\n"
    "ds_read_b64_tr_b16 a[16:17], v10 offset:28672\n"
    "ds_read_b64_tr_b16 a[18:19], v10 offset:30720\n"
    "s_waitcnt lgkmcnt(6)\n"
    "v_mfma_f32_32x32x16_bf16 v[144:159], a[4:7], v[56:59], v[144:159]\n"
    "ds_read_b64_tr_b16 a[20:21], v10 offset:28928\n"
    "ds_read_b64_tr_b16 a[22:23], v10 offset:30976\n"
    "s_waitcnt lgkmcnt(6)\n"
    "v_mfma_f32_32x32x16_bf16 v[160:175], a[8:11], v[56:59], v[160:175]\n"
    "ds_read_b64_tr_b16 a[24:25], v10 offset:29184\n"
    "ds_read_b64_tr_b16 a[26:27], v10 offset:31232\n"
    "s_mov_b32 s77, 0x5000\n"
    "buffer_load_dwordx4 v[52:55], v2, s[40:43], s77 offen offset:1024 sc1\n"
    "s_waitcnt lgkmcnt(6)\n"
    "v_mfma_f32_32x32x16_bf16 v[176:191], a[12:15], v[56:59], v[176:191]\n"
    "ds_read_b64_tr_b16 a[28:29], v10 offset:29440\n"
    "ds_read_b64_tr_b16 a[30:31], v10 offset:31488\n"
    "s_waitcnt lgkmcnt(6)\n"
    "s_waitcnt vmcnt(14)\n"
    "v_mfma_f32_32x32x16_bf16 v[128:143], a[16:19], v[60:63], v[128:143]\n"
    "s_barrier\n"
    "ds_read_b64_tr_b16 a[0:1], v8 offset:0\n"
    "ds_read_b64_tr_b16 a[2:3], v8 offset:2048\n"
    "s_add_u32 m0, s69, 0x20000\n"
    "s_lshl_b32 s78, s64, 14\n"
    "s_add_u32 s78, s78, 0xa0000\n"
    "s_waitcnt lgkmcnt(6)\n"
    "v_mfma_f32_32x32x16_bf16 v[144:159], a[20:23], v[60:63], v[144:159]\n"
    "ds_read_b64_tr_b16 a[4:5], v8 offset:256\n"
    "ds_read_b64_tr_b16 a[6:7], v8 offset:2304\n"
    "buffer_load_dwordx4 v26, s[48:51], s78 offen offset:0 lds\n"
    "buffer_load_dwordx4 v27, s[48:51], s78 offen offset:1024 lds\n"
    "s_waitcnt lgkmcnt(6)\n"
    "v_mfma_f32_32x32x16_bf16 v[160:175], a[24:27], v[60:63], v[160:175]\n"
    "ds_read_b64_tr_b16 a[8:9], v8 offset:512\n"
    "ds_read_b64_tr_b16 a[10:11], v8 offset:2560\n"
    "s_mov_b32 s77, 0x5000\n"
    "buffer_load_dwordx4 v[56:59], v2, s[40:43], s77 offen offset:2048 sc1\n"
    "s_waitcnt lgkmcnt(6)\n"
    "v_mfma_f32_32x32x16_bf16 v[176:191], a[28:31], v[60:63], v[176:191]\n"
    "buffer_load_dwordx4 v29, s[48:51], s78 offen offset:3072 lds\n"
    "buffer_load_dwordx4 v28, s[48:51], s78 offen offset:2048 lds\n"
    "ds_read_b64_tr_b16 a[12:13], v8 offset:768\n"
    "ds_read_b64_tr_b16 a[14:15], v8 offset:2816\n"
    "s_waitcnt lgkmcnt(6)\n"
    "s_waitcnt vmcnt(18)\n"
    "v_mfma_f32_32x32x16_bf16 v[128:143], a[0:3], v[64:67], v[128:143]\n"
    "ds_read_b64_tr_b16 a[16:17], v8 offset:4096\n"
    "ds_read_b64_tr_b16 a[18:19], v8 offset:6144\n"
    "s_waitcnt lgkmcnt(6)\n"
    "v_mfma_f32_32x32x16_bf16 v[144:159], a[4:7], v[64:67], v[144:159]\n"
    "ds_read_b64_tr_b16 a[20:21], v8 offset:4352\n"
    "ds_read_b64_tr_b16 a[22:23], v8 offset:6400\n"
    "s_waitcnt lgkmcnt(6)\n"
    "v_mfma_f32_32x32x16_bf16 v[160:175], a[8:11], v[64:67], v[160:175]\n"
    "ds_read_b64_tr_b16 a[24:25], v8 offset:4608\n"
    "ds_read_b64_tr_b16 a[26:27], v8 offset:6656\n"
    "s_mov_b32 s77, 0x5000\n"
    "buffer_load_dwordx4 v[60:63], v2, s[40:43], s77 offen offset:3072 sc1\n"
    "s_waitcnt lgkmcnt(6)\n"
    "v_mfma_f32_32x32x16_bf16 v[176:191], a[12:15], v[64:67], v[176:191]\n"
    "ds_read_b64_tr_b16 a[28:29], v8 offset:4864\n"
    "ds_read_b64_tr_b16 a[30:31], v8 offset:6912\n"
    "s_waitcnt lgkmcnt(6)\n"
    "s_waitcnt vmcnt(18)\n"
    "v_mfma_f32_32x32x16_bf16 v[128:143], a[16:19], v[68:71], v[128:143]\n"
    "ds_read_b64_tr_b16 a[0:1], v8 offset:8192\n"
    "ds_read_b64_tr_b16 a[2:3], v8 offset:10240\n"
    "s_waitcnt lgkmcnt(6)\n"
    "v_mfma_f32_32x32x16_bf16 v[144:159], a[20:23], v[68:71], v[144:159]\n"
    "ds_read_b64_tr_b16 a[4:5], v8 offset:8448\n"
    "ds_read_b64_tr_b16 a[6:7], v8 offset:10496\n"
    "s_waitcnt lgkmcnt(6)\n"
    "v_mfma_f32_32x32x16_bf16 v[160:175], a[24:27], v[68:71], v[160:175]\n"
    "ds_read_b64_tr_b16 a[8:9], v8 offset:8704\n"
    "ds_read_b64_tr_b16 a[10:11], v8 offset:10752\n"
    "s_mov_b32 s77, 0x6000\n"
    "buffer_load_dwordx4 v[64:67], v2, s[40:43], s77 offen offset:0 sc1\n"
    "s_waitcnt lgkmcnt(6)\n"
    "v_mfma_f32_32x32x16_bf16 v[176:191], a[28:31], v[68:71], v[176:191]\n"
    "ds_read_b64_tr_b16 a[12:13], v8 offset:8960\n"
    "ds_read_b64_tr_b16 a[14:15], v8 offset:11008\n"
    "s_waitcnt lgkmcnt(6)\n"
    "s_waitcnt vmcnt(18)\n"
    "v_mfma_f32_32x32x16_bf16 v[128:143], a[0:3], v[72:75], v[128:143]\n"
    "ds_read_b64_tr_b16 a[16:17], v8 offset:12288\n"
    "ds_read_b64_tr_b16 a[18:19], v8 offset:14336\n"
    "s_waitcnt lgkmcnt(6)\n"
    "v_mfma_f32_32x32x16_bf16 v[144:159], a[4:7], v[72:75], v[144:159]\n"
    "ds_read_b64_tr_b16 a[20:21], v8 offset:12544\n"
    "ds_read_b64_tr_b16 a[22:23], v8 offset:14592\n"
    "s_waitcnt lgkmcnt(6)\n"
    "v_mfma_f32_32x32x16_bf16 v[160:175], a[8:11], v[72:75], v[160:175]\n"
    "ds_read_b64_tr_b16 a[24:25], v8 offset:12800\n"
    "ds_read_b64_tr_b16 a[26:27], v8 offset:14848\n"
    "s_mov_b32 s77, 0x6000\n"
    "buffer_load_dwordx4 v[68:71], v2, s[40:43], s77 offen offset:1024 sc1\n"
    "s_waitcnt lgkmcnt(6)\n"
    "v_mfma_f32_32x32x16_bf16 v[176:191], a[12:15], v[72:75], v[176:191]\n"
    "ds_read_b64_tr_b16 a[28:29], v8 offset:13056\n"
    "ds_read_b64_tr_b16 a[30:31], v8 offset:15104\n"
    "s_waitcnt lgkmcnt(6)\n"
    "s_waitcnt vmcnt(18)\n"
    "v_mfma_f32_32x32x16_bf16 v[128:143], a[16:19], v[76:79], v[128:143]\n"
    "s_barrier\n"
    "ds_read_b64_tr_b16 a[0:1], v8 offset:16384\n"
    "ds_read_b64_tr_b16 a[2:3], v8 offset:18432\n"
    "s_add_u32 m0, s69, 0x24000\n"
    "s_lshl_b32 s78, s64, 14\n"
    "s_add_u32 s78, s78, 0xb0000\n"
    "s_waitcnt lgkmcnt(6)\n"
    "v_mfma_f32_32x32x16_bf16 v[144:159], a[20:23], v[76:79], v[144:159]\n"
    "ds_read_b64_tr_b16 a[4:5], v8 offset:16640\n"
    "ds_read_b64_tr_b16 a[6:7], v8 offset:18688\n"
    "buffer_load_dwordx4 v26, s[48:51], s78 offen offset:0 lds\n"
    "buffer_load_dwordx4 v27, s[48:51], s78 offen offset:1024 lds\n"
    "s_waitcnt lgkmcnt(6)\n"
    "v_mfma_f32_32x32x16_bf16 v[160:175], a[24:27], v[76:79], v[160:175]\n"
    "ds_read_b64_tr_b16 a[8:9], v8 offset:16896\n"
    "ds_read_b64_tr_b16 a[10:11], v8 offset:18944\n"
    "s_mov_b32 s77, 0x6000\n"
    "buffer_load_dwordx4 v[72:75], v2, s[40:43], s77 offen offset:2048 sc1\n"
    "s_waitcnt lgkmcnt(6)\n"
    "v_mfma_f32_32x32x16_bf16 v[176:191], a[28:31], v[76:79], v[176:191]\n"
    "buffer_load_dwordx4 v29, s[48:51], s78 offen offset:3072 lds\n"
    "buffer_load_dwordx4 v28, s[48:51], s78 offen offset:2048 lds\n"
    "ds_read_b64_tr_b16 a[12:13], v8 offset:17152\n"
    "ds_read_b64_tr_b16 a[14:15], v8 offset:19200\n"
    "s_waitcnt lgkmcnt(6)\n"
    "s_waitcnt vmcnt(22)\n"
    "v_mfma_f32_32x32x16_bf16 v[128:143], a[0:3], v[80:83], v[128:143]\n"
    "ds_read_b64_tr_b16 a[16:17], v8 offset:20480\n"
    "ds_read_b64_tr_b16 a[18:19], v8 offset:22528\n"
    "s_waitcnt lgkmcnt(6)\n"
    "v_mfma_f32_32x32x16_bf16 v[144:159], a[4:7], v[80:83], v[144:159]\n"
    "ds_read_b64_tr_b16 a[20:21], v8 offset:20736\n"
    "ds_read_b64_tr_b16 a[22:23], v8 offset:22784\n"
    "s_waitcnt lgkmcnt(6)\n"
    "v_mfma_f32_32x32x16_bf16 v[160:175], a[8:11], v[80:83], v[160:175]\n"
    "ds_read_b64_tr_b16 a[24:25], v8 offset:20992\n"
    "ds_read_b64_tr_b16 a[26:27], v8 offset:23040\n"
    "s_mov_b32 s77, 0x6000\n"
    "buffer_load_dwordx4 v[76:79], v2, s[40:43], s77 offen offset:3072 sc1\n"
    "s_waitcnt lgkmcnt(6)\n"
    "v_mfma_f32_32x32x16_bf16 v[176:191], a[12:15], v[80:83], v[176:191]\n"
    "ds_read_b64_tr_b16 a[28:29], v8 offset:21248\n"
    "ds_read_b64_tr_b16 a[30:31], v8 offset:23296\n"
    "s_waitcnt lgkmcnt(6)\n"
    "s_waitcnt vmcnt(22)\n"
    "v_mfma_f32_32x32x16_bf16 v[128:143], a[16:19], v[84:87], v[128:143]\n"
    "ds_read_b64_tr_b16 a[0:1], v8 offset:24576\n"
    "ds_read_b64_tr_b16 a[2:3], v8 offset:26624\n"
    "s_waitcnt lgkmcnt(6)\n"
    "v_mfma_f32_32x32x16_bf16 v[144:159], a[20:23], v[84:87], v[144:159]\n"
    "ds_read_b64_tr_b16 a[4:5], v8 offset:24832\n"
    "ds_read_b64_tr_b16 a[6:7], v8 offset:26880\n"
    "s_waitcnt lgkmcnt(6)\n"
    "v_mfma_f32_32x32x16_bf16 v[160:175], a[24:27], v[84:87], v[160:175]\n"
    "ds_read_b64_tr_b16 a[8:9], v8 offset:25088\n"
    "ds_read_b64_tr_b16 a[10:11], v8 offset:27136\n"
    "s_mov_b32 s77, 0x7000\n"
    "buffer_load_dwordx4 v[80:83], v2, s[40:43], s77 offen offset:0 sc1\n"
    "s_waitcnt lgkmcnt(6)\n"
    "v_mfma_f32_32x32x16_bf16 v[176:191], a[28:31], v[84:87], v[176:191]\n"
    "ds_read_b64_tr_b16 a[12:13], v8 offset:25344\n"
    "ds_read_b64_tr_b16 a[14:15], v8 offset:27392\n"
    "s_waitcnt lgkmcnt(6)\n"
    "s_waitcnt vmcnt(22)\n"
    "v_mfma_f32_32x32x16_bf16 v[128:143], a[0:3], v[88:91], v[128:143]\n"
    "ds_read_b64_tr_b16 a[16:17], v8 offset:28672\n"
    "ds_read_b64_tr_b16 a[18:19], v8 offset:30720\n"
    "s_waitcnt lgkmcnt(6)\n"
    "v_mfma_f32_32x32x16_bf16 v[144:159], a[4:7], v[88:91], v[144:159]\n"
    "ds_read_b64_tr_b16 a[20:21], v8 offset:28928\n"
    "ds_read_b64_tr_b16 a[22:23], v8 offset:30976\n"
    "s_waitcnt lgkmcnt(6)\n"
    "v_mfma_f32_32x32x16_bf16 v[160:175], a[8:11], v[88:91], v[160:175]\n"
    "ds_read_b64_tr_b16 a[24:25], v8 offset:29184\n"
    "ds_read_b64_tr_b16 a[26:27], v8 offset:31232\n"
    "s_mov_b32 s77, 0x7000\n"
    "buffer_load_dwordx4 v[84:87], v2, s[40:43], s77 offen offset:1024 sc1\n"
    "s_waitcnt lgkmcnt(6)\n"
    "v_mfma_f32_32x32x16_bf16 v[176:191], a[12:15], v[88:91], v[176:191]\n"
    "ds_read_b64_tr_b16 a[28:29], v8 offset:29440\n"
    "ds_read_b64_tr_b16 a[30:31], v8 offset:31488\n"
    "s_waitcnt lgkmcnt(6)\n"
    "s_waitcnt vmcnt(22)\n"
    "v_mfma_f32_32x32x16_bf16 v[128:143], a[16:19], v[92:95], v[128:143]\n"
    "s_barrier\n"
    "ds_read_b64_tr_b16 a[0:1], v9 offset:0\n"
    "ds_read_b64_tr_b16 a[2:3], v9 offset:2048\n"
    "s_add_u32 m0, s69, 0\n"
    "s_lshl_b32 s78, s64, 14\n"
    "s_add_u32 s78, s78, 0xc0000\n"
    "s_waitcnt lgkmcnt(6)\n"
    "v_mfma_f32_32x32x16_bf16 v[144:159], a[20:23], v[92:95], v[144:159]\n"
    "ds_read_b64_tr_b16 a[4:5], v9 offset:256\n"
    "ds_read_b64_tr_b16 a[6:7], v9 offset:2304\n"
    "buffer_load_dwordx4 v26, s[48:51], s78 offen offset:0 lds\n"
    "buffer_load_dwordx4 v27, s[48:51], s78 offen offset:1024 lds\n"
    "s_waitcnt lgkmcnt(6)\n"
    "v_mfma_f32_32x32x16_bf16 v[160:175], a[24:27], v[92:95], v[160:175]\n"
    "ds_read_b64_tr_b16 a[8:9], v9 offset:512\n"
    "ds_read_b64_tr_b16 a[10:11], v9 offset:2560\n"
    "s_mov_b32 s77, 0x7000\n"
    "buffer_load_dwordx4 v[88:91], v2, s[40:43], s77 offen offset:2048 sc1\n"
    "s_waitcnt lgkmcnt(6)\n"
    "v_mfma_f32_32x32x16_bf16 v[176:191], a[28:31], v[92:95], v[176:191]\n"
    "buffer_load_dwordx4 v29, s[48:51], s78 offen offset:3072 lds\n"
    "buffer_load_dwordx4 v28, s[48:51], s78 offen offset:2048 lds\n"
    "ds_read_b64_tr_b16 a[12:13], v9 offset:768\n"
    "ds_read_b64_tr_b16 a[14:15], v9 offset:2816\n"
    "s_waitcnt lgkmcnt(6)\n"
    "s_waitcnt vmcnt(26)\n"
    "v_mfma_f32_32x32x16_bf16 v[128:143], a[0:3], v[32:35], v[128:143]\n"
    "ds_read_b64_tr_b16 a[16:17], v9 offset:4096\n"
    "ds_read_b64_tr_b16 a[18:19], v9 offset:6144\n"
    "s_waitcnt lgkmcnt(6)\n"
    "v_mfma_f32_32x32x16_bf16 v[144:159], a[4:7], v[32:35], v[144:159]\n"
    "ds_read_b64_tr_b16 a[20:21], v9 offset:4352\n"
    "ds_read_b64_tr_b16 a[22:23], v9 offset:6400\n"
    "s_waitcnt lgkmcnt(6)\n"
    "v_mfma_f32_32x32x16_bf16 v[160:175], a[8:11], v[32:35], v[160:175]\n"
    "ds_read_b64_tr_b16 a[24:25], v9 offset:4608\n"
    "ds_read_b64_tr_b16 a[26:27], v9 offset:6656\n"
    "s_mov_b32 s77, 0x7000\n"
    "buffer_load_dwordx4 v[92:95], v2, s[40:43], s77 offen offset:3072 sc1\n"
    "s_waitcnt lgkmcnt(6)\n"
    "v_mfma_f32_32x32x16_bf16 v[176:191], a[12:15], v[32:35], v[176:191]\n"
    "ds_read_b64_tr_b16 a[28:29], v9 offset:4864\n"
    "ds_read_b64_tr_b16 a[30:31], v9 offset:6912\n"
    "s_waitcnt lgkmcnt(6)\n"
    "s_waitcnt vmcnt(26)\n"
    "v_mfma_f32_32x32x16_bf16 v[128:143], a[16:19], v[36:39], v[128:143]\n"
    "ds_read_b64_tr_b16 a[0:1], v9 offset:8192\n"
    "ds_read_b64_tr_b16 a[2:3], v9 offset:10240\n"
    "s_waitcnt lgkmcnt(6)\n"
    "v_mfma_f32_32x32x16_bf16 v[144:159], a[20:23], v[36:39], v[144:159]\n"
    "ds_read_b64_tr_b16 a[4:5], v9 offset:8448\n"
    "ds_read_b64_tr_b16 a[6:7], v9 offset:10496\n"
    "s_waitcnt lgkmcnt(6)\n"
    "v_mfma_f32_32x32x16_bf16 v[160:175], a[24:27], v[36:39], v[160:175]\n"
    "ds_read_b64_tr_b16 a[8:9], v9 offset:8704\n"
    "ds_read_b64_tr_b16 a[10:11], v9 offset:10752\n"
    "s_mov_b32 s77, 0x8000\n"
    "buffer_load_dwordx4 v[32:35], v2, s[40:43], s77 offen offset:0 sc1\n"
    "s_waitcnt lgkmcnt(6)\n"
    "v_mfma_f32_32x32x16_bf16 v[176:191], a[28:31], v[36:39], v[176:191]\n"
    "ds_read_b64_tr_b16 a[12:13], v9 offset:8960\n"
    "ds_read_b64_tr_b16 a[14:15], v9 offset:11008\n"
    "s_waitcnt lgkmcnt(6)\n"
    "s_waitcnt vmcnt(26)\n"
    "v_mfma_f32_32x32x16_bf16 v[128:143], a[0:3], v[40:43], v[128:143]\n"
    "ds_read_b64_tr_b16 a[16:17], v9 offset:12288\n"
    "ds_read_b64_tr_b16 a[18:19], v9 offset:14336\n"
    "s_waitcnt lgkmcnt(6)\n"
    "v_mfma_f32_32x32x16_bf16 v[144:159], a[4:7], v[40:43], v[144:159]\n"
    "ds_read_b64_tr_b16 a[20:21], v9 offset:12544\n"
    "ds_read_b64_tr_b16 a[22:23], v9 offset:14592\n"
    "s_waitcnt lgkmcnt(6)\n"
    "v_mfma_f32_32x32x16_bf16 v[160:175], a[8:11], v[40:43], v[160:175]\n"
    "ds_read_b64_tr_b16 a[24:25], v9 offset:12800\n"
    "ds_read_b64_tr_b16 a[26:27], v9 offset:14848\n"
    "s_mov_b32 s77, 0x8000\n"
    "buffer_load_dwordx4 v[36:39], v2, s[40:43], s77 offen offset:1024 sc1\n"
    "s_waitcnt lgkmcnt(6)\n"
    "v_mfma_f32_32x32x16_bf16 v[176:191], a[12:15], v[40:43], v[176:191]\n"
    "ds_read_b64_tr_b16 a[28:29], v9 offset:13056\n"
    "ds_read_b64_tr_b16 a[30:31], v9 offset:15104\n"
    "s_waitcnt lgkmcnt(6)\n"
    "s_waitcnt vmcnt(26)\n"
    "v_mfma_f32_32x32x16_bf16 v[128:143], a[16:19], v[44:47], v[128:143]\n"
    "s_barrier\n"
    "ds_read_b64_tr_b16 a[0:1], v8 offset:32768\n"
    "ds_read_b64_tr_b16 a[2:3], v8 offset:34816\n"
    "s_add_u32 m0, s69, 0x4000\n"
    "s_lshl_b32 s78, s64, 14\n"
    "s_add_u32 s78, s78, 0xd0000\n"
    "s_waitcnt lgkmcnt(6)\n"
    "v_mfma_f32_32x32x16_bf16 v[144:159], a[20:23], v[44:47], v[144:159]\n"
    "ds_read_b64_tr_b16 a[4:5], v8 offset:33024\n"
    "ds_read_b64_tr_b16 a[6:7], v8 offset:35072\n"
    "buffer_load_dwordx4 v26, s[48:51], s78 offen offset:0 lds\n"
    "buffer_load_dwordx4 v27, s[48:51], s78 offen offset:1024 lds\n"
    "s_waitcnt lgkmcnt(6)\n"
    "v_mfma_f32_32x32x16_bf16 v[160:175], a[24:27], v[44:47], v[160:175]\n"
    "ds_read_b64_tr_b16 a[8:9], v8 offset:33280\n"
    "ds_read_b64_tr_b16 a[10:11], v8 offset:35328\n"
    "s_mov_b32 s77, 0x8000\n"
    "buffer_load_dwordx4 v[40:43], v2, s[40:43], s77 offen offset:2048 sc1\n"
    "s_waitcnt lgkmcnt(6)\n"
    "v_mfma_f32_32x32x16_bf16 v[176:191], a[28:31], v[44:47], v[176:191]\n"
    "buffer_load_dwordx4 v29, s[48:51], s78 offen offset:3072 lds\n"
    "buffer_load_dwordx4 v28, s[48:51], s78 offen offset:2048 lds\n"
    "ds_read_b64_tr_b16 a[12:13], v8 offset:33536\n"
    "ds_read_b64_tr_b16 a[14:15], v8 offset:35584\n"
    "s_waitcnt lgkmcnt(6)\n"
    "s_waitcnt vmcnt(30)\n"
    "v_mfma_f32_32x32x16_bf16 v[128:143], a[0:3], v[48:51], v[128:143]\n"
    "ds_read_b64_tr_b16 a[16:17], v8 offset:36864\n"
    "ds_read_b64_tr_b16 a[18:19], v8 offset:38912\n"
    "s_waitcnt lgkmcnt(6)\n"
    "v_mfma_f32_32x32x16_bf16 v[144:159], a[4:7], v[48:51], v[144:159]\n"
    "ds_read_b64_tr_b16 a[20:21], v8 offset:37120\n"
    "ds_read_b64_tr_b16 a[22:23], v8 offset:39168\n"
    "s_waitcnt lgkmcnt(6)\n"
    "v_mfma_f32_32x32x16_bf16 v[160:175], a[8:11], v[48:51], v[160:175]\n"
    "ds_read_b64_tr_b16 a[24:25], v8 offset:37376\n"
    "ds_read_b64_tr_b16 a[26:27], v8 offset:39424\n"
    "s_mov_b32 s77, 0x8000\n"
    "buffer_load_dwordx4 v[44:47], v2, s[40:43], s77 offen offset:3072 sc1\n"
    "s_waitcnt lgkmcnt(6)\n"
    "v_mfma_f32_32x32x16_bf16 v[176:191], a[12:15], v[48:51], v[176:191]\n"
    "ds_read_b64_tr_b16 a[28:29], v8 offset:37632\n"
    "ds_read_b64_tr_b16 a[30:31], v8 offset:39680\n"
    "s_waitcnt lgkmcnt(6)\n"
    "s_waitcnt vmcnt(30)\n"
    "v_mfma_f32_32x32x16_bf16 v[128:143], a[16:19], v[52:55], v[128:143]\n"
    "ds_read_b64_tr_b16 a[0:1], v8 offset:40960\n"
    "ds_read_b64_tr_b16 a[2:3], v8 offset:43008\n"
    "s_waitcnt lgkmcnt(6)\n"
    "v_mfma_f32_32x32x16_bf16 v[144:159], a[20:23], v[52:55], v[144:159]\n"
    "ds_read_b64_tr_b16 a[4:5], v8 offset:41216\n"
    "ds_read_b64_tr_b16 a[6:7], v8 offset:43264\n"
    "s_waitcnt lgkmcnt(6)\n"
    "v_mfma_f32_32x32x16_bf16 v[160:175], a[24:27], v[52:55], v[160:175]\n"
    "ds_read_b64_tr_b16 a[8:9], v8 offset:41472\n"
    "ds_read_b64_tr_b16 a[10:11], v8 offset:43520\n"
    "s_mov_b32 s77, 0x9000\n"
    "buffer_load_dwordx4 v[48:51], v2, s[40:43], s77 offen offset:0 sc1\n"
    "s_waitcnt lgkmcnt(6)\n"
    "v_mfma_f32_32x32x16_bf16 v[176:191], a[28:31], v[52:55], v[176:191]\n"
    "ds_read_b64_tr_b16 a[12:13], v8 offset:41728\n"
    "ds_read_b64_tr_b16 a[14:15], v8 offset:43776\n"
    "s_waitcnt lgkmcnt(6)\n"
    "s_waitcnt vmcnt(28)\n"
    "v_mfma_f32_32x32x16_bf16 v[128:143], a[0:3], v[56:59], v[128:143]\n"
    "ds_read_b64_tr_b16 a[16:17], v8 offset:45056\n"
    "ds_read_b64_tr_b16 a[18:19], v8 offset:47104\n"
    "s_waitcnt lgkmcnt(6)\n"
    "v_mfma_f32_32x32x16_bf16 v[144:159], a[4:7], v[56:59], v[144:159]\n"
    "ds_read_b64_tr_b16 a[20:21], v8 offset:45312\n"
    "ds_read_b64_tr_b16 a[22:23], v8 offset:47360\n"
    "s_waitcnt lgkmcnt(6)\n"
    "v_mfma_f32_32x32x16_bf16 v[160:175], a[8:11], v[56:59], v[160:175]\n"
    "ds_read_b64_tr_b16 a[24:25], v8 offset:45568\n"
    "ds_read_b64_tr_b16 a[26:27], v8 offset:47616\n"
    "s_mov_b32 s77, 0x9000\n"
    "buffer_load_dwordx4 v[52:55], v2, s[40:43], s77 offen offset:1024 sc1\n"
    "s_waitcnt lgkmcnt(6)\n"
    "v_mfma_f32_32x32x16_bf16 v[176:191], a[12:15], v[56:59], v[176:191]\n"
    "ds_read_b64_tr_b16 a[28:29], v8 offset:45824\n"
    "ds_read_b64_tr_b16 a[30:31], v8 offset:47872\n"
    "s_waitcnt lgkmcnt(6)\n"
    "s_waitcnt vmcnt(26)\n"
    "v_mfma_f32_32x32x16_bf16 v[128:143], a[16:19], v[60:63], v[128:143]\n"
    "s_barrier\n"
    "ds_read_b64_tr_b16 a[0:1], v9 offset:16384\n"
    "ds_read_b64_tr_b16 a[2:3], v9 offset:18432\n"
    "s_waitcnt lgkmcnt(6)\n"
    "v_mfma_f32_32x32x16_bf16 v[144:159], a[20:23], v[60:63], v[144:159]\n"
    "ds_read_b64_tr_b16 a[4:5], v9 offset:16640\n"
    "ds_read_b64_tr_b16 a[6:7], v9 offset:18688\n"
    "s_waitcnt lgkmcnt(6)\n"
    "v_mfma_f32_32x32x16_bf16 v[160:175], a[24:27], v[60:63], v[160:175]\n"
    "ds_read_b64_tr_b16 a[8:9], v9 offset:16896\n"
    "ds_read_b64_tr_b16 a[10:11], v9 offset:18944\n"
    "s_mov_b32 s77, 0x9000\n"
    "buffer_load_dwordx4 v[56:59], v2, s[40:43], s77 offen offset:2048 sc1\n"
    "s_waitcnt lgkmcnt(6)\n"
    "v_mfma_f32_32x32x16_bf16 v[176:191], a[28:31], v[60:63], v[176:191]\n"
    "ds_read_b64_tr_b16 a[12:13], v9 offset:17152\n"
    "ds_read_b64_tr_b16 a[14:15], v9 offset:19200\n"
    "s_waitcnt lgkmcnt(6)\n"
    "s_waitcnt vmcnt(26)\n"
    "v_mfma_f32_32x32x16_bf16 v[128:143], a[0:3], v[64:67], v[128:143]\n"
    "ds_read_b64_tr_b16 a[16:17], v9 offset:20480\n"
    "ds_read_b64_tr_b16 a[18:19], v9 offset:22528\n"
    "s_waitcnt lgkmcnt(6)\n"
    "v_mfma_f32_32x32x16_bf16 v[144:159], a[4:7], v[64:67], v[144:159]\n"
    "ds_read_b64_tr_b16 a[20:21], v9 offset:20736\n"
    "ds_read_b64_tr_b16 a[22:23], v9 offset:22784\n"
    "s_waitcnt lgkmcnt(6)\n"
    "v_mfma_f32_32x32x16_bf16 v[160:175], a[8:11], v[64:67], v[160:175]\n"
    "ds_read_b64_tr_b16 a[24:25], v9 offset:20992\n"
    "ds_read_b64_tr_b16 a[26:27], v9 offset:23040\n"
    "s_mov_b32 s77, 0x9000\n"
    "buffer_load_dwordx4 v[60:63], v2, s[40:43], s77 offen offset:3072 sc1\n"
    "s_waitcnt lgkmcnt(6)\n"
    "v_mfma_f32_32x32x16_bf16 v[176:191], a[12:15], v[64:67], v[176:191]\n"
    "ds_read_b64_tr_b16 a[28:29], v9 offset:21248\n"
    "ds_read_b64_tr_b16 a[30:31], v9 offset:23296\n"
    "s_waitcnt lgkmcnt(6)\n"
    "s_waitcnt vmcnt(26)\n"
    "v_mfma_f32_32x32x16_bf16 v[128:143], a[16:19], v[68:71], v[128:143]\n"
    "ds_read_b64_tr_b16 a[0:1], v9 offset:24576\n"
    "ds_read_b64_tr_b16 a[2:3], v9 offset:26624\n"
    "s_waitcnt lgkmcnt(6)\n"
    "v_mfma_f32_32x32x16_bf16 v[144:159], a[20:23], v[68:71], v[144:159]\n"
    "ds_read_b64_tr_b16 a[4:5], v9 offset:24832\n"
    "ds_read_b64_tr_b16 a[6:7], v9 offset:26880\n"
    "s_waitcnt lgkmcnt(6)\n"
    "v_mfma_f32_32x32x16_bf16 v[160:175], a[24:27], v[68:71], v[160:175]\n"
    "ds_read_b64_tr_b16 a[8:9], v9 offset:25088\n"
    "ds_read_b64_tr_b16 a[10:11], v9 offset:27136\n"
    "s_mov_b32 s77, 0xa000\n"
    "buffer_load_dwordx4 v[64:67], v2, s[40:43], s77 offen offset:0 sc1\n"
    "s_waitcnt lgkmcnt(6)\n"
    "v_mfma_f32_32x32x16_bf16 v[176:191], a[28:31], v[68:71], v[176:191]\n"
    "ds_read_b64_tr_b16 a[12:13], v9 offset:25344\n"
    "ds_read_b64_tr_b16 a[14:15], v9 offset:27392\n"
    "s_waitcnt lgkmcnt(6)\n"
    "s_waitcnt vmcnt(24)\n"
    "v_mfma_f32_32x32x16_bf16 v[128:143], a[0:3], v[72:75], v[128:143]\n"
    "ds_read_b64_tr_b16 a[16:17], v9 offset:28672\n"
    "ds_read_b64_tr_b16 a[18:19], v9 offset:30720\n"
    "s_waitcnt lgkmcnt(6)\n"
    "v_mfma_f32_32x32x16_bf16 v[144:159], a[4:7], v[72:75], v[144:159]\n"
    "ds_read_b64_tr_b16 a[20:21], v9 offset:28928\n"
    "ds_read_b64_tr_b16 a[22:23], v9 offset:30976\n"
    "s_waitcnt lgkmcnt(6)\n"
    "v_mfma_f32_32x32x16_bf16 v[160:175], a[8:11], v[72:75], v[160:175]\n"
    "ds_read_b64_tr_b16 a[24:25], v9 offset:29184\n"
    "ds_read_b64_tr_b16 a[26:27], v9 offset:31232\n"
    "s_mov_b32 s77, 0xa000\n"
    "buffer_load_dwordx4 v[68:71], v2, s[40:43], s77 offen offset:1024 sc1\n"
    "s_waitcnt lgkmcnt(6)\n"
    "v_mfma_f32_32x32x16_bf16 v[176:191], a[12:15], v[72:75], v[176:191]\n"
    "ds_read_b64_tr_b16 a[28:29], v9 offset:29440\n"
    "ds_read_b64_tr_b16 a[30:31], v9 offset:31488\n"
    "s_waitcnt lgkmcnt(6)\n"
    "s_waitcnt vmcnt(22)\n"
    "v_mfma_f32_32x32x16_bf16 v[128:143], a[16:19], v[76:79], v[128:143]\n"
    "s_barrier\n"
    "ds_read_b64_tr_b16 a[0:1], v8 offset:49152\n"
    "ds_read_b64_tr_b16 a[2:3], v8 offset:51200\n"
    "s_waitcnt lgkmcnt(6)\n"
    "v_mfma_f32_32x32x16_bf16 v[144:159], a[20:23], v[76:79], v[144:159]\n"
    "ds_read_b64_tr_b16 a[4:5], v8 offset:49408\n"
    "ds_read_b64_tr_b16 a[6:7], v8 offset:51456\n"
    "s_waitcnt lgkmcnt(6)\n"
    "v_mfma_f32_32x32x16_bf16 v[160:175], a[24:27], v[76:79], v[160:175]\n"
    "ds_read_b64_tr_b16 a[8:9], v8 offset:49664\n"
    "ds_read_b64_tr_b16 a[10:11], v8 offset:51712\n"
    "s_mov_b32 s77, 0xa000\n"
    "buffer_load_dwordx4 v[72:75], v2, s[40:43], s77 offen offset:2048 sc1\n"
    "s_waitcnt lgkmcnt(6)\n"
    "v_mfma_f32_32x32x16_bf16 v[176:191], a[28:31], v[76:79], v[176:191]\n"
    "ds_read_b64_tr_b16 a[12:13], v8 offset:49920\n"
    "ds_read_b64_tr_b16 a[14:15], v8 offset:51968\n"
    "s_waitcnt lgkmcnt(6)\n"
    "s_waitcnt vmcnt(22)\n"
    "v_mfma_f32_32x32x16_bf16 v[128:143], a[0:3], v[80:83], v[128:143]\n"
    "ds_read_b64_tr_b16 a[16:17], v8 offset:53248\n"
    "ds_read_b64_tr_b16 a[18:19], v8 offset:55296\n"
    "s_waitcnt lgkmcnt(6)\n"
    "v_mfma_f32_32x32x16_bf16 v[144:159], a[4:7], v[80:83], v[144:159]\n"
    "ds_read_b64_tr_b16 a[20:21], v8 offset:53504\n"
    "ds_read_b64_tr_b16 a[22:23], v8 offset:55552\n"
    "s_waitcnt lgkmcnt(6)\n"
    "v_mfma_f32_32x32x16_bf16 v[160:175], a[8:11], v[80:83], v[160:175]\n"
    "ds_read_b64_tr_b16 a[24:25], v8 offset:53760\n"
    "ds_read_b64_tr_b16 a[26:27], v8 offset:55808\n"
    "s_mov_b32 s77, 0xa000\n"
    "buffer_load_dwordx4 v[76:79], v2, s[40:43], s77 offen offset:3072 sc1\n"
    "s_waitcnt lgkmcnt(6)\n"
    "v_mfma_f32_32x32x16_bf16 v[176:191], a[12:15], v[80:83], v[176:191]\n"
    "ds_read_b64_tr_b16 a[28:29], v8 offset:54016\n"
    "ds_read_b64_tr_b16 a[30:31], v8 offset:56064\n"
    "s_waitcnt lgkmcnt(6)\n"
    "s_waitcnt vmcnt(22)\n"
    "v_mfma_f32_32x32x16_bf16 v[128:143], a[16:19], v[84:87], v[128:143]\n"
    "ds_read_b64_tr_b16 a[0:1], v8 offset:57344\n"
    "ds_read_b64_tr_b16 a[2:3], v8 offset:59392\n"
    "s_waitcnt lgkmcnt(6)\n"
    "v_mfma_f32_32x32x16_bf16 v[144:159], a[20:23], v[84:87], v[144:159]\n"
    "ds_read_b64_tr_b16 a[4:5], v8 offset:57600\n"
    "ds_read_b64_tr_b16 a[6:7], v8 offset:59648\n"
    "s_waitcnt lgkmcnt(6)\n"
    "v_mfma_f32_32x32x16_bf16 v[160:175], a[24:27], v[84:87], v[160:175]\n"
    "ds_read_b64_tr_b16 a[8:9], v8 offset:57856\n"
    "ds_read_b64_tr_b16 a[10:11], v8 offset:59904\n"
    "s_mov_b32 s77, 0xb000\n"
    "buffer_load_dwordx4 v[80:83], v2, s[40:43], s77 offen offset:0 sc1\n"
    "s_waitcnt lgkmcnt(6)\n"
    "v_mfma_f32_32x32x16_bf16 v[176:191], a[28:31], v[84:87], v[176:191]\n"
    "ds_read_b64_tr_b16 a[12:13], v8 offset:58112\n"
    "ds_read_b64_tr_b16 a[14:15], v8 offset:60160\n"
    "s_waitcnt lgkmcnt(6)\n"
    "s_waitcnt vmcnt(20)\n"
    "v_mfma_f32_32x32x16_bf16 v[128:143], a[0:3], v[88:91], v[128:143]\n"
    "ds_read_b64_tr_b16 a[16:17], v8 offset:61440\n"
    "ds_read_b64_tr_b16 a[18:19], v8 offset:63488\n"
    "s_waitcnt lgkmcnt(6)\n"
    "v_mfma_f32_32x32x16_bf16 v[144:159], a[4:7], v[88:91], v[144:159]\n"
    "ds_read_b64_tr_b16 a[20:21], v8 offset:61696\n"
    "ds_read_b64_tr_b16 a[22:23], v8 offset:63744\n"
    "s_waitcnt lgkmcnt(6)\n"
    "v_mfma_f32_32x32x16_bf16 v[160:175], a[8:11], v[88:91], v[160:175]\n"
    "ds_read_b64_tr_b16 a[24:25], v8 offset:61952\n"
    "ds_read_b64_tr_b16 a[26:27], v8 offset:64000\n"
    "s_mov_b32 s77, 0xb000\n"
    "buffer_load_dwordx4 v[84:87], v2, s[40:43], s77 offen offset:1024 sc1\n"
    "s_waitcnt lgkmcnt(6)\n"
    "v_mfma_f32_32x32x16_bf16 v[176:191], a[12:15], v[88:91], v[176:191]\n"
    "ds_read_b64_tr_b16 a[28:29], v8 offset:62208\n"
    "ds_read_b64_tr_b16 a[30:31], v8 offset:64256\n"
    "s_waitcnt lgkmcnt(6)\n"
    "s_waitcnt vmcnt(18)\n"
    "v_mfma_f32_32x32x16_bf16 v[128:143], a[16:19], v[92:95], v[128:143]\n"
    "s_barrier\n"
    "ds_read_b64_tr_b16 a[0:1], v9 offset:32768\n"
    "ds_read_b64_tr_b16 a[2:3], v9 offset:34816\n"
    "s_mul_i32 s66, s64, 0xe000\n"
    "s_add_u32 s66, s66, 0\n"
    "s_add_u32 m0, s69, 0x10000\n"
    "s_waitcnt lgkmcnt(6)\n"
    "v_mfma_f32_32x32x16_bf16 v[144:159], a[20:23], v[92:95], v[144:159]\n"
    "ds_read_b64_tr_b16 a[4:5], v9 offset:33024\n"
    "ds_read_b64_tr_b16 a[6:7], v9 offset:35072\n"
    "buffer_load_dwordx4 v4, s[52:55], s66 offen offset:0 lds\n"
    "s_waitcnt lgkmcnt(6)\n"
    "v_mfma_f32_32x32x16_bf16 v[160:175], a[24:27], v[92:95], v[160:175]\n"
    "ds_read_b64_tr_b16 a[8:9], v9 offset:33280\n"
    "ds_read_b64_tr_b16 a[10:11], v9 offset:35328\n"
    "s_mov_b32 s77, 0xb000\n"
    "buffer_load_dwordx4 v[88:91], v2, s[40:43], s77 offen offset:2048 sc1\n"
    "s_waitcnt lgkmcnt(6)\n"
    "v_mfma_f32_32x32x16_bf16 v[176:191], a[28:31], v[92:95], v[176:191]\n"
    "buffer_load_dwordx4 v5, s[52:55], s66 offen offset:1024 lds\n"
    "ds_read_b64_tr_b16 a[12:13], v9 offset:33536\n"
    "ds_read_b64_tr_b16 a[14:15], v9 offset:35584\n"
    "s_waitcnt lgkmcnt(6)\n"
    "s_waitcnt vmcnt(20)\n"
    "v_mfma_f32_32x32x16_bf16 v[128:143], a[0:3], v[32:35], v[128:143]\n"
    "ds_read_b64_tr_b16 a[16:17], v9 offset:36864\n"
    "ds_read_b64_tr_b16 a[18:19], v9 offset:38912\n"
    "s_waitcnt lgkmcnt(6)\n"
    "v_mfma_f32_32x32x16_bf16 v[144:159], a[4:7], v[32:35], v[144:159]\n"
    "ds_read_b64_tr_b16 a[20:21], v9 offset:37120\n"
    "ds_read_b64_tr_b16 a[22:23], v9 offset:39168\n"
    "buffer_load_dwordx4 v6, s[52:55], s66 offen offset:2048 lds\n"
    "s_waitcnt lgkmcnt(6)\n"
    "v_mfma_f32_32x32x16_bf16 v[160:175], a[8:11], v[32:35], v[160:175]\n"
    "ds_read_b64_tr_b16 a[24:25], v9 offset:37376\n"
    "ds_read_b64_tr_b16 a[26:27], v9 offset:39424\n"
    "s_mov_b32 s77, 0xb000\n"
    "buffer_load_dwordx4 v[92:95], v2, s[40:43], s77 offen offset:3072 sc1\n"
    "s_waitcnt lgkmcnt(6)\n"
    "v_mfma_f32_32x32x16_bf16 v[176:191], a[12:15], v[32:35], v[176:191]\n"
    "buffer_load_dwordx4 v7, s[52:55], s66 offen offset:3072 lds\n"
    "ds_read_b64_tr_b16 a[28:29], v9 offset:37632\n"
    "ds_read_b64_tr_b16 a[30:31], v9 offset:39680\n"
    "s_add_u32 m0, s69, 0x14000\n"
    "s_waitcnt lgkmcnt(6)\n"
    "s_waitcnt vmcnt(22)\n"
    "v_mfma_f32_32x32x16_bf16 v[128:143], a[16:19], v[36:39], v[128:143]\n"
    "ds_read_b64_tr_b16 a[0:1], v9 offset:40960\n"
    "ds_read_b64_tr_b16 a[2:3], v9 offset:43008\n"
    "s_waitcnt lgkmcnt(6)\n"
    "v_mfma_f32_32x32x16_bf16 v[144:159], a[20:23], v[36:39], v[144:159]\n"
    "ds_read_b64_tr_b16 a[4:5], v9 offset:41216\n"
    "ds_read_b64_tr_b16 a[6:7], v9 offset:43264\n"
    "buffer_load_dwordx4 v16, s[56:59], s66 offen offset:0 lds\n"
    "s_waitcnt lgkmcnt(6)\n"
    "v_mfma_f32_32x32x16_bf16 v[160:175], a[24:27], v[36:39], v[160:175]\n"
    "ds_read_b64_tr_b16 a[8:9], v9 offset:41472\n"
    "ds_read_b64_tr_b16 a[10:11], v9 offset:43520\n"
    "s_mov_b32 s77, 0xc000\n"
    "buffer_load_dwordx4 v[32:35], v2, s[40:43], s77 offen offset:0 sc1\n"
    "s_waitcnt lgkmcnt(6)\n"
    "v_mfma_f32_32x32x16_bf16 v[176:191], a[28:31], v[36:39], v[176:191]\n"
    "buffer_load_dwordx4 v17, s[56:59], s66 offen offset:1024 lds\n"
    "ds_read_b64_tr_b16 a[12:13], v9 offset:41728\n"
    "ds_read_b64_tr_b16 a[14:15], v9 offset:43776\n"
    "s_waitcnt lgkmcnt(6)\n"
    "s_waitcnt vmcnt(22)\n"
    "v_mfma_f32_32x32x16_bf16 v[128:143], a[0:3], v[40:43], v[128:143]\n"
    "ds_read_b64_tr_b16 a[16:17], v9 offset:45056\n"
    "ds_read_b64_tr_b16 a[18:19], v9 offset:47104\n"
    "s_waitcnt lgkmcnt(6)\n"
    "v_mfma_f32_32x32x16_bf16 v[144:159], a[4:7], v[40:43], v[144:159]\n"
    "ds_read_b64_tr_b16 a[20:21], v9 offset:45312\n"
    "ds_read_b64_tr_b16 a[22:23], v9 offset:47360\n"
    "buffer_load_dwordx4 v18, s[56:59], s66 offen offset:2048 lds\n"
    "s_waitcnt lgkmcnt(6)\n"
    "v_mfma_f32_32x32x16_bf16 v[160:175], a[8:11], v[40:43], v[160:175]\n"
    "ds_read_b64_tr_b16 a[24:25], v9 offset:45568\n"
    "ds_read_b64_tr_b16 a[26:27], v9 offset:47616\n"
    "s_mov_b32 s77, 0xc000\n"
    "buffer_load_dwordx4 v[36:39], v2, s[40:43], s77 offen offset:1024 sc1\n"
    "s_waitcnt lgkmcnt(6)\n"
    "v_mfma_f32_32x32x16_bf16 v[176:191], a[12:15], v[40:43], v[176:191]\n"
    "buffer_load_dwordx4 v19, s[56:59], s66 offen offset:3072 lds\n"
    "ds_read_b64_tr_b16 a[28:29], v9 offset:45824\n"
    "ds_read_b64_tr_b16 a[30:31], v9 offset:47872\n"
    "s_waitcnt lgkmcnt(6)\n"
    "s_waitcnt vmcnt(22)\n"
    "v_mfma_f32_32x32x16_bf16 v[128:143], a[16:19], v[44:47], v[128:143]\n"
    "s_barrier\n"
    "ds_read_b64_tr_b16 a[0:1], v9 offset:49152\n"
    "ds_read_b64_tr_b16 a[2:3], v9 offset:51200\n"
    "s_mul_i32 s66, s64, 0xe000\n"
    "s_add_u32 s66, s66, 0x38000\n"
    "s_add_u32 m0, s69, 0x8000\n"
    "s_waitcnt lgkmcnt(6)\n"
    "v_mfma_f32_32x32x16_bf16 v[144:159], a[20:23], v[44:47], v[144:159]\n"
    "ds_read_b64_tr_b16 a[4:5], v9 offset:49408\n"
    "ds_read_b64_tr_b16 a[6:7], v9 offset:51456\n"
    "buffer_load_dwordx4 v4, s[52:55], s66 offen offset:0 lds\n"
    "s_waitcnt lgkmcnt(6)\n"
    "v_mfma_f32_32x32x16_bf16 v[160:175], a[24:27], v[44:47], v[160:175]\n"
    "ds_read_b64_tr_b16 a[8:9], v9 offset:49664\n"
    "ds_read_b64_tr_b16 a[10:11], v9 offset:51712\n"
    "s_mov_b32 s77, 0xc000\n"
    "buffer_load_dwordx4 v[40:43], v2, s[40:43], s77 offen offset:2048 sc1\n"
    "s_waitcnt lgkmcnt(6)\n"
    "v_mfma_f32_32x32x16_bf16 v[176:191], a[28:31], v[44:47], v[176:191]\n"
    "buffer_load_dwordx4 v5, s[52:55], s66 offen offset:1024 lds\n"
    "ds_read_b64_tr_b16 a[12:13], v9 offset:49920\n"
    "ds_read_b64_tr_b16 a[14:15], v9 offset:51968\n"
    "s_waitcnt lgkmcnt(6)\n"
    "s_waitcnt vmcnt(24)\n"
    "v_mfma_f32_32x32x16_bf16 v[128:143], a[0:3], v[48:51], v[128:143]\n"
    "ds_read_b64_tr_b16 a[16:17], v9 offset:53248\n"
    "ds_read_b64_tr_b16 a[18:19], v9 offset:55296\n"
    "s_waitcnt lgkmcnt(6)\n"
    "v_mfma_f32_32x32x16_bf16 v[144:159], a[4:7], v[48:51], v[144:159]\n"
    "ds_read_b64_tr_b16 a[20:21], v9 offset:53504\n"
    "ds_read_b64_tr_b16 a[22:23], v9 offset:55552\n"
    "buffer_load_dwordx4 v6, s[52:55], s66 offen offset:2048 lds\n"
    "s_waitcnt lgkmcnt(6)\n"
    "v_mfma_f32_32x32x16_bf16 v[160:175], a[8:11], v[48:51], v[160:175]\n"
    "ds_read_b64_tr_b16 a[24:25], v9 offset:53760\n"
    "ds_read_b64_tr_b16 a[26:27], v9 offset:55808\n"
    "s_mov_b32 s77, 0xc000\n"
    "buffer_load_dwordx4 v[44:47], v2, s[40:43], s77 offen offset:3072 sc1\n"
    "s_waitcnt lgkmcnt(6)\n"
    "v_mfma_f32_32x32x16_bf16 v[176:191], a[12:15], v[48:51], v[176:191]\n"
    "buffer_load_dwordx4 v7, s[52:55], s66 offen offset:3072 lds\n"
    "ds_read_b64_tr_b16 a[28:29], v9 offset:54016\n"
    "ds_read_b64_tr_b16 a[30:31], v9 offset:56064\n"
    "s_add_u32 m0, s69, 0xc000\n"
    "s_waitcnt lgkmcnt(6)\n"
    "s_waitcnt vmcnt(26)\n"
    "v_mfma_f32_32x32x16_bf16 v[128:143], a[16:19], v[52:55], v[128:143]\n"
    "ds_read_b64_tr_b16 a[0:1], v9 offset:57344\n"
    "ds_read_b64_tr_b16 a[2:3], v9 offset:59392\n"
    "s_waitcnt lgkmcnt(6)\n"
    "v_mfma_f32_32x32x16_bf16 v[144:159], a[20:23], v[52:55], v[144:159]\n"
    "ds_read_b64_tr_b16 a[4:5], v9 offset:57600\n"
    "ds_read_b64_tr_b16 a[6:7], v9 offset:59648\n"
    "buffer_load_dwordx4 v16, s[56:59], s66 offen offset:0 lds\n"
    "s_waitcnt lgkmcnt(6)\n"
    "v_mfma_f32_32x32x16_bf16 v[160:175], a[24:27], v[52:55], v[160:175]\n"
    "ds_read_b64_tr_b16 a[8:9], v9 offset:57856\n"
    "ds_read_b64_tr_b16 a[10:11], v9 offset:59904\n"
    "s_mov_b32 s77, 0xd000\n"
    "buffer_load_dwordx4 v[48:51], v2, s[40:43], s77 offen offset:0 sc1\n"
    "s_waitcnt lgkmcnt(6)\n"
    "v_mfma_f32_32x32x16_bf16 v[176:191], a[28:31], v[52:55], v[176:191]\n"
    "buffer_load_dwordx4 v17, s[56:59], s66 offen offset:1024 lds\n"
    "ds_read_b64_tr_b16 a[12:13], v9 offset:58112\n"
    "ds_read_b64_tr_b16 a[14:15], v9 offset:60160\n"
    "s_waitcnt lgkmcnt(6)\n"
    "s_waitcnt vmcnt(28)\n"
    "v_mfma_f32_32x32x16_bf16 v[128:143], a[0:3], v[56:59], v[128:143]\n"
    "ds_read_b64_tr_b16 a[16:17], v9 offset:61440\n"
    "ds_read_b64_tr_b16 a[18:19], v9 offset:63488\n"
    "s_waitcnt lgkmcnt(6)\n"
    "v_mfma_f32_32x32x16_bf16 v[144:159], a[4:7], v[56:59], v[144:159]\n"
    "ds_read_b64_tr_b16 a[20:21], v9 offset:61696\n"
    "ds_read_b64_tr_b16 a[22:23], v9 offset:63744\n"
    "buffer_load_dwordx4 v18, s[56:59], s66 offen offset:2048 lds\n"
    "s_waitcnt lgkmcnt(6)\n"
    "v_mfma_f32_32x32x16_bf16 v[160:175], a[8:11], v[56:59], v[160:175]\n"
    "ds_read_b64_tr_b16 a[24:25], v9 offset:61952\n"
    "ds_read_b64_tr_b16 a[26:27], v9 offset:64000\n"
    "s_mov_b32 s77, 0xd000\n"
    "buffer_load_dwordx4 v[52:55], v2, s[40:43], s77 offen offset:1024 sc1\n"
    "s_waitcnt lgkmcnt(6)\n"
    "v_mfma_f32_32x32x16_bf16 v[176:191], a[12:15], v[56:59], v[176:191]\n"
    "buffer_load_dwordx4 v19, s[56:59], s66 offen offset:3072 lds\n"
    "ds_read_b64_tr_b16 a[28:29], v9 offset:62208\n"
    "ds_read_b64_tr_b16 a[30:31], v9 offset:64256\n"
    "s_waitcnt lgkmcnt(6)\n"
    "s_waitcnt vmcnt(30)\n"
    "v_mfma_f32_32x32x16_bf16 v[128:143], a[16:19], v[60:63], v[128:143]\n"
    "s_barrier\n"
    "ds_read_b64_tr_b16 a[0:1], v10 offset:0\n"
    "ds_read_b64_tr_b16 a[2:3], v10 offset:2048\n"
    "s_waitcnt lgkmcnt(6)\n"
    "v_mfma_f32_32x32x16_bf16 v[144:159], a[20:23], v[60:63], v[144:159]\n"
    "ds_read_b64_tr_b16 a[4:5], v10 offset:256\n"
    "ds_read_b64_tr_b16 a[6:7], v10 offset:2304\n"
    "s_waitcnt lgkmcnt(6)\n"
    "v_mfma_f32_32x32x16_bf16 v[160:175], a[24:27], v[60:63], v[160:175]\n"
    "ds_read_b64_tr_b16 a[8:9], v10 offset:512\n"
    "ds_read_b64_tr_b16 a[10:11], v10 offset:2560\n"
    "s_mov_b32 s77, 0xd000\n"
    "buffer_load_dwordx4 v[56:59], v2, s[40:43], s77 offen offset:2048 sc1\n"
    "s_waitcnt lgkmcnt(6)\n"
    "v_mfma_f32_32x32x16_bf16 v[176:191], a[28:31], v[60:63], v[176:191]\n"
    "ds_read_b64_tr_b16 a[12:13], v10 offset:768\n"
    "ds_read_b64_tr_b16 a[14:15], v10 offset:2816\n"
    "s_waitcnt lgkmcnt(6)\n"
    "s_waitcnt vmcnt(30)\n"
    "v_mfma_f32_32x32x16_bf16 v[128:143], a[0:3], v[64:67], v[128:143]\n"
    "ds_read_b64_tr_b16 a[16:17], v10 offset:4096\n"
    "ds_read_b64_tr_b16 a[18:19], v10 offset:6144\n"
    "s_waitcnt lgkmcnt(6)\n"
    "v_mfma_f32_32x32x16_bf16 v[144:159], a[4:7], v[64:67], v[144:159]\n"
    "ds_read_b64_tr_b16 a[20:21], v10 offset:4352\n"
    "ds_read_b64_tr_b16 a[22:23], v10 offset:6400\n"
    "s_waitcnt lgkmcnt(6)\n"
    "v_mfma_f32_32x32x16_bf16 v[160:175], a[8:11], v[64:67], v[160:175]\n"
    "ds_read_b64_tr_b16 a[24:25], v10 offset:4608\n"
    "ds_read_b64_tr_b16 a[26:27], v10 offset:6656\n"
    "s_mov_b32 s77, 0xd000\n"
    "buffer_load_dwordx4 v[60:63], v2, s[40:43], s77 offen offset:3072 sc1\n"
    "s_waitcnt lgkmcnt(6)\n"
    "v_mfma_f32_32x32x16_bf16 v[176:191], a[12:15], v[64:67], v[176:191]\n"
    "ds_read_b64_tr_b16 a[28:29], v10 offset:4864\n"
    "ds_read_b64_tr_b16 a[30:31], v10 offset:6912\n"
    "s_waitcnt lgkmcnt(6)\n"
    "s_waitcnt vmcnt(30)\n"
    "v_mfma_f32_32x32x16_bf16 v[128:143], a[16:19], v[68:71], v[128:143]\n"
    "ds_read_b64_tr_b16 a[0:1], v10 offset:8192\n"
    "ds_read_b64_tr_b16 a[2:3], v10 offset:10240\n"
    "s_waitcnt lgkmcnt(6)\n"
    "v_mfma_f32_32x32x16_bf16 v[144:159], a[20:23], v[68:71], v[144:159]\n"
    "ds_read_b64_tr_b16 a[4:5], v10 offset:8448\n"
    "ds_read_b64_tr_b16 a[6:7], v10 offset:10496\n"
    "s_waitcnt lgkmcnt(6)\n"
    "v_mfma_f32_32x32x16_bf16 v[160:175], a[24:27], v[68:71], v[160:175]\n"
    "ds_read_b64_tr_b16 a[8:9], v10 offset:8704\n"
    "ds_read_b64_tr_b16 a[10:11], v10 offset:10752\n"
    "s_mov_b32 s77, 0xe000\n"
    "buffer_load_dwordx4 v[64:67], v2, s[40:43], s77 offen offset:0 sc1\n"
    "s_waitcnt lgkmcnt(6)\n"
    "v_mfma_f32_32x32x16_bf16 v[176:191], a[28:31], v[68:71], v[176:191]\n"
    "ds_read_b64_tr_b16 a[12:13], v10 offset:8960\n"
    "ds_read_b64_tr_b16 a[14:15], v10 offset:11008\n"
    "s_waitcnt lgkmcnt(6)\n"
    "s_waitcnt vmcnt(30)\n"
    "v_mfma_f32_32x32x16_bf16 v[128:143], a[0:3], v[72:75], v[128:143]\n"
    "ds_read_b64_tr_b16 a[16:17], v10 offset:12288\n"
    "ds_read_b64_tr_b16 a[18:19], v10 offset:14336\n"
    "s_waitcnt lgkmcnt(6)\n"
    "v_mfma_f32_32x32x16_bf16 v[144:159], a[4:7], v[72:75], v[144:159]\n"
    "ds_read_b64_tr_b16 a[20:21], v10 offset:12544\n"
    "ds_read_b64_tr_b16 a[22:23], v10 offset:14592\n"
    "s_waitcnt lgkmcnt(6)\n"
    "v_mfma_f32_32x32x16_bf16 v[160:175], a[8:11], v[72:75], v[160:175]\n"
    "ds_read_b64_tr_b16 a[24:25], v10 offset:12800\n"
    "ds_read_b64_tr_b16 a[26:27], v10 offset:14848\n"
    "s_mov_b32 s77, 0xe000\n"
    "buffer_load_dwordx4 v[68:71], v2, s[40:43], s77 offen offset:1024 sc1\n"
    "s_waitcnt lgkmcnt(6)\n"
    "v_mfma_f32_32x32x16_bf16 v[176:191], a[12:15], v[72:75], v[176:191]\n"
    "ds_read_b64_tr_b16 a[28:29], v10 offset:13056\n"
    "ds_read_b64_tr_b16 a[30:31], v10 offset:15104\n"
    "s_waitcnt lgkmcnt(6)\n"
    "s_waitcnt vmcnt(30)\n"
    "v_mfma_f32_32x32x16_bf16 v[128:143], a[16:19], v[76:79], v[128:143]\n"
    "s_barrier\n"
    "ds_read_b64_tr_b16 a[0:1], v10 offset:16384\n"
    "ds_read_b64_tr_b16 a[2:3], v10 offset:18432\n"
    "s_mul_i32 s66, s64, 0xe000\n"
    "s_add_u32 s66, s66, 0x70000\n"
    "s_add_u32 m0, s69, 0x18000\n"
    "s_waitcnt lgkmcnt(6)\n"
    "v_mfma_f32_32x32x16_bf16 v[144:159], a[20:23], v[76:79], v[144:159]\n"
    "ds_read_b64_tr_b16 a[4:5], v10 offset:16640\n"
    "ds_read_b64_tr_b16 a[6:7], v10 offset:18688\n"
    "buffer_load_dwordx4 v4, s[52:55], s66 offen offset:0 lds\n"
    "s_waitcnt lgkmcnt(6)\n"
    "v_mfma_f32_32x32x16_bf16 v[160:175], a[24:27], v[76:79], v[160:175]\n"
    "ds_read_b64_tr_b16 a[8:9], v10 offset:16896\n"
    "ds_read_b64_tr_b16 a[10:11], v10 offset:18944\n"
    "s_mov_b32 s77, 0xe000\n"
    "buffer_load_dwordx4 v[72:75], v2, s[40:43], s77 offen offset:2048 sc1\n"
    "s_waitcnt lgkmcnt(6)\n"
    "v_mfma_f32_32x32x16_bf16 v[176:191], a[28:31], v[76:79], v[176:191]\n"
    "buffer_load_dwordx4 v5, s[52:55], s66 offen offset:1024 lds\n"
    "ds_read_b64_tr_b16 a[12:13], v10 offset:17152\n"
    "ds_read_b64_tr_b16 a[14:15], v10 offset:19200\n"
    "s_waitcnt lgkmcnt(6)\n"
    "s_waitcnt vmcnt(32)\n"
    "v_mfma_f32_32x32x16_bf16 v[128:143], a[0:3], v[80:83], v[128:143]\n"
    "ds_read_b64_tr_b16 a[16:17], v10 offset:20480\n"
    "ds_read_b64_tr_b16 a[18:19], v10 offset:22528\n"
    "s_waitcnt lgkmcnt(6)\n"
    "v_mfma_f32_32x32x16_bf16 v[144:159], a[4:7], v[80:83], v[144:159]\n"
    "ds_read_b64_tr_b16 a[20:21], v10 offset:20736\n"
    "ds_read_b64_tr_b16 a[22:23], v10 offset:22784\n"
    "buffer_load_dwordx4 v6, s[52:55], s66 offen offset:2048 lds\n"
    "s_waitcnt lgkmcnt(6)\n"
    "v_mfma_f32_32x32x16_bf16 v[160:175], a[8:11], v[80:83], v[160:175]\n"
    "ds_read_b64_tr_b16 a[24:25], v10 offset:20992\n"
    "ds_read_b64_tr_b16 a[26:27], v10 offset:23040\n"
    "s_mov_b32 s77, 0xe000\n"
    "buffer_load_dwordx4 v[76:79], v2, s[40:43], s77 offen offset:3072 sc1\n"
    "s_waitcnt lgkmcnt(6)\n"
    "v_mfma_f32_32x32x16_bf16 v[176:191], a[12:15], v[80:83], v[176:191]\n"
    "buffer_load_dwordx4 v7, s[52:55], s66 offen offset:3072 lds\n"
    "ds_read_b64_tr_b16 a[28:29], v10 offset:21248\n"
    "ds_read_b64_tr_b16 a[30:31], v10 offset:23296\n"
    "s_add_u32 m0, s69, 0x1c000\n"
    "s_waitcnt lgkmcnt(6)\n"
    "s_waitcnt vmcnt(34)\n"
    "v_mfma_f32_32x32x16_bf16 v[128:143], a[16:19], v[84:87], v[128:143]\n"
    "ds_read_b64_tr_b16 a[0:1], v10 offset:24576\n"
    "ds_read_b64_tr_b16 a[2:3], v10 offset:26624\n"
    "s_waitcnt lgkmcnt(6)\n"
    "v_mfma_f32_32x32x16_bf16 v[144:159], a[20:23], v[84:87], v[144:159]\n"
    "ds_read_b64_tr_b16 a[4:5], v10 offset:24832\n"
    "ds_read_b64_tr_b16 a[6:7], v10 offset:26880\n"
    "buffer_load_dwordx4 v16, s[56:59], s66 offen offset:0 lds\n"
    "s_waitcnt lgkmcnt(6)\n"
    "v_mfma_f32_32x32x16_bf16 v[160:175], a[24:27], v[84:87], v[160:175]\n"
    "ds_read_b64_tr_b16 a[8:9], v10 offset:25088\n"
    "ds_read_b64_tr_b16 a[10:11], v10 offset:27136\n"
    "s_mov_b32 s77, 0xf000\n"
    "buffer_load_dwordx4 v[80:83], v2, s[40:43], s77 offen offset:0 sc1\n"
    "s_waitcnt lgkmcnt(6)\n"
    "v_mfma_f32_32x32x16_bf16 v[176:191], a[28:31], v[84:87], v[176:191]\n"
    "buffer_load_dwordx4 v17, s[56:59], s66 offen offset:1024 lds\n"
    "ds_read_b64_tr_b16 a[12:13], v10 offset:25344\n"
    "ds_read_b64_tr_b16 a[14:15], v10 offset:27392\n"
    "s_waitcnt lgkmcnt(6)\n"
    "s_waitcnt vmcnt(35)\n"
    "v_mfma_f32_32x32x16_bf16 v[128:143], a[0:3], v[88:91], v[128:143]\n"
    "ds_read_b64_tr_b16 a[16:17], v10 offset:28672\n"
    "ds_read_b64_tr_b16 a[18:19], v10 offset:30720\n"
    "s_waitcnt lgkmcnt(6)\n"
    "v_mfma_f32_32x32x16_bf16 v[144:159], a[4:7], v[88:91], v[144:159]\n"
    "ds_read_b64_tr_b16 a[20:21], v10 offset:28928\n"
    "ds_read_b64_tr_b16 a[22:23], v10 offset:30976\n"
    "buffer_load_dwordx4 v18, s[56:59], s66 offen offset:2048 lds\n"
    "s_waitcnt lgkmcnt(6)\n"
    "v_mfma_f32_32x32x16_bf16 v[160:175], a[8:11], v[88:91], v[160:175]\n"
    "ds_read_b64_tr_b16 a[24:25], v10 offset:29184\n"
    "ds_read_b64_tr_b16 a[26:27], v10 offset:31232\n"
    "s_mov_b32 s77, 0xf000\n"
    "buffer_load_dwordx4 v[84:87], v2, s[40:43], s77 offen offset:1024 sc1\n"
    "s_waitcnt lgkmcnt(6)\n"
    "v_mfma_f32_32x32x16_bf16 v[176:191], a[12:15], v[88:91], v[176:191]\n"
    "buffer_load_dwordx4 v19, s[56:59], s66 offen offset:3072 lds\n"
    "ds_read_b64_tr_b16 a[28:29], v10 offset:29440\n"
    "ds_read_b64_tr_b16 a[30:31], v10 offset:31488\n"
    "s_waitcnt lgkmcnt(6)\n"
    "s_waitcnt vmcnt(35)\n"
    "v_mfma_f32_32x32x16_bf16 v[128:143], a[16:19], v[92:95], v[128:143]\n"
    "s_barrier\n"
    "ds_read_b64_tr_b16 a[0:1], v8 offset:0\n"
    "ds_read_b64_tr_b16 a[2:3], v8 offset:2048\n"
    "s_add_u32 m0, s69, 0x20000\n"
    "s_lshl_b32 s78, s64, 14\n"
    "s_add_u32 s78, s78, 0xe0000\n"
    "s_waitcnt lgkmcnt(6)\n"
    "v_mfma_f32_32x32x16_bf16 v[144:159], a[20:23], v[92:95], v[144:159]\n"
    "ds_read_b64_tr_b16 a[4:5], v8 offset:256\n"
    "ds_read_b64_tr_b16 a[6:7], v8 offset:2304\n"
    "buffer_load_dwordx4 v26, s[48:51], s78 offen offset:0 lds\n"
    "buffer_load_dwordx4 v27, s[48:51], s78 offen offset:1024 lds\n"
    "s_waitcnt lgkmcnt(6)\n"
    "v_mfma_f32_32x32x16_bf16 v[160:175], a[24:27], v[92:95], v[160:175]\n"
    "ds_read_b64_tr_b16 a[8:9], v8 offset:512\n"
    "ds_read_b64_tr_b16 a[10:11], v8 offset:2560\n"
    "s_mov_b32 s77, 0xf000\n"
    "buffer_load_dwordx4 v[88:91], v2, s[40:43], s77 offen offset:2048 sc1\n"
    "s_waitcnt lgkmcnt(6)\n"
    "v_mfma_f32_32x32x16_bf16 v[176:191], a[28:31], v[92:95], v[176:191]\n"
    "buffer_load_dwordx4 v29, s[48:51], s78 offen offset:3072 lds\n"
    "buffer_load_dwordx4 v28, s[48:51], s78 offen offset:2048 lds\n"
    "ds_read_b64_tr_b16 a[12:13], v8 offset:768\n"
    "ds_read_b64_tr_b16 a[14:15], v8 offset:2816\n"
    "s_waitcnt lgkmcnt(6)\n"
    "s_waitcnt vmcnt(37)\n"
    "v_mfma_f32_32x32x16_bf16 v[128:143], a[0:3], v[32:35], v[128:143]\n"
    "ds_read_b64_tr_b16 a[16:17], v8 offset:4096\n"
    "ds_read_b64_tr_b16 a[18:19], v8 offset:6144\n"
    "s_waitcnt lgkmcnt(6)\n"
    "v_mfma_f32_32x32x16_bf16 v[144:159], a[4:7], v[32:35], v[144:159]\n"
    "ds_read_b64_tr_b16 a[20:21], v8 offset:4352\n"
    "ds_read_b64_tr_b16 a[22:23], v8 offset:6400\n"
    "s_waitcnt lgkmcnt(6)\n"
    "v_mfma_f32_32x32x16_bf16 v[160:175], a[8:11], v[32:35], v[160:175]\n"
    "ds_read_b64_tr_b16 a[24:25], v8 offset:4608\n"
    "ds_read_b64_tr_b16 a[26:27], v8 offset:6656\n"
    "s_mov_b32 s77, 0xf000\n"
    "buffer_load_dwordx4 v[92:95], v2, s[40:43], s77 offen offset:3072 sc1\n"
    "s_waitcnt lgkmcnt(6)\n"
    "v_mfma_f32_32x32x16_bf16 v[176:191], a[12:15], v[32:35], v[176:191]\n"
    "ds_read_b64_tr_b16 a[28:29], v8 offset:4864\n"
    "ds_read_b64_tr_b16 a[30:31], v8 offset:6912\n"
    "s_waitcnt lgkmcnt(6)\n"
    "s_waitcnt vmcnt(35)\n"
    "v_mfma_f32_32x32x16_bf16 v[128:143], a[16:19], v[36:39], v[128:143]\n"
    "ds_read_b64_tr_b16 a[0:1], v8 offset:8192\n"
    "ds_read_b64_tr_b16 a[2:3], v8 offset:10240\n"
    "s_waitcnt lgkmcnt(6)\n"
    "v_mfma_f32_32x32x16_bf16 v[144:159], a[20:23], v[36:39], v[144:159]\n"
    "ds_read_b64_tr_b16 a[4:5], v8 offset:8448\n"
    "ds_read_b64_tr_b16 a[6:7], v8 offset:10496\n"
    "s_waitcnt lgkmcnt(6)\n"
    "v_mfma_f32_32x32x16_bf16 v[160:175], a[24:27], v[36:39], v[160:175]\n"
    "ds_read_b64_tr_b16 a[8:9], v8 offset:8704\n"
    "ds_read_b64_tr_b16 a[10:11], v8 offset:10752\n"
    "s_waitcnt lgkmcnt(6)\n"
    "v_mfma_f32_32x32x16_bf16 v[176:191], a[28:31], v[36:39], v[176:191]\n"
    "ds_read_b64_tr_b16 a[12:13], v8 offset:8960\n"
    "ds_read_b64_tr_b16 a[14:15], v8 offset:11008\n"
    "s_waitcnt lgkmcnt(6)\n"
    "s_waitcnt vmcnt(32)\n"
    "v_mfma_f32_32x32x16_bf16 v[128:143], a[0:3], v[40:43], v[128:143]\n"
    "ds_read_b64_tr_b16 a[16:17], v8 offset:12288\n"
    "ds_read_b64_tr_b16 a[18:19], v8 offset:14336\n"
    "s_waitcnt lgkmcnt(6)\n"
    "v_mfma_f32_32x32x16_bf16 v[144:159], a[4:7], v[40:43], v[144:159]\n"
    "ds_read_b64_tr_b16 a[20:21], v8 offset:12544\n"
    "ds_read_b64_tr_b16 a[22:23], v8 offset:14592\n"
    "s_waitcnt lgkmcnt(6)\n"
    "v_mfma_f32_32x32x16_bf16 v[160:175], a[8:11], v[40:43], v[160:175]\n"
    "ds_read_b64_tr_b16 a[24:25], v8 offset:12800\n"
    "ds_read_b64_tr_b16 a[26:27], v8 offset:14848\n"
    "s_waitcnt lgkmcnt(6)\n"
    "v_mfma_f32_32x32x16_bf16 v[176:191], a[12:15], v[40:43], v[176:191]\n"
    "ds_read_b64_tr_b16 a[28:29], v8 offset:13056\n"
    "ds_read_b64_tr_b16 a[30:31], v8 offset:15104\n"
    "s_waitcnt lgkmcnt(6)\n"
    "s_waitcnt vmcnt(29)\n"
    "v_mfma_f32_32x32x16_bf16 v[128:143], a[16:19], v[44:47], v[128:143]\n"
    "s_barrier\n"
    "ds_read_b64_tr_b16 a[0:1], v8 offset:16384\n"
    "ds_read_b64_tr_b16 a[2:3], v8 offset:18432\n"
    "s_add_u32 m0, s69, 0x24000\n"
    "s_lshl_b32 s78, s64, 14\n"
    "s_add_u32 s78, s78, 0xf0000\n"
    "s_waitcnt lgkmcnt(6)\n"
    "v_mfma_f32_32x32x16_bf16 v[144:159], a[20:23], v[44:47], v[144:159]\n"
    "ds_read_b64_tr_b16 a[4:5], v8 offset:16640\n"
    "ds_read_b64_tr_b16 a[6:7], v8 offset:18688\n"
    "buffer_load_dwordx4 v26, s[48:51], s78 offen offset:0 lds\n"
    "buffer_load_dwordx4 v27, s[48:51], s78 offen offset:1024 lds\n"
    "s_waitcnt lgkmcnt(6)\n"
    "v_mfma_f32_32x32x16_bf16 v[160:175], a[24:27], v[44:47], v[160:175]\n"
    "ds_read_b64_tr_b16 a[8:9], v8 offset:16896\n"
    "ds_read_b64_tr_b16 a[10:11], v8 offset:18944\n"
    "s_waitcnt lgkmcnt(6)\n"
    "v_mfma_f32_32x32x16_bf16 v[176:191], a[28:31], v[44:47], v[176:191]\n"
    "buffer_load_dwordx4 v29, s[48:51], s78 offen offset:3072 lds\n"
    "buffer_load_dwordx4 v28, s[48:51], s78 offen offset:2048 lds\n"
    "ds_read_b64_tr_b16 a[12:13], v8 offset:17152\n"
    "ds_read_b64_tr_b16 a[14:15], v8 offset:19200\n"
    "s_waitcnt lgkmcnt(6)\n"
    "s_waitcnt vmcnt(30)\n"
    "v_mfma_f32_32x32x16_bf16 v[128:143], a[0:3], v[48:51], v[128:143]\n"
    "ds_read_b64_tr_b16 a[16:17], v8 offset:20480\n"
    "ds_read_b64_tr_b16 a[18:19], v8 offset:22528\n"
    "s_waitcnt lgkmcnt(6)\n"
    "v_mfma_f32_32x32x16_bf16 v[144:159], a[4:7], v[48:51], v[144:159]\n"
    "ds_read_b64_tr_b16 a[20:21], v8 offset:20736\n"
    "ds_read_b64_tr_b16 a[22:23], v8 offset:22784\n"
    "s_waitcnt lgkmcnt(6)\n"
    "v_mfma_f32_32x32x16_bf16 v[160:175], a[8:11], v[48:51], v[160:175]\n"
    "ds_read_b64_tr_b16 a[24:25], v8 offset:20992\n"
    "ds_read_b64_tr_b16 a[26:27], v8 offset:23040\n"
    "s_waitcnt lgkmcnt(6)\n"
    "v_mfma_f32_32x32x16_bf16 v[176:191], a[12:15], v[48:51], v[176:191]\n"
    "ds_read_b64_tr_b16 a[28:29], v8 offset:21248\n"
    "ds_read_b64_tr_b16 a[30:31], v8 offset:23296\n"
    "s_waitcnt lgkmcnt(6)\n"
    "s_waitcnt vmcnt(27)\n"
    "v_mfma_f32_32x32x16_bf16 v[128:143], a[16:19], v[52:55], v[128:143]\n"
    "ds_read_b64_tr_b16 a[0:1], v8 offset:24576\n"
    "ds_read_b64_tr_b16 a[2:3], v8 offset:26624\n"
    "s_waitcnt lgkmcnt(6)\n"
    "v_mfma_f32_32x32x16_bf16 v[144:159], a[20:23], v[52:55], v[144:159]\n"
    "ds_read_b64_tr_b16 a[4:5], v8 offset:24832\n"
    "ds_read_b64_tr_b16 a[6:7], v8 offset:26880\n"
    "s_waitcnt lgkmcnt(6)\n"
    "v_mfma_f32_32x32x16_bf16 v[160:175], a[24:27], v[52:55], v[160:175]\n"
    "ds_read_b64_tr_b16 a[8:9], v8 offset:25088\n"
    "ds_read_b64_tr_b16 a[10:11], v8 offset:27136\n"
    "s_waitcnt lgkmcnt(6)\n"
    "v_mfma_f32_32x32x16_bf16 v[176:191], a[28:31], v[52:55], v[176:191]\n"
    "ds_read_b64_tr_b16 a[12:13], v8 offset:25344\n"
    "ds_read_b64_tr_b16 a[14:15], v8 offset:27392\n"
    "s_waitcnt lgkmcnt(6)\n"
    "s_waitcnt vmcnt(25)\n"
    "v_mfma_f32_32x32x16_bf16 v[128:143], a[0:3], v[56:59], v[128:143]\n"
    "ds_read_b64_tr_b16 a[16:17], v8 offset:28672\n"
    "ds_read_b64_tr_b16 a[18:19], v8 offset:30720\n"
    "s_waitcnt lgkmcnt(6)\n"
    "v_mfma_f32_32x32x16_bf16 v[144:159], a[4:7], v[56:59], v[144:159]\n"
    "ds_read_b64_tr_b16 a[20:21], v8 offset:28928\n"
    "ds_read_b64_tr_b16 a[22:23], v8 offset:30976\n"
    "s_waitcnt lgkmcnt(6)\n"
    "v_mfma_f32_32x32x16_bf16 v[160:175], a[8:11], v[56:59], v[160:175]\n"
    "ds_read_b64_tr_b16 a[24:25], v8 offset:29184\n"
    "ds_read_b64_tr_b16 a[26:27], v8 offset:31232\n"
    "s_waitcnt lgkmcnt(6)\n"
    "v_mfma_f32_32x32x16_bf16 v[176:191], a[12:15], v[56:59], v[176:191]\n"
    "ds_read_b64_tr_b16 a[28:29], v8 offset:29440\n"
    "ds_read_b64_tr_b16 a[30:31], v8 offset:31488\n"
    "s_waitcnt lgkmcnt(6)\n"
    "s_waitcnt vmcnt(24)\n"
    "v_mfma_f32_32x32x16_bf16 v[128:143], a[16:19], v[60:63], v[128:143]\n"
    "s_waitcnt vmcnt(5)\n"
    "s_barrier\n"
    "ds_read_b64_tr_b16 a[0:1], v10 offset:0\n"
    "ds_read_b64_tr_b16 a[2:3], v10 offset:2048\n"
    "s_waitcnt lgkmcnt(6)\n"
    "v_mfma_f32_32x32x16_bf16 v[144:159], a[20:23], v[60:63], v[144:159]\n"
    "ds_read_b64_tr_b16 a[4:5], v10 offset:256\n"
    "ds_read_b64_tr_b16 a[6:7], v10 offset:2304\n"
    "s_waitcnt lgkmcnt(6)\n"
    "v_mfma_f32_32x32x16_bf16 v[160:175], a[24:27], v[60:63], v[160:175]\n"
    "ds_read_b64_tr_b16 a[8:9], v10 offset:512\n"
    "ds_read_b64_tr_b16 a[10:11], v10 offset:2560\n"
    "s_waitcnt lgkmcnt(6)\n"
    "v_mfma_f32_32x32x16_bf16 v[176:191], a[28:31], v[60:63], v[176:191]\n"
    "ds_read_b64_tr_b16 a[12:13], v10 offset:768\n"
    "ds_read_b64_tr_b16 a[14:15], v10 offset:2816\n"
    "s_waitcnt lgkmcnt(6)\n"
    "v_mfma_f32_32x32x16_bf16 v[128:143], a[0:3], v[64:67], v[128:143]\n"
    "ds_read_b64_tr_b16 a[16:17], v10 offset:4096\n"
    "ds_read_b64_tr_b16 a[18:19], v10 offset:6144\n"
    "s_waitcnt lgkmcnt(6)\n"
    "v_mfma_f32_32x32x16_bf16 v[144:159], a[4:7], v[64:67], v[144:159]\n"
    "ds_read_b64_tr_b16 a[20:21], v10 offset:4352\n"
    "ds_read_b64_tr_b16 a[22:23], v10 offset:6400\n"
    "s_waitcnt lgkmcnt(6)\n"
    "v_mfma_f32_32x32x16_bf16 v[160:175], a[8:11], v[64:67], v[160:175]\n"
    "ds_read_b64_tr_b16 a[24:25], v10 offset:4608\n"
    "ds_read_b64_tr_b16 a[26:27], v10 offset:6656\n"
    "s_waitcnt lgkmcnt(6)\n"
    "v_mfma_f32_32x32x16_bf16 v[176:191], a[12:15], v[64:67], v[176:191]\n"
    "ds_read_b64_tr_b16 a[28:29], v10 offset:4864\n"
    "ds_read_b64_tr_b16 a[30:31], v10 offset:6912\n"
    "s_waitcnt lgkmcnt(6)\n"
    "v_mfma_f32_32x32x16_bf16 v[128:143], a[16:19], v[68:71], v[128:143]\n"
    "ds_read_b64_tr_b16 a[0:1], v10 offset:8192\n"
    "ds_read_b64_tr_b16 a[2:3], v10 offset:10240\n"
    "s_waitcnt lgkmcnt(6)\n"
    "v_mfma_f32_32x32x16_bf16 v[144:159], a[20:23], v[68:71], v[144:159]\n"
    "ds_read_b64_tr_b16 a[4:5], v10 offset:8448\n"
    "ds_read_b64_tr_b16 a[6:7], v10 offset:10496\n"
    "s_waitcnt lgkmcnt(6)\n"
    "v_mfma_f32_32x32x16_bf16 v[160:175], a[24:27], v[68:71], v[160:175]\n"
    "ds_read_b64_tr_b16 a[8:9], v10 offset:8704\n"
    "ds_read_b64_tr_b16 a[10:11], v10 offset:10752\n"
    "s_waitcnt lgkmcnt(6)\n"
    "v_mfma_f32_32x32x16_bf16 v[176:191], a[28:31], v[68:71], v[176:191]\n"
    "ds_read_b64_tr_b16 a[12:13], v10 offset:8960\n"
    "ds_read_b64_tr_b16 a[14:15], v10 offset:11008\n"
    "s_waitcnt lgkmcnt(6)\n"
    "v_mfma_f32_32x32x16_bf16 v[128:143], a[0:3], v[72:75], v[128:143]\n"
    "ds_read_b64_tr_b16 a[16:17], v10 offset:12288\n"
    "ds_read_b64_tr_b16 a[18:19], v10 offset:14336\n"
    "s_waitcnt lgkmcnt(6)\n"
    "v_mfma_f32_32x32x16_bf16 v[144:159], a[4:7], v[72:75], v[144:159]\n"
    "ds_read_b64_tr_b16 a[20:21], v10 offset:12544\n"
    "ds_read_b64_tr_b16 a[22:23], v10 offset:14592\n"
    "s_waitcnt lgkmcnt(6)\n"
    "v_mfma_f32_32x32x16_bf16 v[160:175], a[8:11], v[72:75], v[160:175]\n"
    "ds_read_b64_tr_b16 a[24:25], v10 offset:12800\n"
    "ds_read_b64_tr_b16 a[26:27], v10 offset:14848\n"
    "s_waitcnt lgkmcnt(6)\n"
    "v_mfma_f32_32x32x16_bf16 v[176:191], a[12:15], v[72:75], v[176:191]\n"
    "ds_read_b64_tr_b16 a[28:29], v10 offset:13056\n"
    "ds_read_b64_tr_b16 a[30:31], v10 offset:15104\n"
    "s_waitcnt lgkmcnt(6)\n"
    "v_mfma_f32_32x32x16_bf16 v[128:143], a[16:19], v[76:79], v[128:143]\n"
    "s_waitcnt vmcnt(0)\n"
    "s_barrier\n"
    "ds_read_b64_tr_b16 a[0:1], v10 offset:16384\n"
    "ds_read_b64_tr_b16 a[2:3], v10 offset:18432\n"
    "s_mul_i32 s66, s64, 0xe000\n"
    "s_add_u32 s66, s66, 0xa8000\n"
    "s_add_u32 m0, s69, 0\n"
    "s_waitcnt lgkmcnt(6)\n"
    "v_mfma_f32_32x32x16_bf16 v[144:159], a[20:23], v[76:79], v[144:159]\n"
    "ds_read_b64_tr_b16 a[4:5], v10 offset:16640\n"
    "ds_read_b64_tr_b16 a[6:7], v10 offset:18688\n"
    "buffer_load_dwordx4 v4, s[52:55], s66 offen offset:0 lds\n"
    "s_waitcnt lgkmcnt(6)\n"
    "v_mfma_f32_32x32x16_bf16 v[160:175], a[24:27], v[76:79], v[160:175]\n"
    "ds_read_b64_tr_b16 a[8:9], v10 offset:16896\n"
    "ds_read_b64_tr_b16 a[10:11], v10 offset:18944\n"
    "s_waitcnt lgkmcnt(6)\n"
    "v_mfma_f32_32x32x16_bf16 v[176:191], a[28:31], v[76:79], v[176:191]\n"
    "buffer_load_dwordx4 v5, s[52:55], s66 offen offset:1024 lds\n"
    "ds_read_b64_tr_b16 a[12:13], v10 offset:17152\n"
    "ds_read_b64_tr_b16 a[14:15], v10 offset:19200\n"
    "s_waitcnt lgkmcnt(6)\n"
    "v_mfma_f32_32x32x16_bf16 v[128:143], a[0:3], v[80:83], v[128:143]\n"
    "ds_read_b64_tr_b16 a[16:17], v10 offset:20480\n"
    "ds_read_b64_tr_b16 a[18:19], v10 offset:22528\n"
    "s_waitcnt lgkmcnt(6)\n"
    "v_mfma_f32_32x32x16_bf16 v[144:159], a[4:7], v[80:83], v[144:159]\n"
    "ds_read_b64_tr_b16 a[20:21], v10 offset:20736\n"
    "ds_read_b64_tr_b16 a[22:23], v10 offset:22784\n"
    "buffer_load_dwordx4 v6, s[52:55], s66 offen offset:2048 lds\n"
    "s_waitcnt lgkmcnt(6)\n"
    "v_mfma_f32_32x32x16_bf16 v[160:175], a[8:11], v[80:83], v[160:175]\n"
    "ds_read_b64_tr_b16 a[24:25], v10 offset:20992\n"
    "ds_read_b64_tr_b16 a[26:27], v10 offset:23040\n"
    "s_waitcnt lgkmcnt(6)\n"
    "v_mfma_f32_32x32x16_bf16 v[176:191], a[12:15], v[80:83], v[176:191]\n"
    "buffer_load_dwordx4 v7, s[52:55], s66 offen offset:3072 lds\n"
    "ds_read_b64_tr_b16 a[28:29], v10 offset:21248\n"
    "ds_read_b64_tr_b16 a[30:31], v10 offset:23296\n"
    "s_add_u32 m0, s69, 0x4000\n"
    "s_waitcnt lgkmcnt(6)\n"
    "v_mfma_f32_32x32x16_bf16 v[128:143], a[16:19], v[84:87], v[128:143]\n"
    "ds_read_b64_tr_b16 a[0:1], v10 offset:24576\n"
    "ds_read_b64_tr_b16 a[2:3], v10 offset:26624\n"
    "s_waitcnt lgkmcnt(6)\n"
    "v_mfma_f32_32x32x16_bf16 v[144:159], a[20:23], v[84:87], v[144:159]\n"
    "ds_read_b64_tr_b16 a[4:5], v10 offset:24832\n"
    "ds_read_b64_tr_b16 a[6:7], v10 offset:26880\n"
    "buffer_load_dwordx4 v16, s[56:59], s66 offen offset:0 lds\n"
    "s_waitcnt lgkmcnt(6)\n"
    "v_mfma_f32_32x32x16_bf16 v[160:175], a[24:27], v[84:87], v[160:175]\n"
    "ds_read_b64_tr_b16 a[8:9], v10 offset:25088\n"
    "ds_read_b64_tr_b16 a[10:11], v10 offset:27136\n"
    "s_waitcnt lgkmcnt(6)\n"
    "v_mfma_f32_32x32x16_bf16 v[176:191], a[28:31], v[84:87], v[176:191]\n"
    "buffer_load_dwordx4 v17, s[56:59], s66 offen offset:1024 lds\n"
    "ds_read_b64_tr_b16 a[12:13], v10 offset:25344\n"
    "ds_read_b64_tr_b16 a[14:15], v10 offset:27392\n"
    "s_waitcnt lgkmcnt(6)\n"
    "v_mfma_f32_32x32x16_bf16 v[128:143], a[0:3], v[88:91], v[128:143]\n"
    "ds_read_b64_tr_b16 a[16:17], v10 offset:28672\n"
    "ds_read_b64_tr_b16 a[18:19], v10 offset:30720\n"
    "s_waitcnt lgkmcnt(6)\n"
    "v_mfma_f32_32x32x16_bf16 v[144:159], a[4:7], v[88:91], v[144:159]\n"
    "ds_read_b64_tr_b16 a[20:21], v10 offset:28928\n"
    "ds_read_b64_tr_b16 a[22:23], v10 offset:30976\n"
    "buffer_load_dwordx4 v18, s[56:59], s66 offen offset:2048 lds\n"
    "s_waitcnt lgkmcnt(6)\n"
    "v_mfma_f32_32x32x16_bf16 v[160:175], a[8:11], v[88:91], v[160:175]\n"
    "ds_read_b64_tr_b16 a[24:25], v10 offset:29184\n"
    "ds_read_b64_tr_b16 a[26:27], v10 offset:31232\n"
    "s_waitcnt lgkmcnt(6)\n"
    "v_mfma_f32_32x32x16_bf16 v[176:191], a[12:15], v[88:91], v[176:191]\n"
    "buffer_load_dwordx4 v19, s[56:59], s66 offen offset:3072 lds\n"
    "ds_read_b64_tr_b16 a[28:29], v10 offset:29440\n"
    "ds_read_b64_tr_b16 a[30:31], v10 offset:31488\n"
    "s_waitcnt lgkmcnt(6)\n"
    "v_mfma_f32_32x32x16_bf16 v[128:143], a[16:19], v[92:95], v[128:143]\n"
    "s_waitcnt lgkmcnt(4)\n"
    "v_mfma_f32_32x32x16_bf16 v[144:159], a[20:23], v[92:95], v[144:159]\n"
    "s_waitcnt lgkmcnt(2)\n"
    "v_mfma_f32_32x32x16_bf16 v[160:175], a[24:27], v[92:95], v[160:175]\n"
    "s_waitcnt lgkmcnt(0)\n"
    "v_mfma_f32_32x32x16_bf16 v[176:191], a[28:31], v[92:95], v[176:191]\n"
    "s_barrier\n"
    "s_nop 7\n"
    "s_mov_b32 s76, 0x1000\n"
    "v_cvt_pk_bf16_f32 v32, v128, v129\n"
    "v_cvt_pk_bf16_f32 v33, v130, v131\n"
    "v_cvt_pk_bf16_f32 v34, v132, v133\n"
    "v_cvt_pk_bf16_f32 v35, v134, v135\n"
    "buffer_store_dwordx4 v[32:35], v2, s[44:47], 0 offen offset:0 sc1\n"
    "v_cvt_pk_bf16_f32 v36, v136, v137\n"
    "v_cvt_pk_bf16_f32 v37, v138, v139\n"
    "v_cvt_pk_bf16_f32 v38, v140, v141\n"
    "v_cvt_pk_bf16_f32 v39, v142, v143\n"
    "buffer_store_dwordx4 v[36:39], v2, s[44:47], 0 offen offset:1024 sc1\n"
    "v_cvt_pk_bf16_f32 v40, v144, v145\n"
    "v_cvt_pk_bf16_f32 v41, v146, v147\n"
    "v_cvt_pk_bf16_f32 v42, v148, v149\n"
    "v_cvt_pk_bf16_f32 v43, v150, v151\n"
    "buffer_store_dwordx4 v[40:43], v2, s[44:47], 0 offen offset:2048 sc1\n"
    "v_cvt_pk_bf16_f32 v44, v152, v153\n"
    "v_cvt_pk_bf16_f32 v45, v154, v155\n"
    "v_cvt_pk_bf16_f32 v46, v156, v157\n"
    "v_cvt_pk_bf16_f32 v47, v158, v159\n"
    "buffer_store_dwordx4 v[44:47], v2, s[44:47], 0 offen offset:3072 sc1\n"
    "v_cvt_pk_bf16_f32 v48, v160, v161\n"
    "v_cvt_pk_bf16_f32 v49, v162, v163\n"
    "v_cvt_pk_bf16_f32 v50, v164, v165\n"
    "v_cvt_pk_bf16_f32 v51, v166, v167\n"
    "buffer_store_dwordx4 v[48:51], v2, s[44:47], s76 offen offset:0 sc1\n"
    "v_cvt_pk_bf16_f32 v52, v168, v169\n"
    "v_cvt_pk_bf16_f32 v53, v170, v171\n"
    "v_cvt_pk_bf16_f32 v54, v172, v173\n"
    "v_cvt_pk_bf16_f32 v55, v174, v175\n"
    "buffer_store_dwordx4 v[52:55], v2, s[44:47], s76 offen offset:1024 sc1\n"
    "v_cvt_pk_bf16_f32 v56, v176, v177\n"
    "v_cvt_pk_bf16_f32 v57, v178, v179\n"
    "v_cvt_pk_bf16_f32 v58, v180, v181\n"
    "v_cvt_pk_bf16_f32 v59, v182, v183\n"
    "buffer_store_dwordx4 v[56:59], v2, s[44:47], s76 offen offset:2048 sc1\n"
    "v_cvt_pk_bf16_f32 v60, v184, v185\n"
    "v_cvt_pk_bf16_f32 v61, v186, v187\n"
    "v_cvt_pk_bf16_f32 v62, v188, v189\n"
    "v_cvt_pk_bf16_f32 v63, v190, v191\n"
    "buffer_store_dwordx4 v[60:63], v2, s[44:47], s76 offen offset:3072 sc1\n"
    "s_add_u32 s92, s86, 0x2b00\n"
    "s_addc_u32 s93, s87, 0\n"
    "s_lshl_b32 s99, s70, 8\n"
    "s_add_u32 s92, s92, s99\n"
    "s_addc_u32 s93, s93, 0\n"
    "s_waitcnt vmcnt(0)\n"
    "s_barrier\n"
    "s_cmp_lg_u32 s64, 0\n"
    "s_cbranch_scc1 .Lsig9\n"
    "v_mov_b32 v13, 0\n"
    "v_mov_b32 v14, 1\n"
    "s_mov_b64 exec, 1\n"
    "global_atomic_add v13, v14, s[92:93]\n"
    "s_mov_b64 exec, -1\n"
    ".Lsig9:\n"
    "s_lshl_b32 s76, s70, 2\n"
    "s_lshr_b32 s73, s79, 2\n"
    "s_add_u32 s76, s76, s73\n"
    "s_and_b32 s77, s79, 3\n"
    "s_lshl_b32 s77, s77, 3\n"
    "v_and_b32 v11, 7, v1\n"
    "v_bfe_u32 v12, v1, 3, 1\n"
    "v_lshrrev_b32 v13, 4, v1\n"
    "v_add_u32 v20, s77, v11\n"
    "v_lshlrev_b32 v20, 4, v20\n"
    "v_lshl_add_u32 v20, v12, 9, v20\n"
    "v_lshl_add_u32 v20, v13, 10, v20\n"
    "v_lshlrev_b32 v21, 10, v11\n"
    "v_lshl_add_u32 v21, v13, 5, v21\n"
    "v_lshl_add_u32 v21, v12, 3, v21\n"
    "v_lshlrev_b32 v22, 6, v13\n"
    "v_lshl_add_u32 v22, v12, 4, v22\n"
    "s_mov_b64 s[44:45], %8\n"
    "s_lshl_b32 s73, s70, 17\n"
    "s_add_u32 s44, s44, s73\n"
    "s_addc_u32 s45, s45, 0\n"
    "s_lshl_b32 s73, s79, 13\n"
    "s_add_u32 s44, s44, s73\n"
    "s_addc_u32 s45, s45, 0\n"
    "s_and_b32 s45, s45, 0xffff\n"
    "s_mov_b32 s46, 0x2000\n"
    "s_mov_b32 s47, 0x20000\n"
    "s_mov_b64 s[60:61], %0\n"
    "s_add_u32 s60, s60, 0x0\n"
    "s_addc_u32 s61, s61, 0\n"
    "s_and_b32 s61, s61, 0xffff\n"
    "s_mov_b32 s62, 0x3000\n"
    "s_mov_b32 s63, 0x20000\n"
    "s_lshl_b32 s73, s64, 1\n"
    "s_add_u32 s73, s73, 0\n"
    "s_lshl_b32 s67, s73, 7\n"
    "s_lshl_b32 s74, s73, 8\n"
    "buffer_load_dwordx2 v[32:33], v21, s[44:47], s67 offen offset:0\n"
    "buffer_load_dwordx2 v[34:35], v21, s[44:47], s67 offen offset:16\n"
    "s_add_u32 s75, s74, 0x1000\n"
    "buffer_load_dwordx4 v[40:43], v22, s[60:63], s75 offen offset:0 sc1\n"
    "buffer_load_dwordx4 v[44:47], v22, s[60:63], s75 offen offset:32 sc1\n"
    "s_add_u32 s75, s74, 0x1800\n"
    "buffer_load_dwordx4 v[56:59], v22, s[60:63], s75 offen offset:0 sc1\n"
    "buffer_load_dwordx4 v[60:63], v22, s[60:63], s75 offen offset:32 sc1\n"
    "s_add_u32 s75, s74, 0x2000\n"
    "buffer_load_dwordx4 v[72:75], v22, s[60:63], s75 offen offset:0 sc1\n"
    "buffer_load_dwordx4 v[76:79], v22, s[60:63], s75 offen offset:32 sc1\n"
    "s_lshl_b32 s73, s64, 1\n"
    "s_add_u32 s73, s73, 1\n"
    "s_lshl_b32 s68, s73, 7\n"
    "s_lshl_b32 s74, s73, 8\n"
    "buffer_load_dwordx2 v[36:37], v21, s[44:47], s68 offen offset:0\n"
    "buffer_load_dwordx2 v[38:39], v21, s[44:47], s68 offen offset:16\n"
    "s_add_u32 s75, s74, 0x1000\n"
    "buffer_load_dwordx4 v[48:51], v22, s[60:63], s75 offen offset:0 sc1\n"
    "buffer_load_dwordx4 v[52:55], v22, s[60:63], s75 offen offset:32 sc1\n"
    "s_add_u32 s75, s74, 0x1800\n"
    "buffer_load_dwordx4 v[64:67], v22, s[60:63], s75 offen offset:0 sc1\n"
    "buffer_load_dwordx4 v[68:71], v22, s[60:63], s75 offen offset:32 sc1\n"
    "s_add_u32 s75, s74, 0x2000\n"
    "buffer_load_dwordx4 v[80:83], v22, s[60:63], s75 offen offset:0 sc1\n"
    "buffer_load_dwordx4 v[84:87], v22, s[60:63], s75 offen offset:32 sc1\n"
    "s_add_u32 s92, s86, 0x2b00\n"
    "s_addc_u32 s93, s87, 0\n"
    "s_lshl_b32 s99, s70, 8\n"
    "s_add_u32 s92, s92, s99\n"
    "s_addc_u32 s93, s93, 0\n"
    "s_cmp_lg_u32 s64, 0\n"
    "s_cbranch_scc1 .Lpoll10d\n"
    "s_mov_b32 s94, 0\n"
    ".Lpoll10:\n"
    "s_load_dword s95, s[92:93], 0x0 glc\n"
    "s_waitcnt lgkmcnt(0)\n"
    "s_cmp_ge_u32 s95, 16\n"
    "s_cbranch_scc1 .Lpoll10d\n"
    "s_add_u32 s94, s94, 1\n"
    "s_cmp_gt_u32 s94, 400000\n"
    "s_cbranch_scc1 .Lpoll10d\n"
    "s_sleep 1\n"
    "s_branch .Lpoll10\n"
    ".Lpoll10d:\n"
    "s_barrier\n"
    "s_mov_b64 s[40:41], %0\n"
    "s_add_u32 s40, s40, 0x2220000\n"
    "s_addc_u32 s41, s41, 0\n"
    "s_lshl_b32 s73, s76, 15\n"
    "s_add_u32 s40, s40, s73\n"
    "s_addc_u32 s41, s41, 0\n"
    "s_and_b32 s41, s41, 0xffff\n"
    "s_mov_b32 s42, 0x800000\n"
    "s_mov_b32 s43, 0x20000\n"
    "s_lshl_b32 s73, s64, 13\n"
    "s_add_u32 s73, s73, 0\n"
    "buffer_load_dwordx4 v[96:99], v20, s[40:43], s73 offen offset:0 sc1\n"
    "s_lshl_b32 s73, s64, 13\n"
    "s_add_u32 s73, s73, 0x200000\n"
    "buffer_load_dwordx4 v[100:103], v20, s[40:43], s73 offen offset:0 sc1\n"
    "s_lshl_b32 s73, s64, 13\n"
    "s_add_u32 s73, s73, 0x400000\n"
    "buffer_load_dwordx4 v[104:107], v20, s[40:43], s73 offen offset:0 sc1\n"
    "s_lshl_b32 s73, s64, 13\n"
    "s_add_u32 s73, s73, 0x600000\n"
    "buffer_load_dwordx4 v[108:111], v20, s[40:43], s73 offen offset:0 sc1\n"
    "s_lshl_b32 s73, s64, 13\n"
    "s_add_u32 s73, s73, 0x1000\n"
    "buffer_load_dwordx4 v[112:115], v20, s[40:43], s73 offen offset:0 sc1\n"
    "s_lshl_b32 s73, s64, 13\n"
    "s_add_u32 s73, s73, 0x201000\n"
    "buffer_load_dwordx4 v[116:119], v20, s[40:43], s73 offen offset:0 sc1\n"
    "s_lshl_b32 s73, s64, 13\n"
    "s_add_u32 s73, s73, 0x401000\n"
    "buffer_load_dwordx4 v[120:123], v20, s[40:43], s73 offen offset:0 sc1\n"
    "s_lshl_b32 s73, s64, 13\n"
    "s_add_u32 s73, s73, 0x601000\n"
    "buffer_load_dwordx4 v[124:127], v20, s[40:43], s73 offen offset:0 sc1\n"
    "s_waitcnt vmcnt(7)\n"
    "v_lshlrev_b32 v128, 16, v96\n"
    "s_waitcnt vmcnt(6)\n"
    "v_lshlrev_b32 v152, 16, v100\n"
    "v_add_f32 v128, v128, v152\n"
    "s_waitcnt vmcnt(5)\n"
    "v_lshlrev_b32 v144, 16, v104\n"
    "v_add_f32 v128, v128, v144\n"
    "s_waitcnt vmcnt(4)\n"
    "v_lshlrev_b32 v152, 16, v108\n"
    "v_add_f32 v128, v128, v152\n"
    "v_and_b32 v129, 0xffff0000, v96\n"
    "v_and_b32 v153, 0xffff0000, v100\n"
    "v_add_f32 v129, v129, v153\n"
    "v_and_b32 v145, 0xffff0000, v104\n"
    "v_add_f32 v129, v129, v145\n"
    "v_and_b32 v153, 0xffff0000, v108\n"
    "v_add_f32 v129, v129, v153\n"
    "v_lshlrev_b32 v130, 16, v97\n"
    "v_lshlrev_b32 v154, 16, v101\n"
    "v_add_f32 v130, v130, v154\n"
    "v_lshlrev_b32 v146, 16, v105\n"
    "v_add_f32 v130, v130, v146\n"
    "v_lshlrev_b32 v154, 16, v109\n"
    "v_add_f32 v130, v130, v154\n"
    "v_and_b32 v131, 0xffff0000, v97\n"
    "v_and_b32 v155, 0xffff0000, v101\n"
    "v_add_f32 v131, v131, v155\n"
    "v_and_b32 v147, 0xffff0000, v105\n"
    "v_add_f32 v131, v131, v147\n"
    "v_and_b32 v155, 0xffff0000, v109\n"
    "v_add_f32 v131, v131, v155\n"
    "v_lshlrev_b32 v132, 16, v98\n"
    "v_lshlrev_b32 v156, 16, v102\n"
    "v_add_f32 v132, v132, v156\n"
    "v_lshlrev_b32 v148, 16, v106\n"
    "v_add_f32 v132, v132, v148\n"
    "v_lshlrev_b32 v156, 16, v110\n"
    "v_add_f32 v132, v132, v156\n"
    "v_and_b32 v133, 0xffff0000, v98\n"
    "v_and_b32 v157, 0xffff0000, v102\n"
    "v_add_f32 v133, v133, v157\n"
    "v_and_b32 v149, 0xffff0000, v106\n"
    "v_add_f32 v133, v133, v149\n"
    "v_and_b32 v157, 0xffff0000, v110\n"
    "v_add_f32 v133, v133, v157\n"
    "v_lshlrev_b32 v134, 16, v99\n"
    "v_lshlrev_b32 v158, 16, v103\n"
    "v_add_f32 v134, v134, v158\n"
    "v_lshlrev_b32 v150, 16, v107\n"
    "v_add_f32 v134, v134, v150\n"
    "v_lshlrev_b32 v158, 16, v111\n"
    "v_add_f32 v134, v134, v158\n"
    "v_and_b32 v135, 0xffff0000, v99\n"
    "v_and_b32 v159, 0xffff0000, v103\n"
    "v_add_f32 v135, v135, v159\n"
    "v_and_b32 v151, 0xffff0000, v107\n"
    "v_add_f32 v135, v135, v151\n"
    "v_and_b32 v159, 0xffff0000, v111\n"
    "v_add_f32 v135, v135, v159\n"
    "v_cvt_pk_bf16_f32 v144, v128, v129\n"
    "v_cvt_pk_bf16_f32 v145, v130, v131\n"
    "v_cvt_pk_bf16_f32 v146, v132, v133\n"
    "v_cvt_pk_bf16_f32 v147, v134, v135\n"
    "v_lshlrev_b32 v128, 16, v144\n"
    "v_and_b32 v129, 0xffff0000, v144\n"
    "v_lshlrev_b32 v130, 16, v145\n"
    "v_and_b32 v131, 0xffff0000, v145\n"
    "v_lshlrev_b32 v132, 16, v146\n"
    "v_and_b32 v133, 0xffff0000, v146\n"
    "v_lshlrev_b32 v134, 16, v147\n"
    "v_and_b32 v135, 0xffff0000, v147\n"
    "v_lshlrev_b32 v152, 16, v32\n"
    "v_and_b32 v153, 0xffff0000, v32\n"
    "v_lshlrev_b32 v154, 16, v33\n"
    "v_and_b32 v155, 0xffff0000, v33\n"
    "v_lshlrev_b32 v156, 16, v34\n"
    "v_and_b32 v157, 0xffff0000, v34\n"
    "v_lshlrev_b32 v158, 16, v35\n"
    "v_and_b32 v159, 0xffff0000, v35\n"
    "v_fma_f32 v128, v128, v40, v152\n"
    "v_fma_f32 v129, v129, v41, v153\n"
    "v_fma_f32 v130, v130, v42, v154\n"
    "v_fma_f32 v131, v131, v43, v155\n"
    "v_fma_f32 v132, v132, v44, v156\n"
    "v_fma_f32 v133, v133, v45, v157\n"
    "v_fma_f32 v134, v134, v46, v158\n"
    "v_fma_f32 v135, v135, v47, v159\n"
    "v_cvt_pk_bf16_f32 v160, v128, v129\n"
    "v_cvt_pk_bf16_f32 v161, v130, v131\n"
    "v_cvt_pk_bf16_f32 v162, v132, v133\n"
    "v_cvt_pk_bf16_f32 v163, v134, v135\n"
    "v_lshlrev_b32 v128, 16, v160\n"
    "v_and_b32 v129, 0xffff0000, v160\n"
    "v_lshlrev_b32 v130, 16, v161\n"
    "v_and_b32 v131, 0xffff0000, v161\n"
    "v_lshlrev_b32 v132, 16, v162\n"
    "v_and_b32 v133, 0xffff0000, v162\n"
    "v_lshlrev_b32 v134, 16, v163\n"
    "v_and_b32 v135, 0xffff0000, v163\n"
    "s_waitcnt vmcnt(3)\n"
    "v_lshlrev_b32 v136, 16, v112\n"
    "s_waitcnt vmcnt(2)\n"
    "v_lshlrev_b32 v152, 16, v116\n"
    "v_add_f32 v136, v136, v152\n"
    "s_waitcnt vmcnt(1)\n"
    "v_lshlrev_b32 v144, 16, v120\n"
    "v_add_f32 v136, v136, v144\n"
    "s_waitcnt vmcnt(0)\n"
    "v_lshlrev_b32 v152, 16, v124\n"
    "v_add_f32 v136, v136, v152\n"
    "v_and_b32 v137, 0xffff0000, v112\n"
    "v_and_b32 v153, 0xffff0000, v116\n"
    "v_add_f32 v137, v137, v153\n"
    "v_and_b32 v145, 0xffff0000, v120\n"
    "v_add_f32 v137, v137, v145\n"
    "v_and_b32 v153, 0xffff0000, v124\n"
    "v_add_f32 v137, v137, v153\n"
    "v_lshlrev_b32 v138, 16, v113\n"
    "v_lshlrev_b32 v154, 16, v117\n"
    "v_add_f32 v138, v138, v154\n"
    "v_lshlrev_b32 v146, 16, v121\n"
    "v_add_f32 v138, v138, v146\n"
    "v_lshlrev_b32 v154, 16, v125\n"
    "v_add_f32 v138, v138, v154\n"
    "v_and_b32 v139, 0xffff0000, v113\n"
    "v_and_b32 v155, 0xffff0000, v117\n"
    "v_add_f32 v139, v139, v155\n"
    "v_and_b32 v147, 0xffff0000, v121\n"
    "v_add_f32 v139, v139, v147\n"
    "v_and_b32 v155, 0xffff0000, v125\n"
    "v_add_f32 v139, v139, v155\n"
    "v_lshlrev_b32 v140, 16, v114\n"
    "v_lshlrev_b32 v156, 16, v118\n"
    "v_add_f32 v140, v140, v156\n"
    "v_lshlrev_b32 v148, 16, v122\n"
    "v_add_f32 v140, v140, v148\n"
    "v_lshlrev_b32 v156, 16, v126\n"
    "v_add_f32 v140, v140, v156\n"
    "v_and_b32 v141, 0xffff0000, v114\n"
    "v_and_b32 v157, 0xffff0000, v118\n"
    "v_add_f32 v141, v141, v157\n"
    "v_and_b32 v149, 0xffff0000, v122\n"
    "v_add_f32 v141, v141, v149\n"
    "v_and_b32 v157, 0xffff0000, v126\n"
    "v_add_f32 v141, v141, v157\n"
    "v_lshlrev_b32 v142, 16, v115\n"
    "v_lshlrev_b32 v158, 16, v119\n"
    "v_add_f32 v142, v142, v158\n"
    "v_lshlrev_b32 v150, 16, v123\n"
    "v_add_f32 v142, v142, v150\n"
    "v_lshlrev_b32 v158, 16, v127\n"
    "v_add_f32 v142, v142, v158\n"
    "v_and_b32 v143, 0xffff0000, v115\n"
    "v_and_b32 v159, 0xffff0000, v119\n"
    "v_add_f32 v143, v143, v159\n"
    "v_and_b32 v151, 0xffff0000, v123\n"
    "v_add_f32 v143, v143, v151\n"
    "v_and_b32 v159, 0xffff0000, v127\n"
    "v_add_f32 v143, v143, v159\n"
    "v_cvt_pk_bf16_f32 v144, v136, v137\n"
    "v_cvt_pk_bf16_f32 v145, v138, v139\n"
    "v_cvt_pk_bf16_f32 v146, v140, v141\n"
    "v_cvt_pk_bf16_f32 v147, v142, v143\n"
    "v_lshlrev_b32 v136, 16, v144\n"
    "v_and_b32 v137, 0xffff0000, v144\n"
    "v_lshlrev_b32 v138, 16, v145\n"
    "v_and_b32 v139, 0xffff0000, v145\n"
    "v_lshlrev_b32 v140, 16, v146\n"
    "v_and_b32 v141, 0xffff0000, v146\n"
    "v_lshlrev_b32 v142, 16, v147\n"
    "v_and_b32 v143, 0xffff0000, v147\n"
    "v_lshlrev_b32 v152, 16, v36\n"
    "v_and_b32 v153, 0xffff0000, v36\n"
    "v_lshlrev_b32 v154, 16, v37\n"
    "v_and_b32 v155, 0xffff0000, v37\n"
    "v_lshlrev_b32 v156, 16, v38\n"
    "v_and_b32 v157, 0xffff0000, v38\n"
    "v_lshlrev_b32 v158, 16, v39\n"
    "v_and_b32 v159, 0xffff0000, v39\n"
    "v_fma_f32 v136, v136, v48, v152\n"
    "v_fma_f32 v137, v137, v49, v153\n"
    "v_fma_f32 v138, v138, v50, v154\n"
    "v_fma_f32 v139, v139, v51, v155\n"
    "v_fma_f32 v140, v140, v52, v156\n"
    "v_fma_f32 v141, v141, v53, v157\n"
    "v_fma_f32 v142, v142, v54, v158\n"
    "v_fma_f32 v143, v143, v55, v159\n"
    "v_cvt_pk_bf16_f32 v164, v136, v137\n"
    "v_cvt_pk_bf16_f32 v165, v138, v139\n"
    "v_cvt_pk_bf16_f32 v166, v140, v141\n"
    "v_cvt_pk_bf16_f32 v167, v142, v143\n"
    "v_lshlrev_b32 v136, 16, v164\n"
    "v_and_b32 v137, 0xffff0000, v164\n"
    "v_lshlrev_b32 v138, 16, v165\n"
    "v_and_b32 v139, 0xffff0000, v165\n"
    "v_lshlrev_b32 v140, 16, v166\n"
    "v_and_b32 v141, 0xffff0000, v166\n"
    "v_lshlrev_b32 v142, 16, v167\n"
    "v_and_b32 v143, 0xffff0000, v167\n"
    "s_mov_b64 s[60:61], %0\n"
    "s_add_u32 s60, s60, 0x1220000\n"
    "s_addc_u32 s61, s61, 0\n"
    "s_lshl_b32 s73, s76, 15\n"
    "s_add_u32 s60, s60, s73\n"
    "s_addc_u32 s61, s61, 0\n"
    "s_and_b32 s61, s61, 0xffff\n"
    "s_mov_b32 s62, 0x8000\n"
    "s_mov_b32 s63, 0x20000\n"
    "s_lshl_b32 s67, s64, 13\n"
    "s_add_u32 s68, s67, 0x1000\n"
    "buffer_store_dwordx4 v[160:163], v20, s[60:63], s67 offen offset:0 sc1\n"
    "buffer_store_dwordx4 v[164:167], v20, s[60:63], s68 offen offset:0 sc1\n"
    "v_add_f32 v168, v128, v129\n"
    "v_add_f32 v169, v130, v131\n"
    "v_add_f32 v168, v168, v132\n"
    "v_add_f32 v169, v169, v133\n"
    "v_add_f32 v168, v168, v134\n"
    "v_add_f32 v169, v169, v135\n"
    "v_add_f32 v168, v168, v136\n"
    "v_add_f32 v169, v169, v137\n"
    "v_add_f32 v168, v168, v138\n"
    "v_add_f32 v169, v169, v139\n"
    "v_add_f32 v168, v168, v140\n"
    "v_add_f32 v169, v169, v141\n"
    "v_add_f32 v168, v168, v142\n"
    "v_add_f32 v169, v169, v143\n"
    "v_add_f32 v168, v168, v169\n"
    "s_nop 1\n"
    "v_add_f32_dpp v168, v168, v168 row_ror:8 row_mask:0xf bank_mask:0xf\n"
    "v_mov_b32 v169, v168\n"
    "s_nop 1\n"
    "v_permlane16_swap_b32 v168, v169\n"
    "v_add_f32 v168, v168, v169\n"
    "v_mov_b32 v169, v168\n"
    "s_nop 1\n"
    "v_permlane32_swap_b32 v168, v169\n"
    "v_add_f32 v168, v168, v169\n"
    "v_and_b32 v11, 7, v1\n"
    "v_lshlrev_b32 v11, 2, v11\n"
    "v_add_u32 v171, 0x24000, v11\n"
    "s_lshl_b32 s73, s64, 5\n"
    "v_add_u32 v170, s73, v171\n"
    "ds_write_b32 v170, v168 offset:0\n"
    "s_waitcnt lgkmcnt(0)\n"
    "s_barrier\n"
    "ds_read_b32 v172, v171 offset:0\n"
    "ds_read_b32 v173, v171 offset:32\n"
    "ds_read_b32 v174, v171 offset:64\n"
    "ds_read_b32 v175, v171 offset:96\n"
    "s_waitcnt lgkmcnt(2)\n"
    "v_add_f32 v172, v172, v173\n"
    "s_waitcnt lgkmcnt(0)\n"
    "v_add_f32 v174, v174, v175\n"
    "v_add_f32 v176, v172, v174\n"
    "v_mul_f32 v176, 0x3b000000, v176\n"
    "v_sub_f32 v128, v128, v176\n"
    "v_sub_f32 v129, v129, v176\n"
    "v_sub_f32 v130, v130, v176\n"
    "v_sub_f32 v131, v131, v176\n"
    "v_sub_f32 v132, v132, v176\n"
    "v_sub_f32 v133, v133, v176\n"
    "v_sub_f32 v134, v134, v176\n"
    "v_sub_f32 v135, v135, v176\n"
    "v_sub_f32 v136, v136, v176\n"
    "v_sub_f32 v137, v137, v176\n"
    "v_sub_f32 v138, v138, v176\n"
    "v_sub_f32 v139, v139, v176\n"
    "v_sub_f32 v140, v140, v176\n"
    "v_sub_f32 v141, v141, v176\n"
    "v_sub_f32 v142, v142, v176\n"
    "v_sub_f32 v143, v143, v176\n"
    "v_mul_f32 v168, v128, v128\n"
    "v_mul_f32 v169, v129, v129\n"
    "v_fmac_f32 v168, v130, v130\n"
    "v_fmac_f32 v169, v131, v131\n"
    "v_fmac_f32 v168, v132, v132\n"
    "v_fmac_f32 v169, v133, v133\n"
    "v_fmac_f32 v168, v134, v134\n"
    "v_fmac_f32 v169, v135, v135\n"
    "v_fmac_f32 v168, v136, v136\n"
    "v_fmac_f32 v169, v137, v137\n"
    "v_fmac_f32 v168, v138, v138\n"
    "v_fmac_f32 v169, v139, v139\n"
    "v_fmac_f32 v168, v140, v140\n"
    "v_fmac_f32 v169, v141, v141\n"
    "v_fmac_f32 v168, v142, v142\n"
    "v_fmac_f32 v169, v143, v143\n"
    "v_add_f32 v168, v168, v169\n"
    "s_nop 1\n"
    "v_add_f32_dpp v168, v168, v168 row_ror:8 row_mask:0xf bank_mask:0xf\n"
    "v_mov_b32 v169, v168\n"
    "s_nop 1\n"
    "v_permlane16_swap_b32 v168, v169\n"
    "v_add_f32 v168, v168, v169\n"
    "v_mov_b32 v169, v168\n"
    "s_nop 1\n"
    "v_permlane32_swap_b32 v168, v169\n"
    "v_add_f32 v168, v168, v169\n"
    "ds_write_b32 v170, v168 offset:128\n"
    "s_waitcnt lgkmcnt(0)\n"
    "s_barrier\n"
    "ds_read_b32 v172, v171 offset:128\n"
    "ds_read_b32 v173, v171 offset:160\n"
    "ds_read_b32 v174, v171 offset:192\n"
    "ds_read_b32 v175, v171 offset:224\n"
    "s_waitcnt lgkmcnt(2)\n"
    "v_add_f32 v172, v172, v173\n"
    "s_waitcnt lgkmcnt(0)\n"
    "v_add_f32 v174, v174, v175\n"
    "v_add_f32 v177, v172, v174\n"
    "v_mov_b32 v169, 0x3727c5ac\n"
    "v_fmac_f32 v169, 0x3b000000, v177\n"
    "v_rsq_f32 v177, v169\n"
    "s_nop 0\n"
    "v_mul_f32 v128, v128, v177\n"
    "v_mul_f32 v129, v129, v177\n"
    "v_mul_f32 v130, v130, v177\n"
    "v_mul_f32 v131, v131, v177\n"
    "v_mul_f32 v132, v132, v177\n"
    "v_mul_f32 v133, v133, v177\n"
    "v_mul_f32 v134, v134, v177\n"
    "v_mul_f32 v135, v135, v177\n"
    "v_fma_f32 v128, v128, v72, v56\n"
    "v_fma_f32 v129, v129, v73, v57\n"
    "v_fma_f32 v130, v130, v74, v58\n"
    "v_fma_f32 v131, v131, v75, v59\n"
    "v_fma_f32 v132, v132, v76, v60\n"
    "v_fma_f32 v133, v133, v77, v61\n"
    "v_fma_f32 v134, v134, v78, v62\n"
    "v_fma_f32 v135, v135, v79, v63\n"
    "v_cvt_pk_bf16_f32 v180, v128, v129\n"
    "v_cvt_pk_bf16_f32 v181, v130, v131\n"
    "v_cvt_pk_bf16_f32 v182, v132, v133\n"
    "v_cvt_pk_bf16_f32 v183, v134, v135\n"
    "v_mul_f32 v136, v136, v177\n"
    "v_mul_f32 v137, v137, v177\n"
    "v_mul_f32 v138, v138, v177\n"
    "v_mul_f32 v139, v139, v177\n"
    "v_mul_f32 v140, v140, v177\n"
    "v_mul_f32 v141, v141, v177\n"
    "v_mul_f32 v142, v142, v177\n"
    "v_mul_f32 v143, v143, v177\n"
    "v_fma_f32 v136, v136, v80, v64\n"
    "v_fma_f32 v137, v137, v81, v65\n"
    "v_fma_f32 v138, v138, v82, v66\n"
    "v_fma_f32 v139, v139, v83, v67\n"
    "v_fma_f32 v140, v140, v84, v68\n"
    "v_fma_f32 v141, v141, v85, v69\n"
    "v_fma_f32 v142, v142, v86, v70\n"
    "v_fma_f32 v143, v143, v87, v71\n"
    "v_cvt_pk_bf16_f32 v184, v136, v137\n"
    "v_cvt_pk_bf16_f32 v185, v138, v139\n"
    "v_cvt_pk_bf16_f32 v186, v140, v141\n"
    "v_cvt_pk_bf16_f32 v187, v142, v143\n"
    "s_mov_b64 s[40:41], %0\n"
    "s_add_u32 s40, s40, 0x1620000\n"
    "s_addc_u32 s41, s41, 0\n"
    "s_lshl_b32 s73, s76, 15\n"
    "s_add_u32 s40, s40, s73\n"
    "s_addc_u32 s41, s41, 0\n"
    "s_and_b32 s41, s41, 0xffff\n"
    "s_mov_b32 s42, 0x8000\n"
    "s_mov_b32 s43, 0x20000\n"
    "buffer_store_dwordx4 v[180:183], v20, s[40:43], s67 offen offset:0 sc1\n"
    "buffer_store_dwordx4 v[184:187], v20, s[40:43], s68 offen offset:0 sc1\n"
    "s_add_u32 s92, s86, 0x3b00\n"
    "s_addc_u32 s93, s87, 0\n"
    "s_lshl_b32 s99, s70, 8\n"
    "s_add_u32 s92, s92, s99\n"
    "s_addc_u32 s93, s93, 0\n"
    "s_waitcnt vmcnt(0)\n"
    "s_barrier\n"
    "s_cmp_lg_u32 s64, 0\n"
    "s_cbranch_scc1 .Lsig11\n"
    "v_mov_b32 v13, 0\n"
    "v_mov_b32 v14, 1\n"
    "s_mov_b64 exec, 1\n"
    "global_atomic_add v13, v14, s[92:93]\n"
    "s_mov_b64 exec, -1\n"
    ".Lsig11:\n"
    "s_lshl_b32 s76, s70, 2\n"
    "s_add_u32 s76, s76, s64\n"
    "v_lshlrev_b32 v2, 4, v1\n"
    "v_bfe_u32 v32, v1, 2, 2\n"
    "v_bfe_u32 v33, v1, 1, 1\n"
    "v_lshl_add_u32 v34, v33, 2, v32\n"
    "v_lshlrev_b32 v35, 14, v33\n"
    "v_lshl_add_u32 v35, v32, 8, v35\n"
    "v_lshrrev_b32 v32, 5, v1\n"
    "v_lshl_add_u32 v35, v32, 10, v35\n"
    "v_bfe_u32 v32, v1, 4, 1\n"
    "v_lshl_add_u32 v35, v32, 4, v35\n"
    "v_and_b32 v32, 1, v1\n"
    "v_lshl_add_u32 v35, v32, 3, v35\n"
    "v_add_u32 v35, 0x10000, v35\n"
    "v_xor_b32 v32, 0, v34\n"
    "v_lshl_add_u32 v20, v32, 5, v35\n"
    "v_xor_b32 v32, 1, v34\n"
    "v_lshl_add_u32 v21, v32, 5, v35\n"
    "v_xor_b32 v32, 2, v34\n"
    "v_lshl_add_u32 v22, v32, 5, v35\n"
    "v_xor_b32 v32, 3, v34\n"
    "v_lshl_add_u32 v23, v32, 5, v35\n"
    "v_xor_b32 v32, 4, v34\n"
    "v_lshl_add_u32 v24, v32, 5, v35\n"
    "v_xor_b32 v32, 5, v34\n"
    "v_lshl_add_u32 v25, v32, 5, v35\n"
    "v_xor_b32 v32, 6, v34\n"
    "v_lshl_add_u32 v26, v32, 5, v35\n"
    "s_mul_i32 s66, s64, 0xe000\n"
    "s_add_u32 s66, s66, 0xe0000\n"
    "s_add_u32 m0, s69, 0x20000\n"
    "s_nop 0\n"
    "buffer_load_dwordx4 v4, s[52:55], s66 offen offset:0 lds\n"
    "buffer_load_dwordx4 v5, s[52:55], s66 offen offset:1024 lds\n"
    "buffer_load_dwordx4 v6, s[52:55], s66 offen offset:2048 lds\n"
    "buffer_load_dwordx4 v7, s[52:55], s66 offen offset:3072 lds\n"
    "s_add_u32 m0, s69, 0x24000\n"
    "s_nop 0\n"
    "buffer_load_dwordx4 v16, s[56:59], s66 offen offset:0 lds\n"
    "buffer_load_dwordx4 v17, s[56:59], s66 offen offset:1024 lds\n"
    "buffer_load_dwordx4 v18, s[56:59], s66 offen offset:2048 lds\n"
    "buffer_load_dwordx4 v19, s[56:59], s66 offen offset:3072 lds\n"
    "s_add_u32 s92, s86, 0x3b00\n"
    "s_addc_u32 s93, s87, 0\n"
    "s_lshl_b32 s99, s70, 8\n"
    "s_add_u32 s92, s92, s99\n"
    "s_addc_u32 s93, s93, 0\n"
    "s_cmp_lg_u32 s64, 0\n"
    "s_cbranch_scc1 .Lpoll12d\n"
    "s_mov_b32 s94, 0\n"
    ".Lpoll12:\n"
    "s_load_dword s95, s[92:93], 0x0 glc\n"
    "s_waitcnt lgkmcnt(0)\n"
    "s_cmp_ge_u32 s95, 16\n"
    "s_cbranch_scc1 .Lpoll12d\n"
    "s_add_u32 s94, s94, 1\n"
    "s_cmp_gt_u32 s94, 400000\n"
    "s_cbranch_scc1 .Lpoll12d\n"
    "s_sleep 1\n"
    "s_branch .Lpoll12\n"
    ".Lpoll12d:\n"
    "s_barrier\n"
    "s_mov_b64 s[40:41], %0\n"
    "s_add_u32 s40, s40, 0x1620000\n"
    "s_addc_u32 s41, s41, 0\n"
    "s_lshl_b32 s73, s76, 15\n"
    "s_add_u32 s40, s40, s73\n"
    "s_addc_u32 s41, s41, 0\n"
    "s_and_b32 s41, s41, 0xffff\n"
    "s_mov_b32 s42, 0x8000\n"
    "s_mov_b32 s43, 0x20000\n"
    "s_mov_b64 s[44:45], %0\n"
    "s_add_u32 s44, s44, 0x1a20000\n"
    "s_addc_u32 s45, s45, 0\n"
    "s_mul_i32 s73, s76, 0x1c000\n"
    "s_add_u32 s44, s44, s73\n"
    "s_addc_u32 s45, s45, 0\n"
    "s_mul_i32 s73, s79, 0x1c00\n"
    "s_add_u32 s44, s44, s73\n"
    "s_addc_u32 s45, s45, 0\n"
    "s_and_b32 s45, s45, 0xffff\n"
    "s_mov_b32 s46, 0x1c00\n"
    "s_mov_b32 s47, 0x20000\n"
    "s_mov_b32 s77, 0\n"
    "buffer_load_dwordx4 v[48:51], v2, s[40:43], s77 offen offset:0 sc1\n"
    "s_mov_b32 s77, 0\n"
    "buffer_load_dwordx4 v[52:55], v2, s[40:43], s77 offen offset:1024 sc1\n"
    "s_mov_b32 s77, 0\n"
    "buffer_load_dwordx4 v[56:59], v2, s[40:43], s77 offen offset:2048 sc1\n"
    "s_mov_b32 s77, 0\n"
    "buffer_load_dwordx4 v[60:63], v2, s[40:43], s77 offen offset:3072 sc1\n"
    "s_mov_b32 s77, 0x1000\n"
    "buffer_load_dwordx4 v[64:67], v2, s[40:43], s77 offen offset:0 sc1\n"
    "s_mov_b32 s77, 0x1000\n"
    "buffer_load_dwordx4 v[68:71], v2, s[40:43], s77 offen offset:1024 sc1\n"
    "s_mov_b32 s77, 0x1000\n"
    "buffer_load_dwordx4 v[72:75], v2, s[40:43], s77 offen offset:2048 sc1\n"
    "s_mov_b32 s77, 0x1000\n"
    "buffer_load_dwordx4 v[76:79], v2, s[40:43], s77 offen offset:3072 sc1\n"
    "s_mov_b32 s77, 0x2000\n"
    "buffer_load_dwordx4 v[80:83], v2, s[40:43], s77 offen offset:0 sc1\n"
    "s_mov_b32 s77, 0x2000\n"
    "buffer_load_dwordx4 v[84:87], v2, s[40:43], s77 offen offset:1024 sc1\n"
    "s_mov_b32 s77, 0x2000\n"
    "buffer_load_dwordx4 v[88:91], v2, s[40:43], s77 offen offset:2048 sc1\n"
    "s_mov_b32 s77, 0x2000\n"
    "buffer_load_dwordx4 v[92:95], v2, s[40:43], s77 offen offset:3072 sc1\n"
    "s_mov_b32 s77, 0x3000\n"
    "buffer_load_dwordx4 v[96:99], v2, s[40:43], s77 offen offset:0 sc1\n"
    "s_mov_b32 s77, 0x3000\n"
    "buffer_load_dwordx4 v[100:103], v2, s[40:43], s77 offen offset:1024 sc1\n"
    "s_mov_b32 s77, 0x3000\n"
    "buffer_load_dwordx4 v[104:107], v2, s[40:43], s77 offen offset:2048 sc1\n"
    "ds_read_b64_tr_b16 a[0:1], v20 offset:0\n"
    "ds_read_b64_tr_b16 a[2:3], v20 offset:2048\n"
    "ds_read_b64_tr_b16 a[4:5], v21 offset:0\n"
    "ds_read_b64_tr_b16 a[6:7], v21 offset:2048\n"
    "ds_read_b64_tr_b16 a[8:9], v22 offset:0\n"
    "ds_read_b64_tr_b16 a[10:11], v22 offset:2048\n"
    "ds_read_b64_tr_b16 a[12:13], v23 offset:0\n"
    "ds_read_b64_tr_b16 a[14:15], v23 offset:2048\n"
    "ds_read_b64_tr_b16 a[16:17], v24 offset:0\n"
    "ds_read_b64_tr_b16 a[18:19], v24 offset:2048\n"
    "ds_read_b64_tr_b16 a[20:21], v25 offset:0\n"
    "ds_read_b64_tr_b16 a[22:23], v25 offset:2048\n"
    "ds_read_b64_tr_b16 a[24:25], v26 offset:0\n"
    "ds_read_b64_tr_b16 a[26:27], v26 offset:2048\n"
    "s_waitcnt lgkmcnt(12)\n"
    "s_waitcnt vmcnt(14)\n"
    "v_mfma_f32_32x32x16_bf16 v[128:143], a[0:3], v[48:51], 0\n"
    "ds_read_b64_tr_b16 a[32:33], v20 offset:4096\n"
    "ds_read_b64_tr_b16 a[34:35], v20 offset:6144\n"
    "s_waitcnt lgkmcnt(12)\n"
    "v_mfma_f32_32x32x16_bf16 v[144:159], a[4:7], v[48:51], 0\n"
    "ds_read_b64_tr_b16 a[36:37], v21 offset:4096\n"
    "ds_read_b64_tr_b16 a[38:39], v21 offset:6144\n"
    "s_waitcnt lgkmcnt(12)\n"
    "v_mfma_f32_32x32x16_bf16 v[160:175], a[8:11], v[48:51], 0\n"
    "ds_read_b64_tr_b16 a[40:41], v22 offset:4096\n"
    "ds_read_b64_tr_b16 a[42:43], v22 offset:6144\n"
    "s_waitcnt lgkmcnt(12)\n"
    "v_mfma_f32_32x32x16_bf16 v[176:191], a[12:15], v[48:51], 0\n"
    "ds_read_b64_tr_b16 a[44:45], v23 offset:4096\n"
    "ds_read_b64_tr_b16 a[46:47], v23 offset:6144\n"
    "s_mov_b32 s77, 0x3000\n"
    "buffer_load_dwordx4 v[108:111], v2, s[40:43], s77 offen offset:3072 sc1\n"
    "s_waitcnt lgkmcnt(12)\n"
    "v_mfma_f32_32x32x16_bf16 v[192:207], a[16:19], v[48:51], 0\n"
    "ds_read_b64_tr_b16 a[48:49], v24 offset:4096\n"
    "ds_read_b64_tr_b16 a[50:51], v24 offset:6144\n"
    "s_waitcnt lgkmcnt(12)\n"
    "v_mfma_f32_32x32x16_bf16 v[208:223], a[20:23], v[48:51], 0\n"
    "ds_read_b64_tr_b16 a[52:53], v25 offset:4096\n"
    "ds_read_b64_tr_b16 a[54:55], v25 offset:6144\n"
    "s_waitcnt lgkmcnt(12)\n"
    "v_mfma_f32_32x32x16_bf16 v[224:239], a[24:27], v[48:51], 0\n"
    "ds_read_b64_tr_b16 a[56:57], v26 offset:4096\n"
    "ds_read_b64_tr_b16 a[58:59], v26 offset:6144\n"
    "s_waitcnt lgkmcnt(12)\n"
    "s_waitcnt vmcnt(14)\n"
    "v_mfma_f32_32x32x16_bf16 v[128:143], a[32:35], v[52:55], v[128:143]\n"
    "ds_read_b64_tr_b16 a[0:1], v20 offset:8192\n"
    "ds_read_b64_tr_b16 a[2:3], v20 offset:10240\n"
    "s_waitcnt lgkmcnt(12)\n"
    "v_mfma_f32_32x32x16_bf16 v[144:159], a[36:39], v[52:55], v[144:159]\n"
    "ds_read_b64_tr_b16 a[4:5], v21 offset:8192\n"
    "ds_read_b64_tr_b16 a[6:7], v21 offset:10240\n"
    "s_waitcnt lgkmcnt(12)\n"
    "v_mfma_f32_32x32x16_bf16 v[160:175], a[40:43], v[52:55], v[160:175]\n"
    "ds_read_b64_tr_b16 a[8:9], v22 offset:8192\n"
    "ds_read_b64_tr_b16 a[10:11], v22 offset:10240\n"
    "s_waitcnt lgkmcnt(12)\n"
    "v_mfma_f32_32x32x16_bf16 v[176:191], a[44:47], v[52:55], v[176:191]\n"
    "ds_read_b64_tr_b16 a[12:13], v23 offset:8192\n"
    "ds_read_b64_tr_b16 a[14:15], v23 offset:10240\n"
    "s_mov_b32 s77, 0x4000\n"
    "buffer_load_dwordx4 v[48:51], v2, s[40:43], s77 offen offset:0 sc1\n"
    "s_waitcnt lgkmcnt(12)\n"
    "v_mfma_f32_32x32x16_bf16 v[192:207], a[48:51], v[52:55], v[192:207]\n"
    "ds_read_b64_tr_b16 a[16:17], v24 offset:8192\n"
    "ds_read_b64_tr_b16 a[18:19], v24 offset:10240\n"
    "s_waitcnt lgkmcnt(12)\n"
    "v_mfma_f32_32x32x16_bf16 v[208:223], a[52:55], v[52:55], v[208:223]\n"
    "ds_read_b64_tr_b16 a[20:21], v25 offset:8192\n"
    "ds_read_b64_tr_b16 a[22:23], v25 offset:10240\n"
    "s_waitcnt lgkmcnt(12)\n"
    "v_mfma_f32_32x32x16_bf16 v[224:239], a[56:59], v[52:55], v[224:239]\n"
    "ds_read_b64_tr_b16 a[24:25], v26 offset:8192\n"
    "ds_read_b64_tr_b16 a[26:27], v26 offset:10240\n"
    "s_waitcnt lgkmcnt(12)\n"
    "s_waitcnt vmcnt(14)\n"
    "v_mfma_f32_32x32x16_bf16 v[128:143], a[0:3], v[56:59], v[128:143]\n"
    "ds_read_b64_tr_b16 a[32:33], v20 offset:12288\n"
    "ds_read_b64_tr_b16 a[34:35], v20 offset:14336\n"
    "s_waitcnt lgkmcnt(12)\n"
    "v_mfma_f32_32x32x16_bf16 v[144:159], a[4:7], v[56:59], v[144:159]\n"
    "ds_read_b64_tr_b16 a[36:37], v21 offset:12288\n"
    "ds_read_b64_tr_b16 a[38:39], v21 offset:14336\n"
    "s_waitcnt lgkmcnt(12)\n"
    "v_mfma_f32_32x32x16_bf16 v[160:175], a[8:11], v[56:59], v[160:175]\n"
    "ds_read_b64_tr_b16 a[40:41], v22 offset:12288\n"
    "ds_read_b64_tr_b16 a[42:43], v22 offset:14336\n"
    "s_waitcnt lgkmcnt(12)\n"
    "v_mfma_f32_32x32x16_bf16 v[176:191], a[12:15], v[56:59], v[176:191]\n"
    "ds_read_b64_tr_b16 a[44:45], v23 offset:12288\n"
    "ds_read_b64_tr_b16 a[46:47], v23 offset:14336\n"
    "s_mov_b32 s77, 0x4000\n"
    "buffer_load_dwordx4 v[52:55], v2, s[40:43], s77 offen offset:1024 sc1\n"
    "s_waitcnt lgkmcnt(12)\n"
    "v_mfma_f32_32x32x16_bf16 v[192:207], a[16:19], v[56:59], v[192:207]\n"
    "ds_read_b64_tr_b16 a[48:49], v24 offset:12288\n"
    "ds_read_b64_tr_b16 a[50:51], v24 offset:14336\n"
    "s_waitcnt lgkmcnt(12)\n"
    "v_mfma_f32_32x32x16_bf16 v[208:223], a[20:23], v[56:59], v[208:223]\n"
    "ds_read_b64_tr_b16 a[52:53], v25 offset:12288\n"
    "ds_read_b64_tr_b16 a[54:55], v25 offset:14336\n"
    "s_waitcnt lgkmcnt(12)\n"
    "v_mfma_f32_32x32x16_bf16 v[224:239], a[24:27], v[56:59], v[224:239]\n"
    "ds_read_b64_tr_b16 a[56:57], v26 offset:12288\n"
    "ds_read_b64_tr_b16 a[58:59], v26 offset:14336\n"
    "s_waitcnt lgkmcnt(12)\n"
    "s_waitcnt vmcnt(14)\n"
    "v_mfma_f32_32x32x16_bf16 v[128:143], a[32:35], v[60:63], v[128:143]\n"
    "s_barrier\n"
    "v_add_u32 v20, 0xffff0000, v20\n"
    "v_add_u32 v21, 0xffff0000, v21\n"
    "v_add_u32 v22, 0xffff0000, v22\n"
    "v_add_u32 v23, 0xffff0000, v23\n"
    "v_add_u32 v24, 0xffff0000, v24\n"
    "v_add_u32 v25, 0xffff0000, v25\n"
    "v_add_u32 v26, 0xffff0000, v26\n"
    "ds_read_b64_tr_b16 a[0:1], v20 offset:32768\n"
    "ds_read_b64_tr_b16 a[2:3], v20 offset:34816\n"
    "s_waitcnt lgkmcnt(12)\n"
    "v_mfma_f32_32x32x16_bf16 v[144:159], a[36:39], v[60:63], v[144:159]\n"
    "ds_read_b64_tr_b16 a[4:5], v21 offset:32768\n"
    "ds_read_b64_tr_b16 a[6:7], v21 offset:34816\n"
    "s_waitcnt lgkmcnt(12)\n"
    "v_mfma_f32_32x32x16_bf16 v[160:175], a[40:43], v[60:63], v[160:175]\n"
    "ds_read_b64_tr_b16 a[8:9], v22 offset:32768\n"
    "ds_read_b64_tr_b16 a[10:11], v22 offset:34816\n"
    "s_waitcnt lgkmcnt(12)\n"
    "v_mfma_f32_32x32x16_bf16 v[176:191], a[44:47], v[60:63], v[176:191]\n"
    "ds_read_b64_tr_b16 a[12:13], v23 offset:32768\n"
    "ds_read_b64_tr_b16 a[14:15], v23 offset:34816\n"
    "s_mov_b32 s77, 0x4000\n"
    "buffer_load_dwordx4 v[56:59], v2, s[40:43], s77 offen offset:2048 sc1\n"
    "s_waitcnt lgkmcnt(12)\n"
    "v_mfma_f32_32x32x16_bf16 v[192:207], a[48:51], v[60:63], v[192:207]\n"
    "ds_read_b64_tr_b16 a[16:17], v24 offset:32768\n"
    "ds_read_b64_tr_b16 a[18:19], v24 offset:34816\n"
    "s_waitcnt lgkmcnt(12)\n"
    "v_mfma_f32_32x32x16_bf16 v[208:223], a[52:55], v[60:63], v[208:223]\n"
    "ds_read_b64_tr_b16 a[20:21], v25 offset:32768\n"
    "ds_read_b64_tr_b16 a[22:23], v25 offset:34816\n"
    "s_waitcnt lgkmcnt(12)\n"
    "v_mfma_f32_32x32x16_bf16 v[224:239], a[56:59], v[60:63], v[224:239]\n"
    "ds_read_b64_tr_b16 a[24:25], v26 offset:32768\n"
    "ds_read_b64_tr_b16 a[26:27], v26 offset:34816\n"
    "s_waitcnt lgkmcnt(12)\n"
    "s_waitcnt vmcnt(14)\n"
    "v_mfma_f32_32x32x16_bf16 v[128:143], a[0:3], v[64:67], v[128:143]\n"
    "ds_read_b64_tr_b16 a[32:33], v20 offset:36864\n"
    "ds_read_b64_tr_b16 a[34:35], v20 offset:38912\n"
    "s_waitcnt lgkmcnt(12)\n"
    "v_mfma_f32_32x32x16_bf16 v[144:159], a[4:7], v[64:67], v[144:159]\n"
    "ds_read_b64_tr_b16 a[36:37], v21 offset:36864\n"
    "ds_read_b64_tr_b16 a[38:39], v21 offset:38912\n"
    "s_waitcnt lgkmcnt(12)\n"
    "v_mfma_f32_32x32x16_bf16 v[160:175], a[8:11], v[64:67], v[160:175]\n"
    "ds_read_b64_tr_b16 a[40:41], v22 offset:36864\n"
    "ds_read_b64_tr_b16 a[42:43], v22 offset:38912\n"
    "s_waitcnt lgkmcnt(12)\n"
    "v_mfma_f32_32x32x16_bf16 v[176:191], a[12:15], v[64:67], v[176:191]\n"
    "ds_read_b64_tr_b16 a[44:45], v23 offset:36864\n"
    "ds_read_b64_tr_b16 a[46:47], v23 offset:38912\n"
    "s_mov_b32 s77, 0x4000\n"
    "buffer_load_dwordx4 v[60:63], v2, s[40:43], s77 offen offset:3072 sc1\n"
    "s_waitcnt lgkmcnt(12)\n"
    "v_mfma_f32_32x32x16_bf16 v[192:207], a[16:19], v[64:67], v[192:207]\n"
    "ds_read_b64_tr_b16 a[48:49], v24 offset:36864\n"
    "ds_read_b64_tr_b16 a[50:51], v24 offset:38912\n"
    "s_waitcnt lgkmcnt(12)\n"
    "v_mfma_f32_32x32x16_bf16 v[208:223], a[20:23], v[64:67], v[208:223]\n"
    "ds_read_b64_tr_b16 a[52:53], v25 offset:36864\n"
    "ds_read_b64_tr_b16 a[54:55], v25 offset:38912\n"
    "s_waitcnt lgkmcnt(12)\n"
    "v_mfma_f32_32x32x16_bf16 v[224:239], a[24:27], v[64:67], v[224:239]\n"
    "ds_read_b64_tr_b16 a[56:57], v26 offset:36864\n"
    "ds_read_b64_tr_b16 a[58:59], v26 offset:38912\n"
    "s_waitcnt lgkmcnt(12)\n"
    "s_waitcnt vmcnt(14)\n"
    "v_mfma_f32_32x32x16_bf16 v[128:143], a[32:35], v[68:71], v[128:143]\n"
    "ds_read_b64_tr_b16 a[0:1], v20 offset:40960\n"
    "ds_read_b64_tr_b16 a[2:3], v20 offset:43008\n"
    "s_waitcnt lgkmcnt(12)\n"
    "v_mfma_f32_32x32x16_bf16 v[144:159], a[36:39], v[68:71], v[144:159]\n"
    "ds_read_b64_tr_b16 a[4:5], v21 offset:40960\n"
    "ds_read_b64_tr_b16 a[6:7], v21 offset:43008\n"
    "s_waitcnt lgkmcnt(12)\n"
    "v_mfma_f32_32x32x16_bf16 v[160:175], a[40:43], v[68:71], v[160:175]\n"
    "ds_read_b64_tr_b16 a[8:9], v22 offset:40960\n"
    "ds_read_b64_tr_b16 a[10:11], v22 offset:43008\n"
    "s_waitcnt lgkmcnt(12)\n"
    "v_mfma_f32_32x32x16_bf16 v[176:191], a[44:47], v[68:71], v[176:191]\n"
    "ds_read_b64_tr_b16 a[12:13], v23 offset:40960\n"
    "ds_read_b64_tr_b16 a[14:15], v23 offset:43008\n"
    "s_mov_b32 s77, 0x5000\n"
    "buffer_load_dwordx4 v[64:67], v2, s[40:43], s77 offen offset:0 sc1\n"
    "s_waitcnt lgkmcnt(12)\n"
    "v_mfma_f32_32x32x16_bf16 v[192:207], a[48:51], v[68:71], v[192:207]\n"
    "ds_read_b64_tr_b16 a[16:17], v24 offset:40960\n"
    "ds_read_b64_tr_b16 a[18:19], v24 offset:43008\n"
    "s_waitcnt lgkmcnt(12)\n"
    "v_mfma_f32_32x32x16_bf16 v[208:223], a[52:55], v[68:71], v[208:223]\n"
    "ds_read_b64_tr_b16 a[20:21], v25 offset:40960\n"
    "ds_read_b64_tr_b16 a[22:23], v25 offset:43008\n"
    "s_waitcnt lgkmcnt(12)\n"
    "v_mfma_f32_32x32x16_bf16 v[224:239], a[56:59], v[68:71], v[224:239]\n"
    "ds_read_b64_tr_b16 a[24:25], v26 offset:40960\n"
    "ds_read_b64_tr_b16 a[26:27], v26 offset:43008\n"
    "s_waitcnt lgkmcnt(12)\n"
    "s_waitcnt vmcnt(14)\n"
    "v_mfma_f32_32x32x16_bf16 v[128:143], a[0:3], v[72:75], v[128:143]\n"
    "ds_read_b64_tr_b16 a[32:33], v20 offset:45056\n"
    "ds_read_b64_tr_b16 a[34:35], v20 offset:47104\n"
    "s_waitcnt lgkmcnt(12)\n"
    "v_mfma_f32_32x32x16_bf16 v[144:159], a[4:7], v[72:75], v[144:159]\n"
    "ds_read_b64_tr_b16 a[36:37], v21 offset:45056\n"
    "ds_read_b64_tr_b16 a[38:39], v21 offset:47104\n"
    "s_waitcnt lgkmcnt(12)\n"
    "v_mfma_f32_32x32x16_bf16 v[160:175], a[8:11], v[72:75], v[160:175]\n"
    "ds_read_b64_tr_b16 a[40:41], v22 offset:45056\n"
    "ds_read_b64_tr_b16 a[42:43], v22 offset:47104\n"
    "s_waitcnt lgkmcnt(12)\n"
    "v_mfma_f32_32x32x16_bf16 v[176:191], a[12:15], v[72:75], v[176:191]\n"
    "ds_read_b64_tr_b16 a[44:45], v23 offset:45056\n"
    "ds_read_b64_tr_b16 a[46:47], v23 offset:47104\n"
    "s_mov_b32 s77, 0x5000\n"
    "buffer_load_dwordx4 v[68:71], v2, s[40:43], s77 offen offset:1024 sc1\n"
    "s_waitcnt lgkmcnt(12)\n"
    "v_mfma_f32_32x32x16_bf16 v[192:207], a[16:19], v[72:75], v[192:207]\n"
    "ds_read_b64_tr_b16 a[48:49], v24 offset:45056\n"
    "ds_read_b64_tr_b16 a[50:51], v24 offset:47104\n"
    "s_waitcnt lgkmcnt(12)\n"
    "v_mfma_f32_32x32x16_bf16 v[208:223], a[20:23], v[72:75], v[208:223]\n"
    "ds_read_b64_tr_b16 a[52:53], v25 offset:45056\n"
    "ds_read_b64_tr_b16 a[54:55], v25 offset:47104\n"
    "s_waitcnt lgkmcnt(12)\n"
    "v_mfma_f32_32x32x16_bf16 v[224:239], a[24:27], v[72:75], v[224:239]\n"
    "ds_read_b64_tr_b16 a[56:57], v26 offset:45056\n"
    "ds_read_b64_tr_b16 a[58:59], v26 offset:47104\n"
    "s_waitcnt lgkmcnt(12)\n"
    "s_waitcnt vmcnt(14)\n"
    "v_mfma_f32_32x32x16_bf16 v[128:143], a[32:35], v[76:79], v[128:143]\n"
    "s_barrier\n"
    "v_add_u32 v20, 0x10000, v20\n"
    "v_add_u32 v21, 0x10000, v21\n"
    "v_add_u32 v22, 0x10000, v22\n"
    "v_add_u32 v23, 0x10000, v23\n"
    "v_add_u32 v24, 0x10000, v24\n"
    "v_add_u32 v25, 0x10000, v25\n"
    "v_add_u32 v26, 0x10000, v26\n"
    "ds_read_b64_tr_b16 a[0:1], v20 offset:32768\n"
    "ds_read_b64_tr_b16 a[2:3], v20 offset:34816\n"
    "s_mul_i32 s66, s64, 0xe000\n"
    "s_add_u32 s66, s66, 0x118000\n"
    "s_add_u32 m0, s69, 0x10000\n"
    "s_waitcnt lgkmcnt(12)\n"
    "v_mfma_f32_32x32x16_bf16 v[144:159], a[36:39], v[76:79], v[144:159]\n"
    "ds_read_b64_tr_b16 a[4:5], v21 offset:32768\n"
    "ds_read_b64_tr_b16 a[6:7], v21 offset:34816\n"
    "buffer_load_dwordx4 v4, s[52:55], s66 offen offset:0 lds\n"
    "buffer_load_dwordx4 v5, s[52:55], s66 offen offset:1024 lds\n"
    "s_waitcnt lgkmcnt(12)\n"
    "v_mfma_f32_32x32x16_bf16 v[160:175], a[40:43], v[76:79], v[160:175]\n"
    "ds_read_b64_tr_b16 a[8:9], v22 offset:32768\n"
    "ds_read_b64_tr_b16 a[10:11], v22 offset:34816\n"
    "buffer_load_dwordx4 v6, s[52:55], s66 offen offset:2048 lds\n"
    "s_waitcnt lgkmcnt(12)\n"
    "v_mfma_f32_32x32x16_bf16 v[176:191], a[44:47], v[76:79], v[176:191]\n"
    "ds_read_b64_tr_b16 a[12:13], v23 offset:32768\n"
    "ds_read_b64_tr_b16 a[14:15], v23 offset:34816\n"
    "s_mov_b32 s77, 0x5000\n"
    "buffer_load_dwordx4 v[72:75], v2, s[40:43], s77 offen offset:2048 sc1\n"
    "buffer_load_dwordx4 v7, s[52:55], s66 offen offset:3072 lds\n"
    "s_add_u32 m0, s69, 0x14000\n"
    "s_waitcnt lgkmcnt(12)\n"
    "v_mfma_f32_32x32x16_bf16 v[192:207], a[48:51], v[76:79], v[192:207]\n"
    "ds_read_b64_tr_b16 a[16:17], v24 offset:32768\n"
    "ds_read_b64_tr_b16 a[18:19], v24 offset:34816\n"
    "buffer_load_dwordx4 v16, s[56:59], s66 offen offset:0 lds\n"
    "s_waitcnt lgkmcnt(12)\n"
    "v_mfma_f32_32x32x16_bf16 v[208:223], a[52:55], v[76:79], v[208:223]\n"
    "ds_read_b64_tr_b16 a[20:21], v25 offset:32768\n"
    "ds_read_b64_tr_b16 a[22:23], v25 offset:34816\n"
    "buffer_load_dwordx4 v17, s[56:59], s66 offen offset:1024 lds\n"
    "s_waitcnt lgkmcnt(12)\n"
    "v_mfma_f32_32x32x16_bf16 v[224:239], a[56:59], v[76:79], v[224:239]\n"
    "ds_read_b64_tr_b16 a[24:25], v26 offset:32768\n"
    "ds_read_b64_tr_b16 a[26:27], v26 offset:34816\n"
    "buffer_load_dwordx4 v18, s[56:59], s66 offen offset:2048 lds\n"
    "buffer_load_dwordx4 v19, s[56:59], s66 offen offset:3072 lds\n"
    "s_waitcnt lgkmcnt(12)\n"
    "s_waitcnt vmcnt(22)\n"
    "v_mfma_f32_32x32x16_bf16 v[128:143], a[0:3], v[80:83], v[128:143]\n"
    "ds_read_b64_tr_b16 a[32:33], v20 offset:36864\n"
    "ds_read_b64_tr_b16 a[34:35], v20 offset:38912\n"
    "s_waitcnt lgkmcnt(12)\n"
    "v_mfma_f32_32x32x16_bf16 v[144:159], a[4:7], v[80:83], v[144:159]\n"
    "ds_read_b64_tr_b16 a[36:37], v21 offset:36864\n"
    "ds_read_b64_tr_b16 a[38:39], v21 offset:38912\n"
    "s_waitcnt lgkmcnt(12)\n"
    "v_mfma_f32_32x32x16_bf16 v[160:175], a[8:11], v[80:83], v[160:175]\n"
    "ds_read_b64_tr_b16 a[40:41], v22 offset:36864\n"
    "ds_read_b64_tr_b16 a[42:43], v22 offset:38912\n"
    "s_waitcnt lgkmcnt(12)\n"
    "v_mfma_f32_32x32x16_bf16 v[176:191], a[12:15], v[80:83], v[176:191]\n"
    "ds_read_b64_tr_b16 a[44:45], v23 offset:36864\n"
    "ds_read_b64_tr_b16 a[46:47], v23 offset:38912\n"
    "s_mov_b32 s77, 0x5000\n"
    "buffer_load_dwordx4 v[76:79], v2, s[40:43], s77 offen offset:3072 sc1\n"
    "s_waitcnt lgkmcnt(12)\n"
    "v_mfma_f32_32x32x16_bf16 v[192:207], a[16:19], v[80:83], v[192:207]\n"
    "ds_read_b64_tr_b16 a[48:49], v24 offset:36864\n"
    "ds_read_b64_tr_b16 a[50:51], v24 offset:38912\n"
    "s_waitcnt lgkmcnt(12)\n"
    "v_mfma_f32_32x32x16_bf16 v[208:223], a[20:23], v[80:83], v[208:223]\n"
    "ds_read_b64_tr_b16 a[52:53], v25 offset:36864\n"
    "ds_read_b64_tr_b16 a[54:55], v25 offset:38912\n"
    "s_waitcnt lgkmcnt(12)\n"
    "v_mfma_f32_32x32x16_bf16 v[224:239], a[24:27], v[80:83], v[224:239]\n"
    "ds_read_b64_tr_b16 a[56:57], v26 offset:36864\n"
    "ds_read_b64_tr_b16 a[58:59], v26 offset:38912\n"
    "s_waitcnt lgkmcnt(12)\n"
    "s_waitcnt vmcnt(22)\n"
    "v_mfma_f32_32x32x16_bf16 v[128:143], a[32:35], v[84:87], v[128:143]\n"
    "ds_read_b64_tr_b16 a[0:1], v20 offset:40960\n"
    "ds_read_b64_tr_b16 a[2:3], v20 offset:43008\n"
    "s_waitcnt lgkmcnt(12)\n"
    "v_mfma_f32_32x32x16_bf16 v[144:159], a[36:39], v[84:87], v[144:159]\n"
    "ds_read_b64_tr_b16 a[4:5], v21 offset:40960\n"
    "ds_read_b64_tr_b16 a[6:7], v21 offset:43008\n"
    "s_waitcnt lgkmcnt(12)\n"
    "v_mfma_f32_32x32x16_bf16 v[160:175], a[40:43], v[84:87], v[160:175]\n"
    "ds_read_b64_tr_b16 a[8:9], v22 offset:40960\n"
    "ds_read_b64_tr_b16 a[10:11], v22 offset:43008\n"
    "s_waitcnt lgkmcnt(12)\n"
    "v_mfma_f32_32x32x16_bf16 v[176:191], a[44:47], v[84:87], v[176:191]\n"
    "ds_read_b64_tr_b16 a[12:13], v23 offset:40960\n"
    "ds_read_b64_tr_b16 a[14:15], v23 offset:43008\n"
    "s_mov_b32 s77, 0x6000\n"
    "buffer_load_dwordx4 v[80:83], v2, s[40:43], s77 offen offset:0 sc1\n"
    "s_waitcnt lgkmcnt(12)\n"
    "v_mfma_f32_32x32x16_bf16 v[192:207], a[48:51], v[84:87], v[192:207]\n"
    "ds_read_b64_tr_b16 a[16:17], v24 offset:40960\n"
    "ds_read_b64_tr_b16 a[18:19], v24 offset:43008\n"
    "s_waitcnt lgkmcnt(12)\n"
    "v_mfma_f32_32x32x16_bf16 v[208:223], a[52:55], v[84:87], v[208:223]\n"
    "ds_read_b64_tr_b16 a[20:21], v25 offset:40960\n"
    "ds_read_b64_tr_b16 a[22:23], v25 offset:43008\n"
    "s_waitcnt lgkmcnt(12)\n"
    "v_mfma_f32_32x32x16_bf16 v[224:239], a[56:59], v[84:87], v[224:239]\n"
    "ds_read_b64_tr_b16 a[24:25], v26 offset:40960\n"
    "ds_read_b64_tr_b16 a[26:27], v26 offset:43008\n"
    "s_waitcnt lgkmcnt(12)\n"
    "s_waitcnt vmcnt(22)\n"
    "v_mfma_f32_32x32x16_bf16 v[128:143], a[0:3], v[88:91], v[128:143]\n"
    "ds_read_b64_tr_b16 a[32:33], v20 offset:45056\n"
    "ds_read_b64_tr_b16 a[34:35], v20 offset:47104\n"
    "s_waitcnt lgkmcnt(12)\n"
    "v_mfma_f32_32x32x16_bf16 v[144:159], a[4:7], v[88:91], v[144:159]\n"
    "ds_read_b64_tr_b16 a[36:37], v21 offset:45056\n"
    "ds_read_b64_tr_b16 a[38:39], v21 offset:47104\n"
    "s_waitcnt lgkmcnt(12)\n"
    "v_mfma_f32_32x32x16_bf16 v[160:175], a[8:11], v[88:91], v[160:175]\n"
    "ds_read_b64_tr_b16 a[40:41], v22 offset:45056\n"
    "ds_read_b64_tr_b16 a[42:43], v22 offset:47104\n"
    "s_waitcnt lgkmcnt(12)\n"
    "v_mfma_f32_32x32x16_bf16 v[176:191], a[12:15], v[88:91], v[176:191]\n"
    "ds_read_b64_tr_b16 a[44:45], v23 offset:45056\n"
    "ds_read_b64_tr_b16 a[46:47], v23 offset:47104\n"
    "s_mov_b32 s77, 0x6000\n"
    "buffer_load_dwordx4 v[84:87], v2, s[40:43], s77 offen offset:1024 sc1\n"
    "s_waitcnt lgkmcnt(12)\n"
    "v_mfma_f32_32x32x16_bf16 v[192:207], a[16:19], v[88:91], v[192:207]\n"
    "ds_read_b64_tr_b16 a[48:49], v24 offset:45056\n"
    "ds_read_b64_tr_b16 a[50:51], v24 offset:47104\n"
    "s_waitcnt lgkmcnt(12)\n"
    "v_mfma_f32_32x32x16_bf16 v[208:223], a[20:23], v[88:91], v[208:223]\n"
    "ds_read_b64_tr_b16 a[52:53], v25 offset:45056\n"
    "ds_read_b64_tr_b16 a[54:55], v25 offset:47104\n"
    "s_waitcnt lgkmcnt(12)\n"
    "v_mfma_f32_32x32x16_bf16 v[224:239], a[24:27], v[88:91], v[224:239]\n"
    "ds_read_b64_tr_b16 a[56:57], v26 offset:45056\n"
    "ds_read_b64_tr_b16 a[58:59], v26 offset:47104\n"
    "s_waitcnt lgkmcnt(12)\n"
    "s_waitcnt vmcnt(22)\n"
    "v_mfma_f32_32x32x16_bf16 v[128:143], a[32:35], v[92:95], v[128:143]\n"
    "s_barrier\n"
    "v_add_u32 v20, 0xffff0000, v20\n"
    "v_add_u32 v21, 0xffff0000, v21\n"
    "v_add_u32 v22, 0xffff0000, v22\n"
    "v_add_u32 v23, 0xffff0000, v23\n"
    "v_add_u32 v24, 0xffff0000, v24\n"
    "v_add_u32 v25, 0xffff0000, v25\n"
    "v_add_u32 v26, 0xffff0000, v26\n"
    "ds_read_b64_tr_b16 a[0:1], v20 offset:0\n"
    "ds_read_b64_tr_b16 a[2:3], v20 offset:2048\n"
    "s_mul_i32 s66, s64, 0xe000\n"
    "s_add_u32 s66, s66, 0x150000\n"
    "s_add_u32 m0, s69, 0x8000\n"
    "s_waitcnt lgkmcnt(12)\n"
    "v_mfma_f32_32x32x16_bf16 v[144:159], a[36:39], v[92:95], v[144:159]\n"
    "ds_read_b64_tr_b16 a[4:5], v21 offset:0\n"
    "ds_read_b64_tr_b16 a[6:7], v21 offset:2048\n"
    "buffer_load_dwordx4 v4, s[52:55], s66 offen offset:0 lds\n"
    "buffer_load_dwordx4 v5, s[52:55], s66 offen offset:1024 lds\n"
    "s_waitcnt lgkmcnt(12)\n"
    "v_mfma_f32_32x32x16_bf16 v[160:175], a[40:43], v[92:95], v[160:175]\n"
    "ds_read_b64_tr_b16 a[8:9], v22 offset:0\n"
    "ds_read_b64_tr_b16 a[10:11], v22 offset:2048\n"
    "buffer_load_dwordx4 v6, s[52:55], s66 offen offset:2048 lds\n"
    "s_waitcnt lgkmcnt(12)\n"
    "v_mfma_f32_32x32x16_bf16 v[176:191], a[44:47], v[92:95], v[176:191]\n"
    "ds_read_b64_tr_b16 a[12:13], v23 offset:0\n"
    "ds_read_b64_tr_b16 a[14:15], v23 offset:2048\n"
    "s_mov_b32 s77, 0x6000\n"
    "buffer_load_dwordx4 v[88:91], v2, s[40:43], s77 offen offset:2048 sc1\n"
    "buffer_load_dwordx4 v7, s[52:55], s66 offen offset:3072 lds\n"
    "s_add_u32 m0, s69, 0xc000\n"
    "s_waitcnt lgkmcnt(12)\n"
    "v_mfma_f32_32x32x16_bf16 v[192:207], a[48:51], v[92:95], v[192:207]\n"
    "ds_read_b64_tr_b16 a[16:17], v24 offset:0\n"
    "ds_read_b64_tr_b16 a[18:19], v24 offset:2048\n"
    "buffer_load_dwordx4 v16, s[56:59], s66 offen offset:0 lds\n"
    "s_waitcnt lgkmcnt(12)\n"
    "v_mfma_f32_32x32x16_bf16 v[208:223], a[52:55], v[92:95], v[208:223]\n"
    "ds_read_b64_tr_b16 a[20:21], v25 offset:0\n"
    "ds_read_b64_tr_b16 a[22:23], v25 offset:2048\n"
    "buffer_load_dwordx4 v17, s[56:59], s66 offen offset:1024 lds\n"
    "s_waitcnt lgkmcnt(12)\n"
    "v_mfma_f32_32x32x16_bf16 v[224:239], a[56:59], v[92:95], v[224:239]\n"
    "ds_read_b64_tr_b16 a[24:25], v26 offset:0\n"
    "ds_read_b64_tr_b16 a[26:27], v26 offset:2048\n"
    "buffer_load_dwordx4 v18, s[56:59], s66 offen offset:2048 lds\n"
    "buffer_load_dwordx4 v19, s[56:59], s66 offen offset:3072 lds\n"
    "s_waitcnt lgkmcnt(12)\n"
    "s_waitcnt vmcnt(30)\n"
    "v_mfma_f32_32x32x16_bf16 v[128:143], a[0:3], v[96:99], v[128:143]\n"
    "ds_read_b64_tr_b16 a[32:33], v20 offset:4096\n"
    "ds_read_b64_tr_b16 a[34:35], v20 offset:6144\n"
    "s_waitcnt lgkmcnt(12)\n"
    "v_mfma_f32_32x32x16_bf16 v[144:159], a[4:7], v[96:99], v[144:159]\n"
    "ds_read_b64_tr_b16 a[36:37], v21 offset:4096\n"
    "ds_read_b64_tr_b16 a[38:39], v21 offset:6144\n"
    "s_waitcnt lgkmcnt(12)\n"
    "v_mfma_f32_32x32x16_bf16 v[160:175], a[8:11], v[96:99], v[160:175]\n"
    "ds_read_b64_tr_b16 a[40:41], v22 offset:4096\n"
    "ds_read_b64_tr_b16 a[42:43], v22 offset:6144\n"
    "s_waitcnt lgkmcnt(12)\n"
    "v_mfma_f32_32x32x16_bf16 v[176:191], a[12:15], v[96:99], v[176:191]\n"
    "ds_read_b64_tr_b16 a[44:45], v23 offset:4096\n"
    "ds_read_b64_tr_b16 a[46:47], v23 offset:6144\n"
    "s_mov_b32 s77, 0x6000\n"
    "buffer_load_dwordx4 v[92:95], v2, s[40:43], s77 offen offset:3072 sc1\n"
    "s_waitcnt lgkmcnt(12)\n"
    "v_mfma_f32_32x32x16_bf16 v[192:207], a[16:19], v[96:99], v[192:207]\n"
    "ds_read_b64_tr_b16 a[48:49], v24 offset:4096\n"
    "ds_read_b64_tr_b16 a[50:51], v24 offset:6144\n"
    "s_waitcnt lgkmcnt(12)\n"
    "v_mfma_f32_32x32x16_bf16 v[208:223], a[20:23], v[96:99], v[208:223]\n"
    "ds_read_b64_tr_b16 a[52:53], v25 offset:4096\n"
    "ds_read_b64_tr_b16 a[54:55], v25 offset:6144\n"
    "s_waitcnt lgkmcnt(12)\n"
    "v_mfma_f32_32x32x16_bf16 v[224:239], a[24:27], v[96:99], v[224:239]\n"
    "ds_read_b64_tr_b16 a[56:57], v26 offset:4096\n"
    "ds_read_b64_tr_b16 a[58:59], v26 offset:6144\n"
    "s_waitcnt lgkmcnt(12)\n"
    "s_waitcnt vmcnt(30)\n"
    "v_mfma_f32_32x32x16_bf16 v[128:143], a[32:35], v[100:103], v[128:143]\n"
    "ds_read_b64_tr_b16 a[0:1], v20 offset:8192\n"
    "ds_read_b64_tr_b16 a[2:3], v20 offset:10240\n"
    "s_waitcnt lgkmcnt(12)\n"
    "v_mfma_f32_32x32x16_bf16 v[144:159], a[36:39], v[100:103], v[144:159]\n"
    "ds_read_b64_tr_b16 a[4:5], v21 offset:8192\n"
    "ds_read_b64_tr_b16 a[6:7], v21 offset:10240\n"
    "s_waitcnt lgkmcnt(12)\n"
    "v_mfma_f32_32x32x16_bf16 v[160:175], a[40:43], v[100:103], v[160:175]\n"
    "ds_read_b64_tr_b16 a[8:9], v22 offset:8192\n"
    "ds_read_b64_tr_b16 a[10:11], v22 offset:10240\n"
    "s_waitcnt lgkmcnt(12)\n"
    "v_mfma_f32_32x32x16_bf16 v[176:191], a[44:47], v[100:103], v[176:191]\n"
    "ds_read_b64_tr_b16 a[12:13], v23 offset:8192\n"
    "ds_read_b64_tr_b16 a[14:15], v23 offset:10240\n"
    "s_mov_b32 s77, 0x7000\n"
    "buffer_load_dwordx4 v[96:99], v2, s[40:43], s77 offen offset:0 sc1\n"
    "s_waitcnt lgkmcnt(12)\n"
    "v_mfma_f32_32x32x16_bf16 v[192:207], a[48:51], v[100:103], v[192:207]\n"
    "ds_read_b64_tr_b16 a[16:17], v24 offset:8192\n"
    "ds_read_b64_tr_b16 a[18:19], v24 offset:10240\n"
    "s_waitcnt lgkmcnt(12)\n"
    "v_mfma_f32_32x32x16_bf16 v[208:223], a[52:55], v[100:103], v[208:223]\n"
    "ds_read_b64_tr_b16 a[20:21], v25 offset:8192\n"
    "ds_read_b64_tr_b16 a[22:23], v25 offset:10240\n"
    "s_waitcnt lgkmcnt(12)\n"
    "v_mfma_f32_32x32x16_bf16 v[224:239], a[56:59], v[100:103], v[224:239]\n"
    "ds_read_b64_tr_b16 a[24:25], v26 offset:8192\n"
    "ds_read_b64_tr_b16 a[26:27], v26 offset:10240\n"
    "s_waitcnt lgkmcnt(12)\n"
    "s_waitcnt vmcnt(30)\n"
    "v_mfma_f32_32x32x16_bf16 v[128:143], a[0:3], v[104:107], v[128:143]\n"
    "ds_read_b64_tr_b16 a[32:33], v20 offset:12288\n"
    "ds_read_b64_tr_b16 a[34:35], v20 offset:14336\n"
    "s_waitcnt lgkmcnt(12)\n"
    "v_mfma_f32_32x32x16_bf16 v[144:159], a[4:7], v[104:107], v[144:159]\n"
    "ds_read_b64_tr_b16 a[36:37], v21 offset:12288\n"
    "ds_read_b64_tr_b16 a[38:39], v21 offset:14336\n"
    "s_waitcnt lgkmcnt(12)\n"
    "v_mfma_f32_32x32x16_bf16 v[160:175], a[8:11], v[104:107], v[160:175]\n"
    "ds_read_b64_tr_b16 a[40:41], v22 offset:12288\n"
    "ds_read_b64_tr_b16 a[42:43], v22 offset:14336\n"
    "s_waitcnt lgkmcnt(12)\n"
    "v_mfma_f32_32x32x16_bf16 v[176:191], a[12:15], v[104:107], v[176:191]\n"
    "ds_read_b64_tr_b16 a[44:45], v23 offset:12288\n"
    "ds_read_b64_tr_b16 a[46:47], v23 offset:14336\n"
    "s_mov_b32 s77, 0x7000\n"
    "buffer_load_dwordx4 v[100:103], v2, s[40:43], s77 offen offset:1024 sc1\n"
    "s_waitcnt lgkmcnt(12)\n"
    "v_mfma_f32_32x32x16_bf16 v[192:207], a[16:19], v[104:107], v[192:207]\n"
    "ds_read_b64_tr_b16 a[48:49], v24 offset:12288\n"
    "ds_read_b64_tr_b16 a[50:51], v24 offset:14336\n"
    "s_waitcnt lgkmcnt(12)\n"
    "v_mfma_f32_32x32x16_bf16 v[208:223], a[20:23], v[104:107], v[208:223]\n"
    "ds_read_b64_tr_b16 a[52:53], v25 offset:12288\n"
    "ds_read_b64_tr_b16 a[54:55], v25 offset:14336\n"
    "s_waitcnt lgkmcnt(12)\n"
    "v_mfma_f32_32x32x16_bf16 v[224:239], a[24:27], v[104:107], v[224:239]\n"
    "ds_read_b64_tr_b16 a[56:57], v26 offset:12288\n"
    "ds_read_b64_tr_b16 a[58:59], v26 offset:14336\n"
    "s_waitcnt lgkmcnt(12)\n"
    "s_waitcnt vmcnt(30)\n"
    "v_mfma_f32_32x32x16_bf16 v[128:143], a[32:35], v[108:111], v[128:143]\n"
    "s_barrier\n"
    "v_add_u32 v20, 0x20000, v20\n"
    "v_add_u32 v21, 0x20000, v21\n"
    "v_add_u32 v22, 0x20000, v22\n"
    "v_add_u32 v23, 0x20000, v23\n"
    "v_add_u32 v24, 0x20000, v24\n"
    "v_add_u32 v25, 0x20000, v25\n"
    "v_add_u32 v26, 0x20000, v26\n"
    "ds_read_b64_tr_b16 a[0:1], v20 offset:0\n"
    "ds_read_b64_tr_b16 a[2:3], v20 offset:2048\n"
    "s_mul_i32 s66, s64, 0xe000\n"
    "s_add_u32 s66, s66, 0x188000\n"
    "s_add_u32 m0, s69, 0x18000\n"
    "s_waitcnt lgkmcnt(12)\n"
    "v_mfma_f32_32x32x16_bf16 v[144:159], a[36:39], v[108:111], v[144:159]\n"
    "ds_read_b64_tr_b16 a[4:5], v21 offset:0\n"
    "ds_read_b64_tr_b16 a[6:7], v21 offset:2048\n"
    "buffer_load_dwordx4 v4, s[52:55], s66 offen offset:0 lds\n"
    "buffer_load_dwordx4 v5, s[52:55], s66 offen offset:1024 lds\n"
    "s_waitcnt lgkmcnt(12)\n"
    "v_mfma_f32_32x32x16_bf16 v[160:175], a[40:43], v[108:111], v[160:175]\n"
    "ds_read_b64_tr_b16 a[8:9], v22 offset:0\n"
    "ds_read_b64_tr_b16 a[10:11], v22 offset:2048\n"
    "buffer_load_dwordx4 v6, s[52:55], s66 offen offset:2048 lds\n"
    "s_waitcnt lgkmcnt(12)\n"
    "v_mfma_f32_32x32x16_bf16 v[176:191], a[44:47], v[108:111], v[176:191]\n"
    "ds_read_b64_tr_b16 a[12:13], v23 offset:0\n"
    "ds_read_b64_tr_b16 a[14:15], v23 offset:2048\n"
    "s_mov_b32 s77, 0x7000\n"
    "buffer_load_dwordx4 v[104:107], v2, s[40:43], s77 offen offset:2048 sc1\n"
    "buffer_load_dwordx4 v7, s[52:55], s66 offen offset:3072 lds\n"
    "s_add_u32 m0, s69, 0x1c000\n"
    "s_waitcnt lgkmcnt(12)\n"
    "v_mfma_f32_32x32x16_bf16 v[192:207], a[48:51], v[108:111], v[192:207]\n"
    "ds_read_b64_tr_b16 a[16:17], v24 offset:0\n"
    "ds_read_b64_tr_b16 a[18:19], v24 offset:2048\n"
    "buffer_load_dwordx4 v16, s[56:59], s66 offen offset:0 lds\n"
    "s_waitcnt lgkmcnt(12)\n"
    "v_mfma_f32_32x32x16_bf16 v[208:223], a[52:55], v[108:111], v[208:223]\n"
    "ds_read_b64_tr_b16 a[20:21], v25 offset:0\n"
    "ds_read_b64_tr_b16 a[22:23], v25 offset:2048\n"
    "buffer_load_dwordx4 v17, s[56:59], s66 offen offset:1024 lds\n"
    "s_waitcnt lgkmcnt(12)\n"
    "v_mfma_f32_32x32x16_bf16 v[224:239], a[56:59], v[108:111], v[224:239]\n"
    "ds_read_b64_tr_b16 a[24:25], v26 offset:0\n"
    "ds_read_b64_tr_b16 a[26:27], v26 offset:2048\n"
    "buffer_load_dwordx4 v18, s[56:59], s66 offen offset:2048 lds\n"
    "buffer_load_dwordx4 v19, s[56:59], s66 offen offset:3072 lds\n"
    "s_waitcnt lgkmcnt(12)\n"
    "s_waitcnt vmcnt(38)\n"
    "v_mfma_f32_32x32x16_bf16 v[128:143], a[0:3], v[48:51], v[128:143]\n"
    "ds_read_b64_tr_b16 a[32:33], v20 offset:4096\n"
    "ds_read_b64_tr_b16 a[34:35], v20 offset:6144\n"
    "s_waitcnt lgkmcnt(12)\n"
    "v_mfma_f32_32x32x16_bf16 v[144:159], a[4:7], v[48:51], v[144:159]\n"
    "ds_read_b64_tr_b16 a[36:37], v21 offset:4096\n"
    "ds_read_b64_tr_b16 a[38:39], v21 offset:6144\n"
    "s_waitcnt lgkmcnt(12)\n"
    "v_mfma_f32_32x32x16_bf16 v[160:175], a[8:11], v[48:51], v[160:175]\n"
    "ds_read_b64_tr_b16 a[40:41], v22 offset:4096\n"
    "ds_read_b64_tr_b16 a[42:43], v22 offset:6144\n"
    "s_waitcnt lgkmcnt(12)\n"
    "v_mfma_f32_32x32x16_bf16 v[176:191], a[12:15], v[48:51], v[176:191]\n"
    "ds_read_b64_tr_b16 a[44:45], v23 offset:4096\n"
    "ds_read_b64_tr_b16 a[46:47], v23 offset:6144\n"
    "s_mov_b32 s77, 0x7000\n"
    "buffer_load_dwordx4 v[108:111], v2, s[40:43], s77 offen offset:3072 sc1\n"
    "s_waitcnt lgkmcnt(12)\n"
    "v_mfma_f32_32x32x16_bf16 v[192:207], a[16:19], v[48:51], v[192:207]\n"
    "ds_read_b64_tr_b16 a[48:49], v24 offset:4096\n"
    "ds_read_b64_tr_b16 a[50:51], v24 offset:6144\n"
    "s_waitcnt lgkmcnt(12)\n"
    "v_mfma_f32_32x32x16_bf16 v[208:223], a[20:23], v[48:51], v[208:223]\n"
    "ds_read_b64_tr_b16 a[52:53], v25 offset:4096\n"
    "ds_read_b64_tr_b16 a[54:55], v25 offset:6144\n"
    "s_waitcnt lgkmcnt(12)\n"
    "v_mfma_f32_32x32x16_bf16 v[224:239], a[24:27], v[48:51], v[224:239]\n"
    "ds_read_b64_tr_b16 a[56:57], v26 offset:4096\n"
    "ds_read_b64_tr_b16 a[58:59], v26 offset:6144\n"
    "s_waitcnt lgkmcnt(12)\n"
    "s_waitcnt vmcnt(38)\n"
    "v_mfma_f32_32x32x16_bf16 v[128:143], a[32:35], v[52:55], v[128:143]\n"
    "ds_read_b64_tr_b16 a[0:1], v20 offset:8192\n"
    "ds_read_b64_tr_b16 a[2:3], v20 offset:10240\n"
    "s_lshl_b32 s71, s70, 1\n"
    "s_lshr_b32 s73, s79, 3\n"
    "s_add_u32 s71, s71, s73\n"
    "s_and_b32 s72, s79, 7\n"
    "s_mov_b64 s[48:49], %7\n"
    "s_lshl_b32 s73, s72, 7\n"
    "s_add_u32 s48, s48, s73\n"
    "s_addc_u32 s49, s49, 0\n"
    "s_and_b32 s49, s49, 0xffff\n"
    "s_sub_u32 s50, 0x1c0000, s73\n"
    "s_mov_b32 s51, 0x20000\n"
    "v_lshrrev_b32 v11, 5, v1\n"
    "v_bfe_u32 v12, v1, 2, 2\n"
    "v_lshl_add_u32 v11, v11, 2, v12\n"
    "s_waitcnt lgkmcnt(12)\n"
    "v_mfma_f32_32x32x16_bf16 v[144:159], a[36:39], v[52:55], v[144:159]\n"
    "ds_read_b64_tr_b16 a[4:5], v21 offset:8192\n"
    "ds_read_b64_tr_b16 a[6:7], v21 offset:10240\n"
    "v_lshlrev_b32 v11, 10, v11\n"
    "v_bfe_u32 v12, v1, 4, 1\n"
    "v_lshl_add_u32 v11, v12, 6, v11\n"
    "v_and_b32 v12, 3, v1\n"
    "v_lshl_add_u32 v4, v12, 4, v11\n"
    "v_add_u32 v5, 0x1c00, v4\n"
    "s_lshl_b32 s67, s64, 13\n"
    "s_mul_i32 s68, s64, 0x70000\n"
    "s_waitcnt lgkmcnt(12)\n"
    "v_mfma_f32_32x32x16_bf16 v[160:175], a[40:43], v[52:55], v[160:175]\n"
    "ds_read_b64_tr_b16 a[8:9], v22 offset:8192\n"
    "ds_read_b64_tr_b16 a[10:11], v22 offset:10240\n"
    "s_waitcnt lgkmcnt(12)\n"
    "v_mfma_f32_32x32x16_bf16 v[176:191], a[44:47], v[52:55], v[176:191]\n"
    "ds_read_b64_tr_b16 a[12:13], v23 offset:8192\n"
    "ds_read_b64_tr_b16 a[14:15], v23 offset:10240\n"
    "s_waitcnt lgkmcnt(12)\n"
    "v_mfma_f32_32x32x16_bf16 v[192:207], a[48:51], v[52:55], v[192:207]\n"
    "ds_read_b64_tr_b16 a[16:17], v24 offset:8192\n"
    "ds_read_b64_tr_b16 a[18:19], v24 offset:10240\n"
    "s_waitcnt lgkmcnt(12)\n"
    "v_mfma_f32_32x32x16_bf16 v[208:223], a[52:55], v[52:55], v[208:223]\n"
    "ds_read_b64_tr_b16 a[20:21], v25 offset:8192\n"
    "ds_read_b64_tr_b16 a[22:23], v25 offset:10240\n"
    "s_waitcnt lgkmcnt(12)\n"
    "v_mfma_f32_32x32x16_bf16 v[224:239], a[56:59], v[52:55], v[224:239]\n"
    "ds_read_b64_tr_b16 a[24:25], v26 offset:8192\n"
    "ds_read_b64_tr_b16 a[26:27], v26 offset:10240\n"
    "s_waitcnt lgkmcnt(12)\n"
    "s_waitcnt vmcnt(37)\n"
    "v_mfma_f32_32x32x16_bf16 v[128:143], a[0:3], v[56:59], v[128:143]\n"
    "ds_read_b64_tr_b16 a[32:33], v20 offset:12288\n"
    "ds_read_b64_tr_b16 a[34:35], v20 offset:14336\n"
    "s_waitcnt lgkmcnt(12)\n"
    "v_mfma_f32_32x32x16_bf16 v[144:159], a[4:7], v[56:59], v[144:159]\n"
    "ds_read_b64_tr_b16 a[36:37], v21 offset:12288\n"
    "ds_read_b64_tr_b16 a[38:39], v21 offset:14336\n"
    "s_waitcnt lgkmcnt(12)\n"
    "v_mfma_f32_32x32x16_bf16 v[160:175], a[8:11], v[56:59], v[160:175]\n"
    "ds_read_b64_tr_b16 a[40:41], v22 offset:12288\n"
    "ds_read_b64_tr_b16 a[42:43], v22 offset:14336\n"
    "s_waitcnt lgkmcnt(12)\n"
    "v_mfma_f32_32x32x16_bf16 v[176:191], a[12:15], v[56:59], v[176:191]\n"
    "ds_read_b64_tr_b16 a[44:45], v23 offset:12288\n"
    "ds_read_b64_tr_b16 a[46:47], v23 offset:14336\n"
    "s_waitcnt lgkmcnt(12)\n"
    "v_mfma_f32_32x32x16_bf16 v[192:207], a[16:19], v[56:59], v[192:207]\n"
    "ds_read_b64_tr_b16 a[48:49], v24 offset:12288\n"
    "ds_read_b64_tr_b16 a[50:51], v24 offset:14336\n"
    "s_waitcnt lgkmcnt(12)\n"
    "v_mfma_f32_32x32x16_bf16 v[208:223], a[20:23], v[56:59], v[208:223]\n"
    "ds_read_b64_tr_b16 a[52:53], v25 offset:12288\n"
    "ds_read_b64_tr_b16 a[54:55], v25 offset:14336\n"
    "s_waitcnt lgkmcnt(12)\n"
    "v_mfma_f32_32x32x16_bf16 v[224:239], a[24:27], v[56:59], v[224:239]\n"
    "ds_read_b64_tr_b16 a[56:57], v26 offset:12288\n"
    "ds_read_b64_tr_b16 a[58:59], v26 offset:14336\n"
    "s_waitcnt lgkmcnt(12)\n"
    "s_waitcnt vmcnt(36)\n"
    "v_mfma_f32_32x32x16_bf16 v[128:143], a[32:35], v[60:63], v[128:143]\n"
    "s_waitcnt vmcnt(25)\n"
    "s_barrier\n"
    "v_add_u32 v20, 0xffff0000, v20\n"
    "v_add_u32 v21, 0xffff0000, v21\n"
    "v_add_u32 v22, 0xffff0000, v22\n"
    "v_add_u32 v23, 0xffff0000, v23\n"
    "v_add_u32 v24, 0xffff0000, v24\n"
    "v_add_u32 v25, 0xffff0000, v25\n"
    "v_add_u32 v26, 0xffff0000, v26\n"
    "ds_read_b64_tr_b16 a[0:1], v20 offset:0\n"
    "ds_read_b64_tr_b16 a[2:3], v20 offset:2048\n"
    "s_waitcnt lgkmcnt(12)\n"
    "v_mfma_f32_32x32x16_bf16 v[144:159], a[36:39], v[60:63], v[144:159]\n"
    "ds_read_b64_tr_b16 a[4:5], v21 offset:0\n"
    "ds_read_b64_tr_b16 a[6:7], v21 offset:2048\n"
    "s_add_u32 m0, s67, 0\n"
    "s_waitcnt lgkmcnt(12)\n"
    "v_mfma_f32_32x32x16_bf16 v[160:175], a[40:43], v[60:63], v[160:175]\n"
    "buffer_load_dwordx4 v4, s[48:51], s68 offen offset:0 lds\n"
    "buffer_load_dwordx4 v5, s[48:51], s68 offen offset:1024 lds\n"
    "ds_read_b64_tr_b16 a[8:9], v22 offset:0\n"
    "ds_read_b64_tr_b16 a[10:11], v22 offset:2048\n"
    "s_waitcnt lgkmcnt(12)\n"
    "v_mfma_f32_32x32x16_bf16 v[176:191], a[44:47], v[60:63], v[176:191]\n"
    "ds_read_b64_tr_b16 a[12:13], v23 offset:0\n"
    "ds_read_b64_tr_b16 a[14:15], v23 offset:2048\n"
    "s_waitcnt lgkmcnt(12)\n"
    "v_mfma_f32_32x32x16_bf16 v[192:207], a[48:51], v[60:63], v[192:207]\n"
    "ds_read_b64_tr_b16 a[16:17], v24 offset:0\n"
    "ds_read_b64_tr_b16 a[18:19], v24 offset:2048\n"
    "s_add_u32 s68, s68, 0x4000\n"
    "s_waitcnt lgkmcnt(12)\n"
    "v_mfma_f32_32x32x16_bf16 v[208:223], a[52:55], v[60:63], v[208:223]\n"
    "ds_read_b64_tr_b16 a[20:21], v25 offset:0\n"
    "ds_read_b64_tr_b16 a[22:23], v25 offset:2048\n"
    "s_waitcnt lgkmcnt(12)\n"
    "v_mfma_f32_32x32x16_bf16 v[224:239], a[56:59], v[60:63], v[224:239]\n"
    "ds_read_b64_tr_b16 a[24:25], v26 offset:0\n"
    "ds_read_b64_tr_b16 a[26:27], v26 offset:2048\n"
    "s_waitcnt lgkmcnt(12)\n"
    "v_mfma_f32_32x32x16_bf16 v[128:143], a[0:3], v[64:67], v[128:143]\n"
    "ds_read_b64_tr_b16 a[32:33], v20 offset:4096\n"
    "ds_read_b64_tr_b16 a[34:35], v20 offset:6144\n"
    "s_waitcnt lgkmcnt(12)\n"
    "v_mfma_f32_32x32x16_bf16 v[144:159], a[4:7], v[64:67], v[144:159]\n"
    "ds_read_b64_tr_b16 a[36:37], v21 offset:4096\n"
    "ds_read_b64_tr_b16 a[38:39], v21 offset:6144\n"
    "s_add_u32 m0, s67, 0x800\n"
    "s_waitcnt lgkmcnt(12)\n"
    "v_mfma_f32_32x32x16_bf16 v[160:175], a[8:11], v[64:67], v[160:175]\n"
    "buffer_load_dwordx4 v4, s[48:51], s68 offen offset:0 lds\n"
    "buffer_load_dwordx4 v5, s[48:51], s68 offen offset:1024 lds\n"
    "ds_read_b64_tr_b16 a[40:41], v22 offset:4096\n"
    "ds_read_b64_tr_b16 a[42:43], v22 offset:6144\n"
    "s_waitcnt lgkmcnt(12)\n"
    "v_mfma_f32_32x32x16_bf16 v[176:191], a[12:15], v[64:67], v[176:191]\n"
    "ds_read_b64_tr_b16 a[44:45], v23 offset:4096\n"
    "ds_read_b64_tr_b16 a[46:47], v23 offset:6144\n"
    "s_waitcnt lgkmcnt(12)\n"
    "v_mfma_f32_32x32x16_bf16 v[192:207], a[16:19], v[64:67], v[192:207]\n"
    "ds_read_b64_tr_b16 a[48:49], v24 offset:4096\n"
    "ds_read_b64_tr_b16 a[50:51], v24 offset:6144\n"
    "s_add_u32 s68, s68, 0x4000\n"
    "s_waitcnt lgkmcnt(12)\n"
    "v_mfma_f32_32x32x16_bf16 v[208:223], a[20:23], v[64:67], v[208:223]\n"
    "ds_read_b64_tr_b16 a[52:53], v25 offset:4096\n"
    "ds_read_b64_tr_b16 a[54:55], v25 offset:6144\n"
    "s_waitcnt lgkmcnt(12)\n"
    "v_mfma_f32_32x32x16_bf16 v[224:239], a[24:27], v[64:67], v[224:239]\n"
    "ds_read_b64_tr_b16 a[56:57], v26 offset:4096\n"
    "ds_read_b64_tr_b16 a[58:59], v26 offset:6144\n"
    "s_waitcnt lgkmcnt(12)\n"
    "v_mfma_f32_32x32x16_bf16 v[128:143], a[32:35], v[68:71], v[128:143]\n"
    "ds_read_b64_tr_b16 a[0:1], v20 offset:8192\n"
    "ds_read_b64_tr_b16 a[2:3], v20 offset:10240\n"
    "s_waitcnt lgkmcnt(12)\n"
    "v_mfma_f32_32x32x16_bf16 v[144:159], a[36:39], v[68:71], v[144:159]\n"
    "ds_read_b64_tr_b16 a[4:5], v21 offset:8192\n"
    "ds_read_b64_tr_b16 a[6:7], v21 offset:10240\n"
    "s_add_u32 m0, s67, 0x1000\n"
    "s_waitcnt lgkmcnt(12)\n"
    "v_mfma_f32_32x32x16_bf16 v[160:175], a[40:43], v[68:71], v[160:175]\n"
    "buffer_load_dwordx4 v4, s[48:51], s68 offen offset:0 lds\n"
    "buffer_load_dwordx4 v5, s[48:51], s68 offen offset:1024 lds\n"
    "ds_read_b64_tr_b16 a[8:9], v22 offset:8192\n"
    "ds_read_b64_tr_b16 a[10:11], v22 offset:10240\n"
    "s_waitcnt lgkmcnt(12)\n"
    "v_mfma_f32_32x32x16_bf16 v[176:191], a[44:47], v[68:71], v[176:191]\n"
    "ds_read_b64_tr_b16 a[12:13], v23 offset:8192\n"
    "ds_read_b64_tr_b16 a[14:15], v23 offset:10240\n"
    "s_waitcnt lgkmcnt(12)\n"
    "v_mfma_f32_32x32x16_bf16 v[192:207], a[48:51], v[68:71], v[192:207]\n"
    "ds_read_b64_tr_b16 a[16:17], v24 offset:8192\n"
    "ds_read_b64_tr_b16 a[18:19], v24 offset:10240\n"
    "s_add_u32 s68, s68, 0x4000\n"
    "s_waitcnt lgkmcnt(12)\n"
    "v_mfma_f32_32x32x16_bf16 v[208:223], a[52:55], v[68:71], v[208:223]\n"
    "ds_read_b64_tr_b16 a[20:21], v25 offset:8192\n"
    "ds_read_b64_tr_b16 a[22:23], v25 offset:10240\n"
    "s_waitcnt lgkmcnt(12)\n"
    "v_mfma_f32_32x32x16_bf16 v[224:239], a[56:59], v[68:71], v[224:239]\n"
    "ds_read_b64_tr_b16 a[24:25], v26 offset:8192\n"
    "ds_read_b64_tr_b16 a[26:27], v26 offset:10240\n"
    "s_waitcnt lgkmcnt(12)\n"
    "v_mfma_f32_32x32x16_bf16 v[128:143], a[0:3], v[72:75], v[128:143]\n"
    "ds_read_b64_tr_b16 a[32:33], v20 offset:12288\n"
    "ds_read_b64_tr_b16 a[34:35], v20 offset:14336\n"
    "s_waitcnt lgkmcnt(12)\n"
    "v_mfma_f32_32x32x16_bf16 v[144:159], a[4:7], v[72:75], v[144:159]\n"
    "ds_read_b64_tr_b16 a[36:37], v21 offset:12288\n"
    "ds_read_b64_tr_b16 a[38:39], v21 offset:14336\n"
    "s_add_u32 m0, s67, 0x1800\n"
    "s_waitcnt lgkmcnt(12)\n"
    "v_mfma_f32_32x32x16_bf16 v[160:175], a[8:11], v[72:75], v[160:175]\n"
    "buffer_load_dwordx4 v4, s[48:51], s68 offen offset:0 lds\n"
    "buffer_load_dwordx4 v5, s[48:51], s68 offen offset:1024 lds\n"
    "ds_read_b64_tr_b16 a[40:41], v22 offset:12288\n"
    "ds_read_b64_tr_b16 a[42:43], v22 offset:14336\n"
    "s_waitcnt lgkmcnt(12)\n"
    "v_mfma_f32_32x32x16_bf16 v[176:191], a[12:15], v[72:75], v[176:191]\n"
    "ds_read_b64_tr_b16 a[44:45], v23 offset:12288\n"
    "ds_read_b64_tr_b16 a[46:47], v23 offset:14336\n"
    "s_waitcnt lgkmcnt(12)\n"
    "v_mfma_f32_32x32x16_bf16 v[192:207], a[16:19], v[72:75], v[192:207]\n"
    "ds_read_b64_tr_b16 a[48:49], v24 offset:12288\n"
    "ds_read_b64_tr_b16 a[50:51], v24 offset:14336\n"
    "s_add_u32 s68, s68, 0x4000\n"
    "s_waitcnt lgkmcnt(12)\n"
    "v_mfma_f32_32x32x16_bf16 v[208:223], a[20:23], v[72:75], v[208:223]\n"
    "ds_read_b64_tr_b16 a[52:53], v25 offset:12288\n"
    "ds_read_b64_tr_b16 a[54:55], v25 offset:14336\n"
    "s_waitcnt lgkmcnt(12)\n"
    "v_mfma_f32_32x32x16_bf16 v[224:239], a[24:27], v[72:75], v[224:239]\n"
    "ds_read_b64_tr_b16 a[56:57], v26 offset:12288\n"
    "ds_read_b64_tr_b16 a[58:59], v26 offset:14336\n"
    "s_waitcnt lgkmcnt(12)\n"
    "s_waitcnt vmcnt(32)\n"
    "v_mfma_f32_32x32x16_bf16 v[128:143], a[32:35], v[76:79], v[128:143]\n"
    "s_waitcnt vmcnt(21)\n"
    "s_waitcnt vmcnt(9)\n"
    "s_barrier\n"
    "v_add_u32 v20, 0xffff0000, v20\n"
    "v_add_u32 v21, 0xffff0000, v21\n"
    "v_add_u32 v22, 0xffff0000, v22\n"
    "v_add_u32 v23, 0xffff0000, v23\n"
    "v_add_u32 v24, 0xffff0000, v24\n"
    "v_add_u32 v25, 0xffff0000, v25\n"
    "v_add_u32 v26, 0xffff0000, v26\n"
    "v_add_u32 v112, 0x10000, v20\n"
    "v_add_u32 v113, 0x10000, v21\n"
    "v_add_u32 v114, 0x10000, v22\n"
    "v_add_u32 v115, 0x10000, v23\n"
    "v_add_u32 v116, 0x10000, v24\n"
    "v_add_u32 v117, 0x10000, v25\n"
    "v_add_u32 v118, 0x10000, v26\n"
    "ds_read_b64_tr_b16 a[0:1], v20 offset:32768\n"
    "ds_read_b64_tr_b16 a[2:3], v20 offset:34816\n"
    "s_waitcnt lgkmcnt(12)\n"
    "v_mfma_f32_32x32x16_bf16 v[144:159], a[36:39], v[76:79], v[144:159]\n"
    "ds_read_b64_tr_b16 a[4:5], v21 offset:32768\n"
    "ds_read_b64_tr_b16 a[6:7], v21 offset:34816\n"
    "s_add_u32 m0, s67, 0x20000\n"
    "s_waitcnt lgkmcnt(12)\n"
    "v_mfma_f32_32x32x16_bf16 v[160:175], a[40:43], v[76:79], v[160:175]\n"
    "buffer_load_dwordx4 v4, s[48:51], s68 offen offset:0 lds\n"
    "buffer_load_dwordx4 v5, s[48:51], s68 offen offset:1024 lds\n"
    "ds_read_b64_tr_b16 a[8:9], v22 offset:32768\n"
    "ds_read_b64_tr_b16 a[10:11], v22 offset:34816\n"
    "s_waitcnt lgkmcnt(12)\n"
    "v_mfma_f32_32x32x16_bf16 v[176:191], a[44:47], v[76:79], v[176:191]\n"
    "ds_read_b64_tr_b16 a[12:13], v23 offset:32768\n"
    "ds_read_b64_tr_b16 a[14:15], v23 offset:34816\n"
    "s_waitcnt lgkmcnt(12)\n"
    "v_mfma_f32_32x32x16_bf16 v[192:207], a[48:51], v[76:79], v[192:207]\n"
    "ds_read_b64_tr_b16 a[16:17], v24 offset:32768\n"
    "ds_read_b64_tr_b16 a[18:19], v24 offset:34816\n"
    "s_add_u32 s68, s68, 0x4000\n"
    "s_waitcnt lgkmcnt(12)\n"
    "v_mfma_f32_32x32x16_bf16 v[208:223], a[52:55], v[76:79], v[208:223]\n"
    "ds_read_b64_tr_b16 a[20:21], v25 offset:32768\n"
    "ds_read_b64_tr_b16 a[22:23], v25 offset:34816\n"
    "s_waitcnt lgkmcnt(12)\n"
    "v_mfma_f32_32x32x16_bf16 v[224:239], a[56:59], v[76:79], v[224:239]\n"
    "ds_read_b64_tr_b16 a[24:25], v26 offset:32768\n"
    "ds_read_b64_tr_b16 a[26:27], v26 offset:34816\n"
    "s_waitcnt lgkmcnt(12)\n"
    "v_mfma_f32_32x32x16_bf16 v[128:143], a[0:3], v[80:83], v[128:143]\n"
    "ds_read_b64_tr_b16 a[32:33], v20 offset:36864\n"
    "ds_read_b64_tr_b16 a[34:35], v20 offset:38912\n"
    "ds_read_b64_tr_b16 a[64:65], v112 offset:32768\n"
    "ds_read_b64_tr_b16 a[66:67], v112 offset:34816\n"
    "s_waitcnt lgkmcnt(14)\n"
    "v_mfma_f32_32x32x16_bf16 v[144:159], a[4:7], v[80:83], v[144:159]\n"
    "ds_read_b64_tr_b16 a[36:37], v21 offset:36864\n"
    "ds_read_b64_tr_b16 a[38:39], v21 offset:38912\n"
    "ds_read_b64_tr_b16 a[68:69], v113 offset:32768\n"
    "ds_read_b64_tr_b16 a[70:71], v113 offset:34816\n"
    "s_add_u32 m0, s67, 0x20800\n"
    "s_waitcnt lgkmcnt(15)\n"
    "v_mfma_f32_32x32x16_bf16 v[160:175], a[8:11], v[80:83], v[160:175]\n"
    "buffer_load_dwordx4 v4, s[48:51], s68 offen offset:0 lds\n"
    "buffer_load_dwordx4 v5, s[48:51], s68 offen offset:1024 lds\n"
    "ds_read_b64_tr_b16 a[40:41], v22 offset:36864\n"
    "ds_read_b64_tr_b16 a[42:43], v22 offset:38912\n"
    "ds_read_b64_tr_b16 a[72:73], v114 offset:32768\n"
    "ds_read_b64_tr_b16 a[74:75], v114 offset:34816\n"
    "s_waitcnt lgkmcnt(15)\n"
    "v_mfma_f32_32x32x16_bf16 v[176:191], a[12:15], v[80:83], v[176:191]\n"
    "ds_read_b64_tr_b16 a[44:45], v23 offset:36864\n"
    "ds_read_b64_tr_b16 a[46:47], v23 offset:38912\n"
    "ds_read_b64_tr_b16 a[76:77], v115 offset:32768\n"
    "ds_read_b64_tr_b16 a[78:79], v115 offset:34816\n"
    "v_mfma_f32_32x32x16_bf16 v[192:207], a[16:19], v[80:83], v[192:207]\n"
    "ds_read_b64_tr_b16 a[48:49], v24 offset:36864\n"
    "ds_read_b64_tr_b16 a[50:51], v24 offset:38912\n"
    "ds_read_b64_tr_b16 a[80:81], v116 offset:32768\n"
    "ds_read_b64_tr_b16 a[82:83], v116 offset:34816\n"
    "s_add_u32 s68, s68, 0x4000\n"
    "s_waitcnt lgkmcnt(15)\n"
    "v_mfma_f32_32x32x16_bf16 v[208:223], a[20:23], v[80:83], v[208:223]\n"
    "ds_read_b64_tr_b16 a[52:53], v25 offset:36864\n"
    "ds_read_b64_tr_b16 a[54:55], v25 offset:38912\n"
    "ds_read_b64_tr_b16 a[84:85], v117 offset:32768\n"
    "ds_read_b64_tr_b16 a[86:87], v117 offset:34816\n"
    "v_mfma_f32_32x32x16_bf16 v[224:239], a[24:27], v[80:83], v[224:239]\n"
    "ds_read_b64_tr_b16 a[56:57], v26 offset:36864\n"
    "ds_read_b64_tr_b16 a[58:59], v26 offset:38912\n"
    "ds_read_b64_tr_b16 a[88:89], v118 offset:32768\n"
    "ds_read_b64_tr_b16 a[90:91], v118 offset:34816\n"
    "v_mfma_f32_32x32x16_bf16 v[128:143], a[32:35], v[84:87], v[128:143]\n"
    "ds_read_b64_tr_b16 a[0:1], v20 offset:40960\n"
    "ds_read_b64_tr_b16 a[2:3], v20 offset:43008\n"
    "ds_read_b64_tr_b16 a[92:93], v112 offset:36864\n"
    "ds_read_b64_tr_b16 a[94:95], v112 offset:38912\n"
    "s_waitcnt lgkmcnt(15)\n"
    "v_mfma_f32_32x32x16_bf16 v[144:159], a[36:39], v[84:87], v[144:159]\n"
    "ds_read_b64_tr_b16 a[4:5], v21 offset:40960\n"
    "ds_read_b64_tr_b16 a[6:7], v21 offset:43008\n"
    "ds_read_b64_tr_b16 a[96:97], v113 offset:36864\n"
    "ds_read_b64_tr_b16 a[98:99], v113 offset:38912\n"
    "s_add_u32 m0, s67, 0x21000\n"
    "v_mfma_f32_32x32x16_bf16 v[160:175], a[40:43], v[84:87], v[160:175]\n"
    "buffer_load_dwordx4 v4, s[48:51], s68 offen offset:0 lds\n"
    "buffer_load_dwordx4 v5, s[48:51], s68 offen offset:1024 lds\n"
    "ds_read_b64_tr_b16 a[8:9], v22 offset:40960\n"
    "ds_read_b64_tr_b16 a[10:11], v22 offset:43008\n"
    "ds_read_b64_tr_b16 a[100:101], v114 offset:36864\n"
    "ds_read_b64_tr_b16 a[102:103], v114 offset:38912\n"
    "v_mfma_f32_32x32x16_bf16 v[176:191], a[44:47], v[84:87], v[176:191]\n"
    "ds_read_b64_tr_b16 a[12:13], v23 offset:40960\n"
    "ds_read_b64_tr_b16 a[14:15], v23 offset:43008\n"
    "ds_read_b64_tr_b16 a[104:105], v115 offset:36864\n"
    "ds_read_b64_tr_b16 a[106:107], v115 offset:38912\n"
    "s_waitcnt lgkmcnt(15)\n"
    "v_mfma_f32_32x32x16_bf16 v[192:207], a[48:51], v[84:87], v[192:207]\n"
    "ds_read_b64_tr_b16 a[16:17], v24 offset:40960\n"
    "ds_read_b64_tr_b16 a[18:19], v24 offset:43008\n"
    "ds_read_b64_tr_b16 a[108:109], v116 offset:36864\n"
    "ds_read_b64_tr_b16 a[110:111], v116 offset:38912\n"
    "s_add_u32 s68, s68, 0x4000\n"
    "v_mfma_f32_32x32x16_bf16 v[208:223], a[52:55], v[84:87], v[208:223]\n"
    "ds_read_b64_tr_b16 a[20:21], v25 offset:40960\n"
    "ds_read_b64_tr_b16 a[22:23], v25 offset:43008\n"
    "ds_read_b64_tr_b16 a[112:113], v117 offset:36864\n"
    "ds_read_b64_tr_b16 a[114:115], v117 offset:38912\n"
    "v_mfma_f32_32x32x16_bf16 v[224:239], a[56:59], v[84:87], v[224:239]\n"
    "ds_read_b64_tr_b16 a[24:25], v26 offset:40960\n"
    "ds_read_b64_tr_b16 a[26:27], v26 offset:43008\n"
    "ds_read_b64_tr_b16 a[116:117], v118 offset:36864\n"
    "ds_read_b64_tr_b16 a[118:119], v118 offset:38912\n"
    "s_waitcnt lgkmcnt(15)\n"
    "v_mfma_f32_32x32x16_bf16 v[128:143], a[0:3], v[88:91], v[128:143]\n"
    "ds_read_b64_tr_b16 a[32:33], v20 offset:45056\n"
    "ds_read_b64_tr_b16 a[34:35], v20 offset:47104\n"
    "ds_read_b64_tr_b16 a[120:121], v112 offset:40960\n"
    "ds_read_b64_tr_b16 a[122:123], v112 offset:43008\n"
    "v_mfma_f32_32x32x16_bf16 v[144:159], a[4:7], v[88:91], v[144:159]\n"
    "ds_read_b64_tr_b16 a[36:37], v21 offset:45056\n"
    "ds_read_b64_tr_b16 a[38:39], v21 offset:47104\n"
    "ds_read_b64_tr_b16 a[124:125], v113 offset:40960\n"
    "ds_read_b64_tr_b16 a[126:127], v113 offset:43008\n"
    "s_add_u32 m0, s67, 0x21800\n"
    "v_mfma_f32_32x32x16_bf16 v[160:175], a[8:11], v[88:91], v[160:175]\n"
    "buffer_load_dwordx4 v4, s[48:51], s68 offen offset:0 lds\n"
    "buffer_load_dwordx4 v5, s[48:51], s68 offen offset:1024 lds\n"
    "ds_read_b64_tr_b16 a[40:41], v22 offset:45056\n"
    "ds_read_b64_tr_b16 a[42:43], v22 offset:47104\n"
    "ds_read_b64_tr_b16 a[128:129], v114 offset:40960\n"
    "ds_read_b64_tr_b16 a[130:131], v114 offset:43008\n"
    "s_waitcnt lgkmcnt(15)\n"
    "v_mfma_f32_32x32x16_bf16 v[176:191], a[12:15], v[88:91], v[176:191]\n"
    "ds_read_b64_tr_b16 a[44:45], v23 offset:45056\n"
    "ds_read_b64_tr_b16 a[46:47], v23 offset:47104\n"
    "ds_read_b64_tr_b16 a[132:133], v115 offset:40960\n"
    "ds_read_b64_tr_b16 a[134:135], v115 offset:43008\n"
    "v_mfma_f32_32x32x16_bf16 v[192:207], a[16:19], v[88:91], v[192:207]\n"
    "ds_read_b64_tr_b16 a[48:49], v24 offset:45056\n"
    "ds_read_b64_tr_b16 a[50:51], v24 offset:47104\n"
    "ds_read_b64_tr_b16 a[136:137], v116 offset:40960\n"
    "ds_read_b64_tr_b16 a[138:139], v116 offset:43008\n"
    "s_add_u32 s68, s68, 0x4000\n"
    "v_mfma_f32_32x32x16_bf16 v[208:223], a[20:23], v[88:91], v[208:223]\n"
    "ds_read_b64_tr_b16 a[52:53], v25 offset:45056\n"
    "ds_read_b64_tr_b16 a[54:55], v25 offset:47104\n"
    "ds_read_b64_tr_b16 a[140:141], v117 offset:40960\n"
    "ds_read_b64_tr_b16 a[142:143], v117 offset:43008\n"
    "s_waitcnt lgkmcnt(15)\n"
    "v_mfma_f32_32x32x16_bf16 v[224:239], a[24:27], v[88:91], v[224:239]\n"
    "ds_read_b64_tr_b16 a[56:57], v26 offset:45056\n"
    "ds_read_b64_tr_b16 a[58:59], v26 offset:47104\n"
    "ds_read_b64_tr_b16 a[144:145], v118 offset:40960\n"
    "ds_read_b64_tr_b16 a[146:147], v118 offset:43008\n"
    "v_mfma_f32_32x32x16_bf16 v[128:143], a[32:35], v[92:95], v[128:143]\n"
    "ds_read_b64_tr_b16 a[148:149], v112 offset:45056\n"
    "ds_read_b64_tr_b16 a[150:151], v112 offset:47104\n"
    "v_mfma_f32_32x32x16_bf16 v[144:159], a[36:39], v[92:95], v[144:159]\n"
    "ds_read_b64_tr_b16 a[152:153], v113 offset:45056\n"
    "ds_read_b64_tr_b16 a[154:155], v113 offset:47104\n"
    "s_waitcnt lgkmcnt(15)\n"
    "v_mfma_f32_32x32x16_bf16 v[160:175], a[40:43], v[92:95], v[160:175]\n"
    "ds_read_b64_tr_b16 a[156:157], v114 offset:45056\n"
    "ds_read_b64_tr_b16 a[158:159], v114 offset:47104\n"
    "v_mfma_f32_32x32x16_bf16 v[176:191], a[44:47], v[92:95], v[176:191]\n"
    "ds_read_b64_tr_b16 a[160:161], v115 offset:45056\n"
    "ds_read_b64_tr_b16 a[162:163], v115 offset:47104\n"
    "s_waitcnt lgkmcnt(15)\n"
    "v_mfma_f32_32x32x16_bf16 v[192:207], a[48:51], v[92:95], v[192:207]\n"
    "ds_read_b64_tr_b16 a[164:165], v116 offset:45056\n"
    "ds_read_b64_tr_b16 a[166:167], v116 offset:47104\n"
    "s_waitcnt lgkmcnt(15)\n"
    "v_mfma_f32_32x32x16_bf16 v[208:223], a[52:55], v[92:95], v[208:223]\n"
    "ds_read_b64_tr_b16 a[168:169], v117 offset:45056\n"
    "ds_read_b64_tr_b16 a[170:171], v117 offset:47104\n"
    "s_waitcnt lgkmcnt(14)\n"
    "v_mfma_f32_32x32x16_bf16 v[224:239], a[56:59], v[92:95], v[224:239]\n"
    "ds_read_b64_tr_b16 a[172:173], v118 offset:45056\n"
    "ds_read_b64_tr_b16 a[174:175], v118 offset:47104\n"
    "s_mov_b32 s77, 0x1000\n"
    "s_waitcnt lgkmcnt(0)\n"
    "s_barrier\n"
    "v_mfma_f32_32x32x16_bf16 v[128:143], a[64:67], v[96:99], v[128:143]\n"
    "v_mfma_f32_32x32x16_bf16 v[128:143], a[92:95], v[100:103], v[128:143]\n"
    "s_add_u32 m0, s67, 0x10000\n"
    "s_nop 0\n"
    "buffer_load_dwordx4 v4, s[48:51], s68 offen offset:0 lds\n"
    "buffer_load_dwordx4 v5, s[48:51], s68 offen offset:1024 lds\n"
    "s_add_u32 s68, s68, 0x4000\n"
    "v_mfma_f32_32x32x16_bf16 v[128:143], a[120:123], v[104:107], v[128:143]\n"
    "s_waitcnt vmcnt(18)\n"
    "v_mfma_f32_32x32x16_bf16 v[128:143], a[148:151], v[108:111], v[128:143]\n"
    "v_mfma_f32_32x32x16_bf16 v[144:159], a[68:71], v[96:99], v[144:159]\n"
    "s_add_u32 m0, s67, 0x10800\n"
    "s_nop 0\n"
    "buffer_load_dwordx4 v4, s[48:51], s68 offen offset:0 lds\n"
    "buffer_load_dwordx4 v5, s[48:51], s68 offen offset:1024 lds\n"
    "s_add_u32 s68, s68, 0x4000\n"
    "v_mfma_f32_32x32x16_bf16 v[144:159], a[96:99], v[100:103], v[144:159]\n"
    "v_mfma_f32_32x32x16_bf16 v[144:159], a[124:127], v[104:107], v[144:159]\n"
    "s_nop 4\n"
    "v_mul_f32 v32, 0xbfb8aa3b, v128\n"
    "v_mul_f32 v33, 0xbfb8aa3b, v129\n"
    "v_mul_f32 v34, 0xbfb8aa3b, v130\n"
    "v_mul_f32 v35, 0xbfb8aa3b, v131\n"
    "v_mul_f32 v36, 0xbfb8aa3b, v136\n"
    "v_mul_f32 v37, 0xbfb8aa3b, v137\n"
    "v_mfma_f32_32x32x16_bf16 v[144:159], a[152:155], v[108:111], v[144:159]\n"
    "s_add_u32 m0, s67, 0x11000\n"
    "s_nop 0\n"
    "buffer_load_dwordx4 v4, s[48:51], s68 offen offset:0 lds\n"
    "buffer_load_dwordx4 v5, s[48:51], s68 offen offset:1024 lds\n"
    "s_add_u32 s68, s68, 0x4000\n"
    "v_mul_f32 v38, 0xbfb8aa3b, v138\n"
    "v_mul_f32 v39, 0xbfb8aa3b, v139\n"
    "v_exp_f32 v32, v32\n"
    "v_exp_f32 v33, v33\n"
    "v_mfma_f32_32x32x16_bf16 v[160:175], a[72:75], v[96:99], v[160:175]\n"
    "v_exp_f32 v34, v34\n"
    "v_exp_f32 v35, v35\n"
    "v_exp_f32 v36, v36\n"
    "v_mfma_f32_32x32x16_bf16 v[160:175], a[100:103], v[100:103], v[160:175]\n"
    "v_exp_f32 v37, v37\n"
    "v_exp_f32 v38, v38\n"
    "v_exp_f32 v39, v39\n"
    "v_mfma_f32_32x32x16_bf16 v[160:175], a[128:131], v[104:107], v[160:175]\n"
    "s_add_u32 m0, s67, 0x11800\n"
    "s_nop 0\n"
    "buffer_load_dwordx4 v4, s[48:51], s68 offen offset:0 lds\n"
    "buffer_load_dwordx4 v5, s[48:51], s68 offen offset:1024 lds\n"
    "s_add_u32 s68, s68, 0x4000\n"
    "v_mul_f32 v128, v128, v132\n"
    "v_mul_f32 v129, v129, v133\n"
    "v_mul_f32 v130, v130, v134\n"
    "v_mul_f32 v131, v131, v135\n"
    "v_mul_f32 v136, v136, v140\n"
    "v_mul_f32 v137, v137, v141\n"
    "v_mfma_f32_32x32x16_bf16 v[160:175], a[156:159], v[108:111], v[160:175]\n"
    "v_mul_f32 v138, v138, v142\n"
    "v_mul_f32 v139, v139, v143\n"
    "v_add_f32 v32, 0x3f800000, v32\n"
    "v_add_f32 v33, 0x3f800000, v33\n"
    "v_add_f32 v34, 0x3f800000, v34\n"
    "v_add_f32 v35, 0x3f800000, v35\n"
    "v_mfma_f32_32x32x16_bf16 v[176:191], a[76:79], v[96:99], v[176:191]\n"
    "v_add_f32 v36, 0x3f800000, v36\n"
    "v_add_f32 v37, 0x3f800000, v37\n"
    "v_add_f32 v38, 0x3f800000, v38\n"
    "v_add_f32 v39, 0x3f800000, v39\n"
    "v_rcp_f32 v32, v32\n"
    "v_mfma_f32_32x32x16_bf16 v[176:191], a[104:107], v[100:103], v[176:191]\n"
    "s_add_u32 m0, s67, 0x8000\n"
    "s_nop 0\n"
    "buffer_load_dwordx4 v4, s[48:51], s68 offen offset:0 lds\n"
    "buffer_load_dwordx4 v5, s[48:51], s68 offen offset:1024 lds\n"
    "s_add_u32 s68, s68, 0x4000\n"
    "v_rcp_f32 v33, v33\n"
    "v_rcp_f32 v34, v34\n"
    "v_rcp_f32 v35, v35\n"
    "v_mfma_f32_32x32x16_bf16 v[176:191], a[132:135], v[104:107], v[176:191]\n"
    "v_rcp_f32 v36, v36\n"
    "v_rcp_f32 v37, v37\n"
    "v_rcp_f32 v38, v38\n"
    "v_mfma_f32_32x32x16_bf16 v[176:191], a[160:163], v[108:111], v[176:191]\n"
    "v_rcp_f32 v39, v39\n"
    "v_mul_f32 v32, v32, v128\n"
    "v_mul_f32 v33, v33, v129\n"
    "v_mul_f32 v34, v34, v130\n"
    "v_mul_f32 v35, v35, v131\n"
    "v_mfma_f32_32x32x16_bf16 v[192:207], a[80:83], v[96:99], v[192:207]\n"
    "s_add_u32 m0, s67, 0x8800\n"
    "s_nop 0\n"
    "buffer_load_dwordx4 v4, s[48:51], s68 offen offset:0 lds\n"
    "buffer_load_dwordx4 v5, s[48:51], s68 offen offset:1024 lds\n"
    "s_add_u32 s68, s68, 0x4000\n"
    "v_mul_f32 v36, v36, v136\n"
    "v_mul_f32 v37, v37, v137\n"
    "v_mul_f32 v38, v38, v138\n"
    "v_mul_f32 v39, v39, v139\n"
    "v_cvt_pk_bf16_f32 v48, v32, v33\n"
    "v_cvt_pk_bf16_f32 v49, v34, v35\n"
    "v_mfma_f32_32x32x16_bf16 v[192:207], a[108:111], v[100:103], v[192:207]\n"
    "v_cvt_pk_bf16_f32 v50, v36, v37\n"
    "v_cvt_pk_bf16_f32 v51, v38, v39\n"
    "buffer_store_dwordx4 v[48:51], v2, s[44:47], 0 offen offset:0 sc1\n"
    "v_mul_f32 v40, 0xbfb8aa3b, v144\n"
    "v_mul_f32 v41, 0xbfb8aa3b, v145\n"
    "v_mul_f32 v42, 0xbfb8aa3b, v146\n"
    "v_mfma_f32_32x32x16_bf16 v[192:207], a[136:139], v[104:107], v[192:207]\n"
    "v_mul_f32 v43, 0xbfb8aa3b, v147\n"
    "v_mul_f32 v44, 0xbfb8aa3b, v152\n"
    "v_mul_f32 v45, 0xbfb8aa3b, v153\n"
    "v_mul_f32 v46, 0xbfb8aa3b, v154\n"
    "v_mul_f32 v47, 0xbfb8aa3b, v155\n"
    "v_mfma_f32_32x32x16_bf16 v[192:207], a[164:167], v[108:111], v[192:207]\n"
    "s_add_u32 m0, s67, 0x9000\n"
    "s_nop 0\n"
    "buffer_load_dwordx4 v4, s[48:51], s68 offen offset:0 lds\n"
    "buffer_load_dwordx4 v5, s[48:51], s68 offen offset:1024 lds\n"
    "s_add_u32 s68, s68, 0x4000\n"
    "v_exp_f32 v40, v40\n"
    "v_exp_f32 v41, v41\n"
    "v_exp_f32 v42, v42\n"
    "v_mfma_f32_32x32x16_bf16 v[208:223], a[84:87], v[96:99], v[208:223]\n"
    "v_exp_f32 v43, v43\n"
    "v_exp_f32 v44, v44\n"
    "v_exp_f32 v45, v45\n"
    "v_mfma_f32_32x32x16_bf16 v[208:223], a[112:115], v[100:103], v[208:223]\n"
    "v_exp_f32 v46, v46\n"
    "v_exp_f32 v47, v47\n"
    "v_mul_f32 v144, v144, v148\n"
    "v_mul_f32 v145, v145, v149\n"
    "v_mfma_f32_32x32x16_bf16 v[208:223], a[140:143], v[104:107], v[208:223]\n"
    "s_add_u32 m0, s67, 0x9800\n"
    "s_nop 0\n"
    "buffer_load_dwordx4 v4, s[48:51], s68 offen offset:0 lds\n"
    "buffer_load_dwordx4 v5, s[48:51], s68 offen offset:1024 lds\n"
    "s_add_u32 s68, s68, 0x4000\n"
    "v_mul_f32 v146, v146, v150\n"
    "v_mul_f32 v147, v147, v151\n"
    "v_mul_f32 v152, v152, v156\n"
    "v_mul_f32 v153, v153, v157\n"
    "v_mul_f32 v154, v154, v158\n"
    "v_mul_f32 v155, v155, v159\n"
    "v_mfma_f32_32x32x16_bf16 v[208:223], a[168:171], v[108:111], v[208:223]\n"
    "v_add_f32 v40, 0x3f800000, v40\n"
    "v_add_f32 v41, 0x3f800000, v41\n"
    "v_add_f32 v42, 0x3f800000, v42\n"
    "v_add_f32 v43, 0x3f800000, v43\n"
    "v_add_f32 v44, 0x3f800000, v44\n"
    "v_add_f32 v45, 0x3f800000, v45\n"
    "v_mfma_f32_32x32x16_bf16 v[224:239], a[88:91], v[96:99], v[224:239]\n"
    "v_add_f32 v46, 0x3f800000, v46\n"
    "v_add_f32 v47, 0x3f800000, v47\n"
    "v_rcp_f32 v40, v40\n"
    "v_rcp_f32 v41, v41\n"
    "v_mfma_f32_32x32x16_bf16 v[224:239], a[116:119], v[100:103], v[224:239]\n"
    "v_rcp_f32 v42, v42\n"
    "v_rcp_f32 v43, v43\n"
    "v_rcp_f32 v44, v44\n"
    "v_mfma_f32_32x32x16_bf16 v[224:239], a[144:147], v[104:107], v[224:239]\n"
    "v_rcp_f32 v45, v45\n"
    "v_rcp_f32 v46, v46\n"
    "v_rcp_f32 v47, v47\n"
    "v_mfma_f32_32x32x16_bf16 v[224:239], a[172:175], v[108:111], v[224:239]\n"
    "v_mul_f32 v40, v40, v144\n"
    "v_mul_f32 v41, v41, v145\n"
    "v_mul_f32 v42, v42, v146\n"
    "v_mul_f32 v43, v43, v147\n"
    "v_mul_f32 v44, v44, v152\n"
    "v_mul_f32 v45, v45, v153\n"
    "s_nop 7\n"
    "v_mul_f32 v46, v46, v154\n"
    "v_mul_f32 v47, v47, v155\n"
    "v_cvt_pk_bf16_f32 v52, v40, v41\n"
    "v_cvt_pk_bf16_f32 v53, v42, v43\n"
    "v_cvt_pk_bf16_f32 v54, v44, v45\n"
    "v_cvt_pk_bf16_f32 v55, v46, v47\n"
    "buffer_store_dwordx4 v[52:55], v2, s[44:47], 0 offen offset:1024 sc1\n"
    "v_mul_f32 v32, 0xbfb8aa3b, v160\n"
    "v_mul_f32 v33, 0xbfb8aa3b, v161\n"
    "v_mul_f32 v34, 0xbfb8aa3b, v162\n"
    "v_mul_f32 v35, 0xbfb8aa3b, v163\n"
    "v_mul_f32 v36, 0xbfb8aa3b, v168\n"
    "v_mul_f32 v37, 0xbfb8aa3b, v169\n"
    "v_mul_f32 v38, 0xbfb8aa3b, v170\n"
    "v_mul_f32 v39, 0xbfb8aa3b, v171\n"
    "v_exp_f32 v32, v32\n"
    "v_exp_f32 v33, v33\n"
    "v_exp_f32 v34, v34\n"
    "v_exp_f32 v35, v35\n"
    "v_exp_f32 v36, v36\n"
    "v_exp_f32 v37, v37\n"
    "v_exp_f32 v38, v38\n"
    "v_exp_f32 v39, v39\n"
    "v_mul_f32 v160, v160, v164\n"
    "v_mul_f32 v161, v161, v165\n"
    "v_mul_f32 v162, v162, v166\n"
    "v_mul_f32 v163, v163, v167\n"
    "v_mul_f32 v168, v168, v172\n"
    "v_mul_f32 v169, v169, v173\n"
    "v_mul_f32 v170, v170, v174\n"
    "v_mul_f32 v171, v171, v175\n"
    "v_add_f32 v32, 0x3f800000, v32\n"
    "v_add_f32 v33, 0x3f800000, v33\n"
    "v_add_f32 v34, 0x3f800000, v34\n"
    "v_add_f32 v35, 0x3f800000, v35\n"
    "v_add_f32 v36, 0x3f800000, v36\n"
    "v_add_f32 v37, 0x3f800000, v37\n"
    "v_add_f32 v38, 0x3f800000, v38\n"
    "v_add_f32 v39, 0x3f800000, v39\n"
    "v_rcp_f32 v32, v32\n"
    "v_rcp_f32 v33, v33\n"
    "v_rcp_f32 v34, v34\n"
    "v_rcp_f32 v35, v35\n"
    "v_rcp_f32 v36, v36\n"
    "v_rcp_f32 v37, v37\n"
    "v_rcp_f32 v38, v38\n"
    "v_rcp_f32 v39, v39\n"
    "v_mul_f32 v32, v32, v160\n"
    "v_mul_f32 v33, v33, v161\n"
    "v_mul_f32 v34, v34, v162\n"
    "v_mul_f32 v35, v35, v163\n"
    "v_mul_f32 v36, v36, v168\n"
    "v_mul_f32 v37, v37, v169\n"
    "v_mul_f32 v38, v38, v170\n"
    "v_mul_f32 v39, v39, v171\n"
    "v_cvt_pk_bf16_f32 v56, v32, v33\n"
    "v_cvt_pk_bf16_f32 v57, v34, v35\n"
    "v_cvt_pk_bf16_f32 v58, v36, v37\n"
    "v_cvt_pk_bf16_f32 v59, v38, v39\n"
    "buffer_store_dwordx4 v[56:59], v2, s[44:47], 0 offen offset:2048 sc1\n"
    "v_mul_f32 v40, 0xbfb8aa3b, v176\n"
    "v_mul_f32 v41, 0xbfb8aa3b, v177\n"
    "v_mul_f32 v42, 0xbfb8aa3b, v178\n"
    "v_mul_f32 v43, 0xbfb8aa3b, v179\n"
    "v_mul_f32 v44, 0xbfb8aa3b, v184\n"
    "v_mul_f32 v45, 0xbfb8aa3b, v185\n"
    "v_mul_f32 v46, 0xbfb8aa3b, v186\n"
    "v_mul_f32 v47, 0xbfb8aa3b, v187\n"
    "v_exp_f32 v40, v40\n"
    "v_exp_f32 v41, v41\n"
    "v_exp_f32 v42, v42\n"
    "v_exp_f32 v43, v43\n"
    "v_exp_f32 v44, v44\n"
    "v_exp_f32 v45, v45\n"
    "v_exp_f32 v46, v46\n"
    "v_exp_f32 v47, v47\n"
    "v_mul_f32 v176, v176, v180\n"
    "v_mul_f32 v177, v177, v181\n"
    "v_mul_f32 v178, v178, v182\n"
    "v_mul_f32 v179, v179, v183\n"
    "v_mul_f32 v184, v184, v188\n"
    "v_mul_f32 v185, v185, v189\n"
    "v_mul_f32 v186, v186, v190\n"
    "v_mul_f32 v187, v187, v191\n"
    "v_add_f32 v40, 0x3f800000, v40\n"
    "v_add_f32 v41, 0x3f800000, v41\n"
    "v_add_f32 v42, 0x3f800000, v42\n"
    "v_add_f32 v43, 0x3f800000, v43\n"
    "v_add_f32 v44, 0x3f800000, v44\n"
    "v_add_f32 v45, 0x3f800000, v45\n"
    "v_add_f32 v46, 0x3f800000, v46\n"
    "v_add_f32 v47, 0x3f800000, v47\n"
    "v_rcp_f32 v40, v40\n"
    "v_rcp_f32 v41, v41\n"
    "v_rcp_f32 v42, v42\n"
    "v_rcp_f32 v43, v43\n"
    "v_rcp_f32 v44, v44\n"
    "v_rcp_f32 v45, v45\n"
    "v_rcp_f32 v46, v46\n"
    "v_rcp_f32 v47, v47\n"
    "v_mul_f32 v40, v40, v176\n"
    "v_mul_f32 v41, v41, v177\n"
    "v_mul_f32 v42, v42, v178\n"
    "v_mul_f32 v43, v43, v179\n"
    "v_mul_f32 v44, v44, v184\n"
    "v_mul_f32 v45, v45, v185\n"
    "v_mul_f32 v46, v46, v186\n"
    "v_mul_f32 v47, v47, v187\n"
    "v_cvt_pk_bf16_f32 v60, v40, v41\n"
    "v_cvt_pk_bf16_f32 v61, v42, v43\n"
    "v_cvt_pk_bf16_f32 v62, v44, v45\n"
    "v_cvt_pk_bf16_f32 v63, v46, v47\n"
    "buffer_store_dwordx4 v[60:63], v2, s[44:47], 0 offen offset:3072 sc1\n"
    "v_mul_f32 v32, 0xbfb8aa3b, v192\n"
    "v_mul_f32 v33, 0xbfb8aa3b, v193\n"
    "v_mul_f32 v34, 0xbfb8aa3b, v194\n"
    "v_mul_f32 v35, 0xbfb8aa3b, v195\n"
    "v_mul_f32 v36, 0xbfb8aa3b, v200\n"
    "v_mul_f32 v37, 0xbfb8aa3b, v201\n"
    "v_mul_f32 v38, 0xbfb8aa3b, v202\n"
    "v_mul_f32 v39, 0xbfb8aa3b, v203\n"
    "v_exp_f32 v32, v32\n"
    "v_exp_f32 v33, v33\n"
    "v_exp_f32 v34, v34\n"
    "v_exp_f32 v35, v35\n"
    "v_exp_f32 v36, v36\n"
    "v_exp_f32 v37, v37\n"
    "v_exp_f32 v38, v38\n"
    "v_exp_f32 v39, v39\n"
    "v_mul_f32 v192, v192, v196\n"
    "v_mul_f32 v193, v193, v197\n"
    "v_mul_f32 v194, v194, v198\n"
    "v_mul_f32 v195, v195, v199\n"
    "v_mul_f32 v200, v200, v204\n"
    "v_mul_f32 v201, v201, v205\n"
    "v_mul_f32 v202, v202, v206\n"
    "v_mul_f32 v203, v203, v207\n"
    "v_add_f32 v32, 0x3f800000, v32\n"
    "v_add_f32 v33, 0x3f800000, v33\n"
    "v_add_f32 v34, 0x3f800000, v34\n"
    "v_add_f32 v35, 0x3f800000, v35\n"
    "v_add_f32 v36, 0x3f800000, v36\n"
    "v_add_f32 v37, 0x3f800000, v37\n"
    "v_add_f32 v38, 0x3f800000, v38\n"
    "v_add_f32 v39, 0x3f800000, v39\n"
    "v_rcp_f32 v32, v32\n"
    "v_rcp_f32 v33, v33\n"
    "v_rcp_f32 v34, v34\n"
    "v_rcp_f32 v35, v35\n"
    "v_rcp_f32 v36, v36\n"
    "v_rcp_f32 v37, v37\n"
    "v_rcp_f32 v38, v38\n"
    "v_rcp_f32 v39, v39\n"
    "v_mul_f32 v32, v32, v192\n"
    "v_mul_f32 v33, v33, v193\n"
    "v_mul_f32 v34, v34, v194\n"
    "v_mul_f32 v35, v35, v195\n"
    "v_mul_f32 v36, v36, v200\n"
    "v_mul_f32 v37, v37, v201\n"
    "v_mul_f32 v38, v38, v202\n"
    "v_mul_f32 v39, v39, v203\n"
    "v_cvt_pk_bf16_f32 v48, v32, v33\n"
    "v_cvt_pk_bf16_f32 v49, v34, v35\n"
    "v_cvt_pk_bf16_f32 v50, v36, v37\n"
    "v_cvt_pk_bf16_f32 v51, v38, v39\n"
    "buffer_store_dwordx4 v[48:51], v2, s[44:47], s77 offen offset:0 sc1\n"
    "v_mul_f32 v40, 0xbfb8aa3b, v208\n"
    "v_mul_f32 v41, 0xbfb8aa3b, v209\n"
    "v_mul_f32 v42, 0xbfb8aa3b, v210\n"
    "v_mul_f32 v43, 0xbfb8aa3b, v211\n"
    "v_mul_f32 v44, 0xbfb8aa3b, v216\n"
    "v_mul_f32 v45, 0xbfb8aa3b, v217\n"
    "v_mul_f32 v46, 0xbfb8aa3b, v218\n"
    "v_mul_f32 v47, 0xbfb8aa3b, v219\n"
    "v_exp_f32 v40, v40\n"
    "v_exp_f32 v41, v41\n"
    "v_exp_f32 v42, v42\n"
    "v_exp_f32 v43, v43\n"
    "v_exp_f32 v44, v44\n"
    "v_exp_f32 v45, v45\n"
    "v_exp_f32 v46, v46\n"
    "v_exp_f32 v47, v47\n"
    "v_mul_f32 v208, v208, v212\n"
    "v_mul_f32 v209, v209, v213\n"
    "v_mul_f32 v210, v210, v214\n"
    "v_mul_f32 v211, v211, v215\n"
    "v_mul_f32 v216, v216, v220\n"
    "v_mul_f32 v217, v217, v221\n"
    "v_mul_f32 v218, v218, v222\n"
    "v_mul_f32 v219, v219, v223\n"
    "v_add_f32 v40, 0x3f800000, v40\n"
    "v_add_f32 v41, 0x3f800000, v41\n"
    "v_add_f32 v42, 0x3f800000, v42\n"
    "v_add_f32 v43, 0x3f800000, v43\n"
    "v_add_f32 v44, 0x3f800000, v44\n"
    "v_add_f32 v45, 0x3f800000, v45\n"
    "v_add_f32 v46, 0x3f800000, v46\n"
    "v_add_f32 v47, 0x3f800000, v47\n"
    "v_rcp_f32 v40, v40\n"
    "v_rcp_f32 v41, v41\n"
    "v_rcp_f32 v42, v42\n"
    "v_rcp_f32 v43, v43\n"
    "v_rcp_f32 v44, v44\n"
    "v_rcp_f32 v45, v45\n"
    "v_rcp_f32 v46, v46\n"
    "v_rcp_f32 v47, v47\n"
    "v_mul_f32 v40, v40, v208\n"
    "v_mul_f32 v41, v41, v209\n"
    "v_mul_f32 v42, v42, v210\n"
    "v_mul_f32 v43, v43, v211\n"
    "v_mul_f32 v44, v44, v216\n"
    "v_mul_f32 v45, v45, v217\n"
    "v_mul_f32 v46, v46, v218\n"
    "v_mul_f32 v47, v47, v219\n"
    "v_cvt_pk_bf16_f32 v52, v40, v41\n"
    "v_cvt_pk_bf16_f32 v53, v42, v43\n"
    "v_cvt_pk_bf16_f32 v54, v44, v45\n"
    "v_cvt_pk_bf16_f32 v55, v46, v47\n"
    "buffer_store_dwordx4 v[52:55], v2, s[44:47], s77 offen offset:1024 sc1\n"
    "v_mul_f32 v32, 0xbfb8aa3b, v224\n"
    "v_mul_f32 v33, 0xbfb8aa3b, v225\n"
    "v_mul_f32 v34, 0xbfb8aa3b, v226\n"
    "v_mul_f32 v35, 0xbfb8aa3b, v227\n"
    "v_mul_f32 v36, 0xbfb8aa3b, v232\n"
    "v_mul_f32 v37, 0xbfb8aa3b, v233\n"
    "v_mul_f32 v38, 0xbfb8aa3b, v234\n"
    "v_mul_f32 v39, 0xbfb8aa3b, v235\n"
    "v_exp_f32 v32, v32\n"
    "v_exp_f32 v33, v33\n"
    "v_exp_f32 v34, v34\n"
    "v_exp_f32 v35, v35\n"
    "v_exp_f32 v36, v36\n"
    "v_exp_f32 v37, v37\n"
    "v_exp_f32 v38, v38\n"
    "v_exp_f32 v39, v39\n"
    "v_mul_f32 v224, v224, v228\n"
    "v_mul_f32 v225, v225, v229\n"
    "v_mul_f32 v226, v226, v230\n"
    "v_mul_f32 v227, v227, v231\n"
    "v_mul_f32 v232, v232, v236\n"
    "v_mul_f32 v233, v233, v237\n"
    "v_mul_f32 v234, v234, v238\n"
    "v_mul_f32 v235, v235, v239\n"
    "v_add_f32 v32, 0x3f800000, v32\n"
    "v_add_f32 v33, 0x3f800000, v33\n"
    "v_add_f32 v34, 0x3f800000, v34\n"
    "v_add_f32 v35, 0x3f800000, v35\n"
    "v_add_f32 v36, 0x3f800000, v36\n"
    "v_add_f32 v37, 0x3f800000, v37\n"
    "v_add_f32 v38, 0x3f800000, v38\n"
    "v_add_f32 v39, 0x3f800000, v39\n"
    "v_rcp_f32 v32, v32\n"
    "v_rcp_f32 v33, v33\n"
    "v_rcp_f32 v34, v34\n"
    "v_rcp_f32 v35, v35\n"
    "v_rcp_f32 v36, v36\n"
    "v_rcp_f32 v37, v37\n"
    "v_rcp_f32 v38, v38\n"
    "v_rcp_f32 v39, v39\n"
    "v_mul_f32 v32, v32, v224\n"
    "v_mul_f32 v33, v33, v225\n"
    "v_mul_f32 v34, v34, v226\n"
    "v_mul_f32 v35, v35, v227\n"
    "v_mul_f32 v36, v36, v232\n"
    "v_mul_f32 v37, v37, v233\n"
    "v_mul_f32 v38, v38, v234\n"
    "v_mul_f32 v39, v39, v235\n"
    "v_cvt_pk_bf16_f32 v56, v32, v33\n"
    "v_cvt_pk_bf16_f32 v57, v34, v35\n"
    "v_cvt_pk_bf16_f32 v58, v36, v37\n"
    "v_cvt_pk_bf16_f32 v59, v38, v39\n"
    "buffer_store_dwordx4 v[56:59], v2, s[44:47], s77 offen offset:2048 sc1\n"
    "s_add_u32 s92, s86, 0x4b00\n"
    "s_addc_u32 s93, s87, 0\n"
    "s_lshl_b32 s99, s70, 8\n"
    "s_add_u32 s92, s92, s99\n"
    "s_addc_u32 s93, s93, 0\n"
    "s_waitcnt vmcnt(0)\n"
    "s_barrier\n"
    "s_cmp_lg_u32 s64, 0\n"
    "s_cbranch_scc1 .Lsig13\n"
    "v_mov_b32 v13, 0\n"
    "v_mov_b32 v14, 1\n"
    "s_mov_b64 exec, 1\n"
    "global_atomic_add v13, v14, s[92:93]\n"
    "s_mov_b64 exec, -1\n"
    ".Lsig13:\n"
    "v_lshlrev_b32 v2, 4, v1\n"
    "v_add_u32 v3, 0x1c000, v2\n"
    "v_lshrrev_b32 v8, 5, v1\n"
    "v_lshlrev_b32 v8, 9, v8\n"
    "v_bfe_u32 v9, v1, 2, 2\n"
    "v_lshl_add_u32 v8, v9, 6, v8\n"
    "v_bfe_u32 v9, v1, 4, 1\n"
    "v_lshl_add_u32 v8, v9, 5, v8\n"
    "v_and_b32 v9, 3, v1\n"
    "v_lshl_add_u32 v8, v9, 3, v8\n"
    "v_add_u32 v6, s67, v8\n"
    "v_add_u32 v7, 0x10000, v6\n"
    "v_add_u32 v25, 0x20000, v6\n"
    "s_mul_i32 s77, s64, 0x7000\n"
    "s_mul_i32 s78, s64, 0x7000\n"
    "s_lshr_b32 s74, s64, 1\n"
    "s_and_b32 s75, s64, 1\n"
    "s_mov_b64 s[44:45], %0\n"
    "s_add_u32 s44, s44, 0x2800\n"
    "s_addc_u32 s45, s45, 0\n"
    "s_and_b32 s45, s45, 0xffff\n"
    "s_mov_b32 s46, 0x800\n"
    "s_mov_b32 s47, 0x20000\n"
    "v_lshrrev_b32 v8, 5, v1\n"
    "v_lshlrev_b32 v8, 4, v8\n"
    "s_lshl_b32 s65, s72, 8\n"
    "s_lshl_b32 s66, s74, 7\n"
    "s_add_u32 s65, s65, s66\n"
    "buffer_load_dwordx4 v[96:99], v8, s[44:47], s65 offen offset:0 sc1\n"
    "buffer_load_dwordx4 v[100:103], v8, s[44:47], s65 offen offset:32 sc1\n"
    "buffer_load_dwordx4 v[104:107], v8, s[44:47], s65 offen offset:64 sc1\n"
    "buffer_load_dwordx4 v[108:111], v8, s[44:47], s65 offen offset:96 sc1\n"
    "s_mov_b64 s[52:53], %0\n"
    "s_add_u32 s52, s52, 0x1220000\n"
    "s_addc_u32 s53, s53, 0\n"
    "s_and_b32 s53, s53, 0xffff\n"
    "s_mov_b32 s54, 0x200000\n"
    "s_mov_b32 s55, 0x20000\n"
    "s_lshl_b32 s65, s71, 1\n"
    "s_add_u32 s65, s65, s75\n"
    "s_lshl_b32 s65, s65, 15\n"
    "s_lshl_b32 s66, s72, 12\n"
    "s_add_u32 s65, s65, s66\n"
    "s_lshl_b32 s66, s74, 11\n"
    "s_add_u32 s65, s65, s66\n"
    "buffer_load_dwordx4 v[16:19], v2, s[52:55], s65 offen offset:0 sc1\n"
    "buffer_load_dwordx4 v[20:23], v2, s[52:55], s65 offen offset:1024 sc1\n"
    "s_mov_b32 s66, s65\n"
    "s_add_u32 s92, s86, 0x4b00\n"
    "s_addc_u32 s93, s87, 0\n"
    "s_lshl_b32 s99, s70, 8\n"
    "s_add_u32 s92, s92, s99\n"
    "s_addc_u32 s93, s93, 0\n"
    "s_cmp_lg_u32 s64, 0\n"
    "s_cbranch_scc1 .Lpoll14d\n"
    "s_mov_b32 s94, 0\n"
    ".Lpoll14:\n"
    "s_load_dword s95, s[92:93], 0x0 glc\n"
    "s_waitcnt lgkmcnt(0)\n"
    "s_cmp_ge_u32 s95, 16\n"
    "s_cbranch_scc1 .Lpoll14d\n"
    "s_add_u32 s94, s94, 1\n"
    "s_cmp_gt_u32 s94, 400000\n"
    "s_cbranch_scc1 .Lpoll14d\n"
    "s_sleep 1\n"
    "s_branch .Lpoll14\n"
    ".Lpoll14d:\n"
    "s_barrier\n"
    "s_mov_b64 s[40:41], %0\n"
    "s_add_u32 s40, s40, 0x1a20000\n"
    "s_addc_u32 s41, s41, 0\n"
    "s_mul_i32 s73, s71, 0x38000\n"
    "s_add_u32 s40, s40, s73\n"
    "s_addc_u32 s41, s41, 0\n"
    "s_and_b32 s41, s41, 0xffff\n"
    "s_mov_b32 s42, 0x38000\n"
    "s_mov_b32 s43, 0x20000\n"
    "buffer_load_dwordx4 v[32:35], v2, s[40:43], s77 offen offset:0 sc1\n"
    "buffer_load_dwordx4 v[36:39], v3, s[40:43], s77 offen offset:0 sc1\n"
    "s_add_u32 s77, s77, 0x400\n"
    "buffer_load_dwordx4 v[40:43], v2, s[40:43], s77 offen offset:0 sc1\n"
    "buffer_load_dwordx4 v[44:47], v3, s[40:43], s77 offen offset:0 sc1\n"
    "s_add_u32 s77, s77, 0x400\n"
    "buffer_load_dwordx4 v[48:51], v2, s[40:43], s77 offen offset:0 sc1\n"
    "buffer_load_dwordx4 v[52:55], v3, s[40:43], s77 offen offset:0 sc1\n"
    "s_add_u32 s77, s77, 0x400\n"
    "buffer_load_dwordx4 v[56:59], v2, s[40:43], s77 offen offset:0 sc1\n"
    "buffer_load_dwordx4 v[60:63], v3, s[40:43], s77 offen offset:0 sc1\n"
    "s_add_u32 s77, s77, 0x400\n"
    "buffer_load_dwordx4 v[64:67], v2, s[40:43], s77 offen offset:0 sc1\n"
    "buffer_load_dwordx4 v[68:71], v3, s[40:43], s77 offen offset:0 sc1\n"
    "s_add_u32 s77, s77, 0x400\n"
    "buffer_load_dwordx4 v[72:75], v2, s[40:43], s77 offen offset:0 sc1\n"
    "buffer_load_dwordx4 v[76:79], v3, s[40:43], s77 offen offset:0 sc1\n"
    "s_add_u32 s77, s77, 0x400\n"
    "buffer_load_dwordx4 v[80:83], v2, s[40:43], s77 offen offset:0 sc1\n"
    "buffer_load_dwordx4 v[84:87], v3, s[40:43], s77 offen offset:0 sc1\n"
    "s_add_u32 s77, s77, 0x400\n"
    "buffer_load_dwordx4 v[88:91], v2, s[40:43], s77 offen offset:0 sc1\n"
    "buffer_load_dwordx4 v[92:95], v3, s[40:43], s77 offen offset:0 sc1\n"
    "s_add_u32 s77, s77, 0x400\n"
    "buffer_load_dwordx4 v[192:195], v2, s[40:43], s77 offen offset:0 sc1\n"
    "buffer_load_dwordx4 v[196:199], v3, s[40:43], s77 offen offset:0 sc1\n"
    "s_add_u32 s77, s77, 0x400\n"
    "buffer_load_dwordx4 v[200:203], v2, s[40:43], s77 offen offset:0 sc1\n"
    "buffer_load_dwordx4 v[204:207], v3, s[40:43], s77 offen offset:0 sc1\n"
    "s_add_u32 s77, s77, 0x400\n"
    "buffer_load_dwordx4 v[208:211], v2, s[40:43], s77 offen offset:0 sc1\n"
    "buffer_load_dwordx4 v[212:215], v3, s[40:43], s77 offen offset:0 sc1\n"
    "s_add_u32 s77, s77, 0x400\n"
    "buffer_load_dwordx4 v[216:219], v2, s[40:43], s77 offen offset:0 sc1\n"
    "buffer_load_dwordx4 v[220:223], v3, s[40:43], s77 offen offset:0 sc1\n"
    "s_add_u32 s77, s77, 0x400\n"
    "buffer_load_dwordx4 v[224:227], v2, s[40:43], s77 offen offset:0 sc1\n"
    "buffer_load_dwordx4 v[228:231], v3, s[40:43], s77 offen offset:0 sc1\n"
    "s_add_u32 s77, s77, 0x400\n"
    "buffer_load_dwordx4 v[232:235], v2, s[40:43], s77 offen offset:0 sc1\n"
    "buffer_load_dwordx4 v[236:239], v3, s[40:43], s77 offen offset:0 sc1\n"
    "s_add_u32 s77, s77, 0x400\n"
    "buffer_load_dwordx4 v[240:243], v2, s[40:43], s77 offen offset:0 sc1\n"
    "buffer_load_dwordx4 v[244:247], v3, s[40:43], s77 offen offset:0 sc1\n"
    "s_add_u32 s77, s77, 0x400\n"
    "ds_read_b64_tr_b16 a[128:129], v6 offset:0\n"
    "ds_read_b64_tr_b16 a[130:131], v6 offset:1024\n"
    "ds_read_b64_tr_b16 a[132:133], v6 offset:256\n"
    "ds_read_b64_tr_b16 a[134:135], v6 offset:1280\n"
    "s_waitcnt lgkmcnt(2)\n"
    "s_waitcnt vmcnt(29)\n"
    "v_mfma_f32_32x32x16_bf16 v[128:143], a[128:131], v[32:35], 0\n"
    "ds_read_b64_tr_b16 a[136:137], v6 offset:2048\n"
    "ds_read_b64_tr_b16 a[138:139], v6 offset:3072\n"
    "ds_read_b64_tr_b16 a[140:141], v6 offset:2304\n"
    "ds_read_b64_tr_b16 a[142:143], v6 offset:3328\n"
    "s_waitcnt vmcnt(28)\n"
    "v_mfma_f32_32x32x16_bf16 v[144:159], a[128:131], v[36:39], 0\n"
    "s_waitcnt lgkmcnt(4)\n"
    "v_mfma_f32_32x32x16_bf16 v[160:175], a[132:135], v[32:35], 0\n"
    "v_mfma_f32_32x32x16_bf16 v[176:191], a[132:135], v[36:39], 0\n"
    "s_waitcnt lgkmcnt(2)\n"
    "s_waitcnt vmcnt(27)\n"
    "v_mfma_f32_32x32x16_bf16 v[128:143], a[136:139], v[40:43], v[128:143]\n"
    "ds_read_b64_tr_b16 a[128:129], v6 offset:4096\n"
    "ds_read_b64_tr_b16 a[130:131], v6 offset:5120\n"
    "ds_read_b64_tr_b16 a[132:133], v6 offset:4352\n"
    "ds_read_b64_tr_b16 a[134:135], v6 offset:5376\n"
    "s_add_u32 m0, s67, 0\n"
    "s_waitcnt vmcnt(26)\n"
    "v_mfma_f32_32x32x16_bf16 v[144:159], a[136:139], v[44:47], v[144:159]\n"
    "buffer_load_dwordx4 v4, s[48:51], s68 offen offset:0 lds\n"
    "buffer_load_dwordx4 v5, s[48:51], s68 offen offset:1024 lds\n"
    "s_waitcnt lgkmcnt(4)\n"
    "v_mfma_f32_32x32x16_bf16 v[160:175], a[140:143], v[40:43], v[160:175]\n"
    "buffer_load_dwordx4 v[248:251], v2, s[40:43], s77 offen offset:0 sc1\n"
    "buffer_load_dwordx4 v[252:255], v3, s[40:43], s77 offen offset:0 sc1\n"
    "v_mfma_f32_32x32x16_bf16 v[176:191], a[140:143], v[44:47], v[176:191]\n"
    "s_add_u32 s68, s68, 0x4000\n"
    "s_add_u32 s77, s77, 0x400\n"
    "s_waitcnt lgkmcnt(2)\n"
    "s_waitcnt vmcnt(29)\n"
    "v_mfma_f32_32x32x16_bf16 v[128:143], a[128:131], v[48:51], v[128:143]\n"
    "ds_read_b64_tr_b16 a[136:137], v6 offset:6144\n"
    "ds_read_b64_tr_b16 a[138:139], v6 offset:7168\n"
    "ds_read_b64_tr_b16 a[140:141], v6 offset:6400\n"
    "ds_read_b64_tr_b16 a[142:143], v6 offset:7424\n"
    "s_add_u32 m0, s67, 0x800\n"
    "s_waitcnt vmcnt(28)\n"
    "v_mfma_f32_32x32x16_bf16 v[144:159], a[128:131], v[52:55], v[144:159]\n"
    "buffer_load_dwordx4 v4, s[48:51], s68 offen offset:0 lds\n"
    "buffer_load_dwordx4 v5, s[48:51], s68 offen offset:1024 lds\n"
    "s_waitcnt lgkmcnt(4)\n"
    "v_mfma_f32_32x32x16_bf16 v[160:175], a[132:135], v[48:51], v[160:175]\n"
    "buffer_load_dwordx4 v[32:35], v2, s[40:43], s77 offen offset:0 sc1\n"
    "buffer_load_dwordx4 v[36:39], v3, s[40:43], s77 offen offset:0 sc1\n"
    "v_mfma_f32_32x32x16_bf16 v[176:191], a[132:135], v[52:55], v[176:191]\n"
    "s_add_u32 s68, s68, 0x4000\n"
    "s_add_u32 s77, s77, 0x400\n"
    "s_waitcnt lgkmcnt(2)\n"
    "s_waitcnt vmcnt(31)\n"
    "v_mfma_f32_32x32x16_bf16 v[128:143], a[136:139], v[56:59], v[128:143]\n"
    "ds_read_b64_tr_b16 a[128:129], v25 offset:0\n"
    "ds_read_b64_tr_b16 a[130:131], v25 offset:1024\n"
    "ds_read_b64_tr_b16 a[132:133], v25 offset:256\n"
    "ds_read_b64_tr_b16 a[134:135], v25 offset:1280\n"
    "s_add_u32 m0, s67, 0x1000\n"
    "s_waitcnt vmcnt(30)\n"
    "v_mfma_f32_32x32x16_bf16 v[144:159], a[136:139], v[60:63], v[144:159]\n"
    "buffer_load_dwordx4 v4, s[48:51], s68 offen offset:0 lds\n"
    "buffer_load_dwordx4 v5, s[48:51], s68 offen offset:1024 lds\n"
    "s_waitcnt lgkmcnt(4)\n"
    "v_mfma_f32_32x32x16_bf16 v[160:175], a[140:143], v[56:59], v[160:175]\n"
    "buffer_load_dwordx4 v[40:43], v2, s[40:43], s77 offen offset:0 sc1\n"
    "buffer_load_dwordx4 v[44:47], v3, s[40:43], s77 offen offset:0 sc1\n"
    "v_mfma_f32_32x32x16_bf16 v[176:191], a[140:143], v[60:63], v[176:191]\n"
    "s_add_u32 s68, s68, 0x4000\n"
    "s_add_u32 s77, s77, 0x400\n"
    "s_waitcnt lgkmcnt(2)\n"
    "s_waitcnt vmcnt(33)\n"
    "v_mfma_f32_32x32x16_bf16 v[128:143], a[128:131], v[64:67], v[128:143]\n"
    "ds_read_b64_tr_b16 a[136:137], v25 offset:2048\n"
    "ds_read_b64_tr_b16 a[138:139], v25 offset:3072\n"
    "ds_read_b64_tr_b16 a[140:141], v25 offset:2304\n"
    "ds_read_b64_tr_b16 a[142:143], v25 offset:3328\n"
    "s_add_u32 m0, s67, 0x1800\n"
    "s_waitcnt vmcnt(32)\n"
    "v_mfma_f32_32x32x16_bf16 v[144:159], a[128:131], v[68:71], v[144:159]\n"
    "buffer_load_dwordx4 v4, s[48:51], s68 offen offset:0 lds\n"
    "buffer_load_dwordx4 v5, s[48:51], s68 offen offset:1024 lds\n"
    "s_waitcnt lgkmcnt(4)\n"
    "v_mfma_f32_32x32x16_bf16 v[160:175], a[132:135], v[64:67], v[160:175]\n"
    "buffer_load_dwordx4 v[48:51], v2, s[40:43], s77 offen offset:0 sc1\n"
    "buffer_load_dwordx4 v[52:55], v3, s[40:43], s77 offen offset:0 sc1\n"
    "v_mfma_f32_32x32x16_bf16 v[176:191], a[132:135], v[68:71], v[176:191]\n"
    "s_add_u32 s68, s68, 0x4000\n"
    "s_add_u32 s77, s77, 0x400\n"
    "s_waitcnt lgkmcnt(2)\n"
    "s_waitcnt vmcnt(35)\n"
    "v_mfma_f32_32x32x16_bf16 v[128:143], a[136:139], v[72:75], v[128:143]\n"
    "ds_read_b64_tr_b16 a[128:129], v25 offset:4096\n"
    "ds_read_b64_tr_b16 a[130:131], v25 offset:5120\n"
    "ds_read_b64_tr_b16 a[132:133], v25 offset:4352\n"
    "ds_read_b64_tr_b16 a[134:135], v25 offset:5376\n"
    "s_add_u32 m0, s67, 0x20000\n"
    "s_waitcnt vmcnt(34)\n"
    "v_mfma_f32_32x32x16_bf16 v[144:159], a[136:139], v[76:79], v[144:159]\n"
    "buffer_load_dwordx4 v4, s[48:51], s68 offen offset:0 lds\n"
    "buffer_load_dwordx4 v5, s[48:51], s68 offen offset:1024 lds\n"
    "s_waitcnt lgkmcnt(4)\n"
    "v_mfma_f32_32x32x16_bf16 v[160:175], a[140:143], v[72:75], v[160:175]\n"
    "buffer_load_dwordx4 v[56:59], v2, s[40:43], s77 offen offset:0 sc1\n"
    "buffer_load_dwordx4 v[60:63], v3, s[40:43], s77 offen offset:0 sc1\n"
    "v_mfma_f32_32x32x16_bf16 v[176:191], a[140:143], v[76:79], v[176:191]\n"
    "s_add_u32 s68, s68, 0x4000\n"
    "s_add_u32 s77, s77, 0x400\n"
    "s_waitcnt lgkmcnt(2)\n"
    "s_waitcnt vmcnt(37)\n"
    "v_mfma_f32_32x32x16_bf16 v[128:143], a[128:131], v[80:83], v[128:143]\n"
    "ds_read_b64_tr_b16 a[136:137], v25 offset:6144\n"
    "ds_read_b64_tr_b16 a[138:139], v25 offset:7168\n"
    "ds_read_b64_tr_b16 a[140:141], v25 offset:6400\n"
    "ds_read_b64_tr_b16 a[142:143], v25 offset:7424\n"
    "s_add_u32 m0, s67, 0x20800\n"
    "s_waitcnt vmcnt(36)\n"
    "v_mfma_f32_32x32x16_bf16 v[144:159], a[128:131], v[84:87], v[144:159]\n"
    "buffer_load_dwordx4 v4, s[48:51], s68 offen offset:0 lds\n"
    "buffer_load_dwordx4 v5, s[48:51], s68 offen offset:1024 lds\n"
    "s_waitcnt lgkmcnt(4)\n"
    "v_mfma_f32_32x32x16_bf16 v[160:175], a[132:135], v[80:83], v[160:175]\n"
    "buffer_load_dwordx4 v[64:67], v2, s[40:43], s77 offen offset:0 sc1\n"
    "buffer_load_dwordx4 v[68:71], v3, s[40:43], s77 offen offset:0 sc1\n"
    "v_mfma_f32_32x32x16_bf16 v[176:191], a[132:135], v[84:87], v[176:191]\n"
    "s_add_u32 s68, s68, 0x4000\n"
    "s_add_u32 s77, s77, 0x400\n"
    "s_waitcnt lgkmcnt(2)\n"
    "s_waitcnt vmcnt(39)\n"
    "v_mfma_f32_32x32x16_bf16 v[128:143], a[136:139], v[88:91], v[128:143]\n"
    "ds_read_b64_tr_b16 a[128:129], v7 offset:0\n"
    "ds_read_b64_tr_b16 a[130:131], v7 offset:1024\n"
    "ds_read_b64_tr_b16 a[132:133], v7 offset:256\n"
    "ds_read_b64_tr_b16 a[134:135], v7 offset:1280\n"
    "s_add_u32 m0, s67, 0x21000\n"
    "s_waitcnt vmcnt(38)\n"
    "v_mfma_f32_32x32x16_bf16 v[144:159], a[136:139], v[92:95], v[144:159]\n"
    "buffer_load_dwordx4 v4, s[48:51], s68 offen offset:0 lds\n"
    "buffer_load_dwordx4 v5, s[48:51], s68 offen offset:1024 lds\n"
    "s_waitcnt lgkmcnt(4)\n"
    "v_mfma_f32_32x32x16_bf16 v[160:175], a[140:143], v[88:91], v[160:175]\n"
    "buffer_load_dwordx4 v[72:75], v2, s[40:43], s77 offen offset:0 sc1\n"
    "buffer_load_dwordx4 v[76:79], v3, s[40:43], s77 offen offset:0 sc1\n"
    "v_mfma_f32_32x32x16_bf16 v[176:191], a[140:143], v[92:95], v[176:191]\n"
    "s_add_u32 s68, s68, 0x4000\n"
    "s_add_u32 s77, s77, 0x400\n"
    "s_waitcnt lgkmcnt(2)\n"
    "s_waitcnt vmcnt(41)\n"
    "v_mfma_f32_32x32x16_bf16 v[128:143], a[128:131], v[192:195], v[128:143]\n"
    "ds_read_b64_tr_b16 a[136:137], v7 offset:2048\n"
    "ds_read_b64_tr_b16 a[138:139], v7 offset:3072\n"
    "ds_read_b64_tr_b16 a[140:141], v7 offset:2304\n"
    "ds_read_b64_tr_b16 a[142:143], v7 offset:3328\n"
    "s_add_u32 m0, s67, 0x21800\n"
    "s_waitcnt vmcnt(40)\n"
    "v_mfma_f32_32x32x16_bf16 v[144:159], a[128:131], v[196:199], v[144:159]\n"
    "buffer_load_dwordx4 v4, s[48:51], s68 offen offset:0 lds\n"
    "buffer_load_dwordx4 v5, s[48:51], s68 offen offset:1024 lds\n"
    "s_waitcnt lgkmcnt(4)\n"
    "v_mfma_f32_32x32x16_bf16 v[160:175], a[132:135], v[192:195], v[160:175]\n"
    "buffer_load_dwordx4 v[80:83], v2, s[40:43], s77 offen offset:0 sc1\n"
    "buffer_load_dwordx4 v[84:87], v3, s[40:43], s77 offen offset:0 sc1\n"
    "v_mfma_f32_32x32x16_bf16 v[176:191], a[132:135], v[196:199], v[176:191]\n"
    "s_add_u32 s68, s68, 0x4000\n"
    "s_add_u32 s77, s77, 0x400\n"
    "s_waitcnt lgkmcnt(2)\n"
    "s_waitcnt vmcnt(43)\n"
    "v_mfma_f32_32x32x16_bf16 v[128:143], a[136:139], v[200:203], v[128:143]\n"
    "ds_read_b64_tr_b16 a[128:129], v7 offset:4096\n"
    "ds_read_b64_tr_b16 a[130:131], v7 offset:5120\n"
    "ds_read_b64_tr_b16 a[132:133], v7 offset:4352\n"
    "ds_read_b64_tr_b16 a[134:135], v7 offset:5376\n"
    "s_add_u32 m0, s67, 0x10000\n"
    "s_waitcnt vmcnt(42)\n"
    "v_mfma_f32_32x32x16_bf16 v[144:159], a[136:139], v[204:207], v[144:159]\n"
    "buffer_load_dwordx4 v4, s[48:51], s68 offen offset:0 lds\n"
    "buffer_load_dwordx4 v5, s[48:51], s68 offen offset:1024 lds\n"
    "s_waitcnt lgkmcnt(4)\n"
    "v_mfma_f32_32x32x16_bf16 v[160:175], a[140:143], v[200:203], v[160:175]\n"
    "buffer_load_dwordx4 v[88:91], v2, s[40:43], s77 offen offset:0 sc1\n"
    "buffer_load_dwordx4 v[92:95], v3, s[40:43], s77 offen offset:0 sc1\n"
    "v_mfma_f32_32x32x16_bf16 v[176:191], a[140:143], v[204:207], v[176:191]\n"
    "s_add_u32 s68, s68, 0x4000\n"
    "s_add_u32 s77, s77, 0x400\n"
    "s_waitcnt lgkmcnt(2)\n"
    "s_waitcnt vmcnt(45)\n"
    "v_mfma_f32_32x32x16_bf16 v[128:143], a[128:131], v[208:211], v[128:143]\n"
    "ds_read_b64_tr_b16 a[136:137], v7 offset:6144\n"
    "ds_read_b64_tr_b16 a[138:139], v7 offset:7168\n"
    "ds_read_b64_tr_b16 a[140:141], v7 offset:6400\n"
    "ds_read_b64_tr_b16 a[142:143], v7 offset:7424\n"
    "s_add_u32 m0, s67, 0x10800\n"
    "s_waitcnt vmcnt(44)\n"
    "v_mfma_f32_32x32x16_bf16 v[144:159], a[128:131], v[212:215], v[144:159]\n"
    "buffer_load_dwordx4 v4, s[48:51], s68 offen offset:0 lds\n"
    "buffer_load_dwordx4 v5, s[48:51], s68 offen offset:1024 lds\n"
    "s_waitcnt lgkmcnt(4)\n"
    "v_mfma_f32_32x32x16_bf16 v[160:175], a[132:135], v[208:211], v[160:175]\n"
    "buffer_load_dwordx4 v[192:195], v2, s[40:43], s77 offen offset:0 sc1\n"
    "buffer_load_dwordx4 v[196:199], v3, s[40:43], s77 offen offset:0 sc1\n"
    "v_mfma_f32_32x32x16_bf16 v[176:191], a[132:135], v[212:215], v[176:191]\n"
    "s_add_u32 s68, s68, 0x4000\n"
    "s_add_u32 s77, s77, 0x400\n"
    "s_waitcnt lgkmcnt(2)\n"
    "s_waitcnt vmcnt(47)\n"
    "v_mfma_f32_32x32x16_bf16 v[128:143], a[136:139], v[216:219], v[128:143]\n"
    "ds_read_b64_tr_b16 a[128:129], v6 offset:32768\n"
    "ds_read_b64_tr_b16 a[130:131], v6 offset:33792\n"
    "ds_read_b64_tr_b16 a[132:133], v6 offset:33024\n"
    "ds_read_b64_tr_b16 a[134:135], v6 offset:34048\n"
    "s_add_u32 m0, s67, 0x11000\n"
    "s_waitcnt vmcnt(46)\n"
    "v_mfma_f32_32x32x16_bf16 v[144:159], a[136:139], v[220:223], v[144:159]\n"
    "buffer_load_dwordx4 v4, s[48:51], s68 offen offset:0 lds\n"
    "buffer_load_dwordx4 v5, s[48:51], s68 offen offset:1024 lds\n"
    "s_waitcnt lgkmcnt(4)\n"
    "v_mfma_f32_32x32x16_bf16 v[160:175], a[140:143], v[216:219], v[160:175]\n"
    "buffer_load_dwordx4 v[200:203], v2, s[40:43], s77 offen offset:0 sc1\n"
    "buffer_load_dwordx4 v[204:207], v3, s[40:43], s77 offen offset:0 sc1\n"
    "v_mfma_f32_32x32x16_bf16 v[176:191], a[140:143], v[220:223], v[176:191]\n"
    "s_add_u32 s68, s68, 0x4000\n"
    "s_add_u32 s77, s77, 0x400\n"
    "s_waitcnt lgkmcnt(2)\n"
    "s_waitcnt vmcnt(49)\n"
    "v_mfma_f32_32x32x16_bf16 v[128:143], a[128:131], v[224:227], v[128:143]\n"
    "ds_read_b64_tr_b16 a[136:137], v6 offset:34816\n"
    "ds_read_b64_tr_b16 a[138:139], v6 offset:35840\n"
    "ds_read_b64_tr_b16 a[140:141], v6 offset:35072\n"
    "ds_read_b64_tr_b16 a[142:143], v6 offset:36096\n"
    "s_add_u32 m0, s67, 0x11800\n"
    "s_waitcnt vmcnt(48)\n"
    "v_mfma_f32_32x32x16_bf16 v[144:159], a[128:131], v[228:231], v[144:159]\n"
    "buffer_load_dwordx4 v4, s[48:51], s68 offen offset:0 lds\n"
    "buffer_load_dwordx4 v5, s[48:51], s68 offen offset:1024 lds\n"
    "s_waitcnt lgkmcnt(4)\n"
    "v_mfma_f32_32x32x16_bf16 v[160:175], a[132:135], v[224:227], v[160:175]\n"
    "buffer_load_dwordx4 v[208:211], v2, s[40:43], s77 offen offset:0 sc1\n"
    "buffer_load_dwordx4 v[212:215], v3, s[40:43], s77 offen offset:0 sc1\n"
    "v_mfma_f32_32x32x16_bf16 v[176:191], a[132:135], v[228:231], v[176:191]\n"
    "s_add_u32 s68, s68, 0x4000\n"
    "s_add_u32 s77, s77, 0x400\n"
    "s_waitcnt lgkmcnt(2)\n"
    "s_waitcnt vmcnt(51)\n"
    "v_mfma_f32_32x32x16_bf16 v[128:143], a[136:139], v[232:235], v[128:143]\n"
    "ds_read_b64_tr_b16 a[128:129], v6 offset:36864\n"
    "ds_read_b64_tr_b16 a[130:131], v6 offset:37888\n"
    "ds_read_b64_tr_b16 a[132:133], v6 offset:37120\n"
    "ds_read_b64_tr_b16 a[134:135], v6 offset:38144\n"
    "s_waitcnt vmcnt(50)\n"
    "v_mfma_f32_32x32x16_bf16 v[144:159], a[136:139], v[236:239], v[144:159]\n"
    "s_waitcnt lgkmcnt(4)\n"
    "v_mfma_f32_32x32x16_bf16 v[160:175], a[140:143], v[232:235], v[160:175]\n"
    "buffer_load_dwordx4 v[216:219], v2, s[40:43], s77 offen offset:0 sc1\n"
    "buffer_load_dwordx4 v[220:223], v3, s[40:43], s77 offen offset:0 sc1\n"
    "v_mfma_f32_32x32x16_bf16 v[176:191], a[140:143], v[236:239], v[176:191]\n"
    "s_add_u32 s77, s77, 0x400\n"
    "s_waitcnt lgkmcnt(2)\n"
    "s_waitcnt vmcnt(51)\n"
    "v_mfma_f32_32x32x16_bf16 v[128:143], a[128:131], v[240:243], v[128:143]\n"
    "ds_read_b64_tr_b16 a[136:137], v6 offset:38912\n"
    "ds_read_b64_tr_b16 a[138:139], v6 offset:39936\n"
    "ds_read_b64_tr_b16 a[140:141], v6 offset:39168\n"
    "ds_read_b64_tr_b16 a[142:143], v6 offset:40192\n"
    "s_waitcnt vmcnt(50)\n"
    "v_mfma_f32_32x32x16_bf16 v[144:159], a[128:131], v[244:247], v[144:159]\n"
    "s_waitcnt lgkmcnt(4)\n"
    "v_mfma_f32_32x32x16_bf16 v[160:175], a[132:135], v[240:243], v[160:175]\n"
    "v_mfma_f32_32x32x16_bf16 v[176:191], a[132:135], v[244:247], v[176:191]\n"
    "s_waitcnt lgkmcnt(2)\n"
    "s_waitcnt vmcnt(47)\n"
    "v_mfma_f32_32x32x16_bf16 v[128:143], a[136:139], v[248:251], v[128:143]\n"
    "ds_read_b64_tr_b16 a[128:129], v6 offset:0\n"
    "ds_read_b64_tr_b16 a[130:131], v6 offset:1024\n"
    "ds_read_b64_tr_b16 a[132:133], v6 offset:256\n"
    "ds_read_b64_tr_b16 a[134:135], v6 offset:1280\n"
    "s_waitcnt vmcnt(46)\n"
    "v_mfma_f32_32x32x16_bf16 v[144:159], a[136:139], v[252:255], v[144:159]\n"
    "s_waitcnt lgkmcnt(4)\n"
    "v_mfma_f32_32x32x16_bf16 v[160:175], a[140:143], v[248:251], v[160:175]\n"
    "v_mfma_f32_32x32x16_bf16 v[176:191], a[140:143], v[252:255], v[176:191]\n"
    "s_waitcnt lgkmcnt(2)\n"
    "s_waitcnt vmcnt(43)\n"
    "v_mfma_f32_32x32x16_bf16 v[128:143], a[128:131], v[32:35], v[128:143]\n"
    "ds_read_b64_tr_b16 a[136:137], v6 offset:2048\n"
    "ds_read_b64_tr_b16 a[138:139], v6 offset:3072\n"
    "ds_read_b64_tr_b16 a[140:141], v6 offset:2304\n"
    "ds_read_b64_tr_b16 a[142:143], v6 offset:3328\n"
    "s_waitcnt vmcnt(42)\n"
    "v_mfma_f32_32x32x16_bf16 v[144:159], a[128:131], v[36:39], v[144:159]\n"
    "s_waitcnt lgkmcnt(4)\n"
    "v_mfma_f32_32x32x16_bf16 v[160:175], a[132:135], v[32:35], v[160:175]\n"
    "v_mfma_f32_32x32x16_bf16 v[176:191], a[132:135], v[36:39], v[176:191]\n"
    "s_waitcnt lgkmcnt(2)\n"
    "s_waitcnt vmcnt(39)\n"
    "v_mfma_f32_32x32x16_bf16 v[128:143], a[136:139], v[40:43], v[128:143]\n"
    "ds_read_b64_tr_b16 a[128:129], v6 offset:4096\n"
    "ds_read_b64_tr_b16 a[130:131], v6 offset:5120\n"
    "ds_read_b64_tr_b16 a[132:133], v6 offset:4352\n"
    "ds_read_b64_tr_b16 a[134:135], v6 offset:5376\n"
    "s_waitcnt vmcnt(38)\n"
    "v_mfma_f32_32x32x16_bf16 v[144:159], a[136:139], v[44:47], v[144:159]\n"
    "s_waitcnt lgkmcnt(4)\n"
    "v_mfma_f32_32x32x16_bf16 v[160:175], a[140:143], v[40:43], v[160:175]\n"
    "v_mfma_f32_32x32x16_bf16 v[176:191], a[140:143], v[44:47], v[176:191]\n"
    "s_waitcnt lgkmcnt(2)\n"
    "s_waitcnt vmcnt(35)\n"
    "v_mfma_f32_32x32x16_bf16 v[128:143], a[128:131], v[48:51], v[128:143]\n"
    "ds_read_b64_tr_b16 a[136:137], v6 offset:6144\n"
    "ds_read_b64_tr_b16 a[138:139], v6 offset:7168\n"
    "ds_read_b64_tr_b16 a[140:141], v6 offset:6400\n"
    "ds_read_b64_tr_b16 a[142:143], v6 offset:7424\n"
    "s_waitcnt vmcnt(34)\n"
    "v_mfma_f32_32x32x16_bf16 v[144:159], a[128:131], v[52:55], v[144:159]\n"
    "s_waitcnt lgkmcnt(4)\n"
    "v_mfma_f32_32x32x16_bf16 v[160:175], a[132:135], v[48:51], v[160:175]\n"
    "v_mfma_f32_32x32x16_bf16 v[176:191], a[132:135], v[52:55], v[176:191]\n"
    "s_waitcnt lgkmcnt(2)\n"
    "s_waitcnt vmcnt(31)\n"
    "v_mfma_f32_32x32x16_bf16 v[128:143], a[136:139], v[56:59], v[128:143]\n"
    "ds_read_b64_tr_b16 a[128:129], v25 offset:0\n"
    "ds_read_b64_tr_b16 a[130:131], v25 offset:1024\n"
    "ds_read_b64_tr_b16 a[132:133], v25 offset:256\n"
    "ds_read_b64_tr_b16 a[134:135], v25 offset:1280\n"
    "s_waitcnt vmcnt(30)\n"
    "v_mfma_f32_32x32x16_bf16 v[144:159], a[136:139], v[60:63], v[144:159]\n"
    "s_waitcnt lgkmcnt(4)\n"
    "v_mfma_f32_32x32x16_bf16 v[160:175], a[140:143], v[56:59], v[160:175]\n"
    "v_mfma_f32_32x32x16_bf16 v[176:191], a[140:143], v[60:63], v[176:191]\n"
    "s_waitcnt lgkmcnt(2)\n"
    "s_waitcnt vmcnt(27)\n"
    "v_mfma_f32_32x32x16_bf16 v[128:143], a[128:131], v[64:67], v[128:143]\n"
    "ds_read_b64_tr_b16 a[136:137], v25 offset:2048\n"
    "ds_read_b64_tr_b16 a[138:139], v25 offset:3072\n"
    "ds_read_b64_tr_b16 a[140:141], v25 offset:2304\n"
    "ds_read_b64_tr_b16 a[142:143], v25 offset:3328\n"
    "s_waitcnt vmcnt(26)\n"
    "v_mfma_f32_32x32x16_bf16 v[144:159], a[128:131], v[68:71], v[144:159]\n"
    "s_waitcnt lgkmcnt(4)\n"
    "v_mfma_f32_32x32x16_bf16 v[160:175], a[132:135], v[64:67], v[160:175]\n"
    "v_mfma_f32_32x32x16_bf16 v[176:191], a[132:135], v[68:71], v[176:191]\n"
    "s_waitcnt lgkmcnt(2)\n"
    "s_waitcnt vmcnt(23)\n"
    "v_mfma_f32_32x32x16_bf16 v[128:143], a[136:139], v[72:75], v[128:143]\n"
    "ds_read_b64_tr_b16 a[128:129], v25 offset:4096\n"
    "ds_read_b64_tr_b16 a[130:131], v25 offset:5120\n"
    "ds_read_b64_tr_b16 a[132:133], v25 offset:4352\n"
    "ds_read_b64_tr_b16 a[134:135], v25 offset:5376\n"
    "s_waitcnt vmcnt(22)\n"
    "v_mfma_f32_32x32x16_bf16 v[144:159], a[136:139], v[76:79], v[144:159]\n"
    "s_waitcnt lgkmcnt(4)\n"
    "v_mfma_f32_32x32x16_bf16 v[160:175], a[140:143], v[72:75], v[160:175]\n"
    "v_mfma_f32_32x32x16_bf16 v[176:191], a[140:143], v[76:79], v[176:191]\n"
    "s_waitcnt lgkmcnt(2)\n"
    "s_waitcnt vmcnt(19)\n"
    "v_mfma_f32_32x32x16_bf16 v[128:143], a[128:131], v[80:83], v[128:143]\n"
    "ds_read_b64_tr_b16 a[136:137], v25 offset:6144\n"
    "ds_read_b64_tr_b16 a[138:139], v25 offset:7168\n"
    "ds_read_b64_tr_b16 a[140:141], v25 offset:6400\n"
    "ds_read_b64_tr_b16 a[142:143], v25 offset:7424\n"
    "s_waitcnt vmcnt(18)\n"
    "v_mfma_f32_32x32x16_bf16 v[144:159], a[128:131], v[84:87], v[144:159]\n"
    "s_waitcnt lgkmcnt(4)\n"
    "v_mfma_f32_32x32x16_bf16 v[160:175], a[132:135], v[80:83], v[160:175]\n"
    "v_mfma_f32_32x32x16_bf16 v[176:191], a[132:135], v[84:87], v[176:191]\n"
    "s_waitcnt lgkmcnt(2)\n"
    "s_waitcnt vmcnt(15)\n"
    "v_mfma_f32_32x32x16_bf16 v[128:143], a[136:139], v[88:91], v[128:143]\n"
    "ds_read_b64_tr_b16 a[128:129], v7 offset:0\n"
    "ds_read_b64_tr_b16 a[130:131], v7 offset:1024\n"
    "ds_read_b64_tr_b16 a[132:133], v7 offset:256\n"
    "ds_read_b64_tr_b16 a[134:135], v7 offset:1280\n"
    "s_waitcnt vmcnt(14)\n"
    "v_mfma_f32_32x32x16_bf16 v[144:159], a[136:139], v[92:95], v[144:159]\n"
    "s_waitcnt lgkmcnt(4)\n"
    "v_mfma_f32_32x32x16_bf16 v[160:175], a[140:143], v[88:91], v[160:175]\n"
    "v_mfma_f32_32x32x16_bf16 v[176:191], a[140:143], v[92:95], v[176:191]\n"
    "s_waitcnt lgkmcnt(2)\n"
    "s_waitcnt vmcnt(11)\n"
    "v_mfma_f32_32x32x16_bf16 v[128:143], a[128:131], v[192:195], v[128:143]\n"
    "ds_read_b64_tr_b16 a[136:137], v7 offset:2048\n"
    "ds_read_b64_tr_b16 a[138:139], v7 offset:3072\n"
    "ds_read_b64_tr_b16 a[140:141], v7 offset:2304\n"
    "ds_read_b64_tr_b16 a[142:143], v7 offset:3328\n"
    "s_waitcnt vmcnt(10)\n"
    "v_mfma_f32_32x32x16_bf16 v[144:159], a[128:131], v[196:199], v[144:159]\n"
    "s_waitcnt lgkmcnt(4)\n"
    "v_mfma_f32_32x32x16_bf16 v[160:175], a[132:135], v[192:195], v[160:175]\n"
    "v_mfma_f32_32x32x16_bf16 v[176:191], a[132:135], v[196:199], v[176:191]\n"
    "s_waitcnt lgkmcnt(2)\n"
    "s_waitcnt vmcnt(7)\n"
    "v_mfma_f32_32x32x16_bf16 v[128:143], a[136:139], v[200:203], v[128:143]\n"
    "ds_read_b64_tr_b16 a[128:129], v7 offset:4096\n"
    "ds_read_b64_tr_b16 a[130:131], v7 offset:5120\n"
    "ds_read_b64_tr_b16 a[132:133], v7 offset:4352\n"
    "ds_read_b64_tr_b16 a[134:135], v7 offset:5376\n"
    "s_waitcnt vmcnt(6)\n"
    "v_mfma_f32_32x32x16_bf16 v[144:159], a[136:139], v[204:207], v[144:159]\n"
    "s_waitcnt lgkmcnt(4)\n"
    "v_mfma_f32_32x32x16_bf16 v[160:175], a[140:143], v[200:203], v[160:175]\n"
    "v_mfma_f32_32x32x16_bf16 v[176:191], a[140:143], v[204:207], v[176:191]\n"
    "s_waitcnt lgkmcnt(2)\n"
    "s_waitcnt vmcnt(3)\n"
    "v_mfma_f32_32x32x16_bf16 v[128:143], a[128:131], v[208:211], v[128:143]\n"
    "ds_read_b64_tr_b16 a[136:137], v7 offset:6144\n"
    "ds_read_b64_tr_b16 a[138:139], v7 offset:7168\n"
    "ds_read_b64_tr_b16 a[140:141], v7 offset:6400\n"
    "ds_read_b64_tr_b16 a[142:143], v7 offset:7424\n"
    "s_waitcnt vmcnt(2)\n"
    "v_mfma_f32_32x32x16_bf16 v[144:159], a[128:131], v[212:215], v[144:159]\n"
    "s_waitcnt lgkmcnt(4)\n"
    "v_mfma_f32_32x32x16_bf16 v[160:175], a[132:135], v[208:211], v[160:175]\n"
    "v_mfma_f32_32x32x16_bf16 v[176:191], a[132:135], v[212:215], v[176:191]\n"
    "s_waitcnt lgkmcnt(2)\n"
    "s_waitcnt vmcnt(1)\n"
    "v_mfma_f32_32x32x16_bf16 v[128:143], a[136:139], v[216:219], v[128:143]\n"
    "s_waitcnt vmcnt(0)\n"
    "v_mfma_f32_32x32x16_bf16 v[144:159], a[136:139], v[220:223], v[144:159]\n"
    "s_waitcnt lgkmcnt(0)\n"
    "v_mfma_f32_32x32x16_bf16 v[160:175], a[140:143], v[216:219], v[160:175]\n"
    "v_mfma_f32_32x32x16_bf16 v[176:191], a[140:143], v[220:223], v[176:191]\n"
    "s_nop 7\n"
    "s_nop 7\n"
    "s_barrier\n"
    "v_lshlrev_b32 v11, 4, v1\n"
    "s_lshl_b32 s77, s64, 14\n"
    "v_add_u32 v12, s77, v11\n"
    "ds_write_b128 v12, v[128:131] offset:0\n"
    "ds_write_b128 v12, v[132:135] offset:1024\n"
    "ds_write_b128 v12, v[136:139] offset:2048\n"
    "ds_write_b128 v12, v[140:143] offset:3072\n"
    "ds_write_b128 v12, v[144:147] offset:4096\n"
    "ds_write_b128 v12, v[148:151] offset:5120\n"
    "ds_write_b128 v12, v[152:155] offset:6144\n"
    "ds_write_b128 v12, v[156:159] offset:7168\n"
    "ds_write_b128 v12, v[160:163] offset:8192\n"
    "ds_write_b128 v12, v[164:167] offset:9216\n"
    "ds_write_b128 v12, v[168:171] offset:10240\n"
    "ds_write_b128 v12, v[172:175] offset:11264\n"
    "ds_write_b128 v12, v[176:179] offset:12288\n"
    "ds_write_b128 v12, v[180:183] offset:13312\n"
    "ds_write_b128 v12, v[184:187] offset:14336\n"
    "ds_write_b128 v12, v[188:191] offset:15360\n"
    "s_waitcnt lgkmcnt(0)\n"
    "s_barrier\n"
    "s_lshl_b32 s77, s64, 12\n"
    "v_add_u32 v12, s77, v11\n"
    "ds_read_b128 v[128:131], v12 offset:0\n"
    "ds_read_b128 v[132:135], v12 offset:1024\n"
    "ds_read_b128 v[136:139], v12 offset:2048\n"
    "ds_read_b128 v[140:143], v12 offset:3072\n"
    "ds_read_b128 v[144:147], v12 offset:16384\n"
    "ds_read_b128 v[148:151], v12 offset:17408\n"
    "ds_read_b128 v[152:155], v12 offset:18432\n"
    "ds_read_b128 v[156:159], v12 offset:19456\n"
    "ds_read_b128 v[160:163], v12 offset:32768\n"
    "ds_read_b128 v[164:167], v12 offset:33792\n"
    "ds_read_b128 v[168:171], v12 offset:34816\n"
    "ds_read_b128 v[172:175], v12 offset:35840\n"
    "ds_read_b128 v[176:179], v12 offset:49152\n"
    "ds_read_b128 v[180:183], v12 offset:50176\n"
    "ds_read_b128 v[184:187], v12 offset:51200\n"
    "ds_read_b128 v[188:191], v12 offset:52224\n"
    "s_waitcnt lgkmcnt(11)\n"
    "v_add_f32 v128, v128, v144\n"
    "s_waitcnt lgkmcnt(3)\n"
    "v_add_f32 v160, v160, v176\n"
    "v_add_f32 v129, v129, v145\n"
    "v_add_f32 v161, v161, v177\n"
    "v_add_f32 v130, v130, v146\n"
    "v_add_f32 v162, v162, v178\n"
    "v_add_f32 v131, v131, v147\n"
    "v_add_f32 v163, v163, v179\n"
    "v_add_f32 v132, v132, v148\n"
    "s_waitcnt lgkmcnt(2)\n"
    "v_add_f32 v164, v164, v180\n"
    "v_add_f32 v133, v133, v149\n"
    "v_add_f32 v165, v165, v181\n"
    "v_add_f32 v134, v134, v150\n"
    "v_add_f32 v166, v166, v182\n"
    "v_add_f32 v135, v135, v151\n"
    "v_add_f32 v167, v167, v183\n"
    "v_add_f32 v136, v136, v152\n"
    "s_waitcnt lgkmcnt(1)\n"
    "v_add_f32 v168, v168, v184\n"
    "v_add_f32 v137, v137, v153\n"
    "v_add_f32 v169, v169, v185\n"
    "v_add_f32 v138, v138, v154\n"
    "v_add_f32 v170, v170, v186\n"
    "v_add_f32 v139, v139, v155\n"
    "v_add_f32 v171, v171, v187\n"
    "v_add_f32 v140, v140, v156\n"
    "s_waitcnt lgkmcnt(0)\n"
    "v_add_f32 v172, v172, v188\n"
    "v_add_f32 v141, v141, v157\n"
    "v_add_f32 v173, v173, v189\n"
    "v_add_f32 v142, v142, v158\n"
    "v_add_f32 v174, v174, v190\n"
    "v_add_f32 v143, v143, v159\n"
    "v_add_f32 v175, v175, v191\n"
    "v_add_f32 v128, v128, v160\n"
    "v_add_f32 v129, v129, v161\n"
    "v_add_f32 v130, v130, v162\n"
    "v_add_f32 v131, v131, v163\n"
    "v_add_f32 v132, v132, v164\n"
    "v_add_f32 v133, v133, v165\n"
    "v_add_f32 v134, v134, v166\n"
    "v_add_f32 v135, v135, v167\n"
    "v_add_f32 v136, v136, v168\n"
    "v_add_f32 v137, v137, v169\n"
    "v_add_f32 v138, v138, v170\n"
    "v_add_f32 v139, v139, v171\n"
    "v_add_f32 v140, v140, v172\n"
    "v_add_f32 v141, v141, v173\n"
    "v_add_f32 v142, v142, v174\n"
    "v_add_f32 v143, v143, v175\n"
    "v_lshlrev_b32 v144, 16, v16\n"
    "v_and_b32 v145, 0xffff0000, v16\n"
    "v_lshlrev_b32 v146, 16, v17\n"
    "v_and_b32 v147, 0xffff0000, v17\n"
    "v_lshlrev_b32 v148, 16, v18\n"
    "v_and_b32 v149, 0xffff0000, v18\n"
    "v_lshlrev_b32 v150, 16, v19\n"
    "v_and_b32 v151, 0xffff0000, v19\n"
    "v_lshlrev_b32 v152, 16, v20\n"
    "v_and_b32 v153, 0xffff0000, v20\n"
    "v_lshlrev_b32 v154, 16, v21\n"
    "v_and_b32 v155, 0xffff0000, v21\n"
    "v_lshlrev_b32 v156, 16, v22\n"
    "v_and_b32 v157, 0xffff0000, v22\n"
    "v_lshlrev_b32 v158, 16, v23\n"
    "v_and_b32 v159, 0xffff0000, v23\n"
    "v_cvt_pk_bf16_f32 v160, v128, v129\n"
    "v_cvt_pk_bf16_f32 v161, v130, v131\n"
    "v_cvt_pk_bf16_f32 v162, v132, v133\n"
    "v_cvt_pk_bf16_f32 v163, v134, v135\n"
    "v_cvt_pk_bf16_f32 v164, v136, v137\n"
    "v_cvt_pk_bf16_f32 v165, v138, v139\n"
    "v_cvt_pk_bf16_f32 v166, v140, v141\n"
    "v_cvt_pk_bf16_f32 v167, v142, v143\n"
    "v_lshlrev_b32 v128, 16, v160\n"
    "v_and_b32 v129, 0xffff0000, v160\n"
    "v_lshlrev_b32 v130, 16, v161\n"
    "v_and_b32 v131, 0xffff0000, v161\n"
    "v_lshlrev_b32 v132, 16, v162\n"
    "v_and_b32 v133, 0xffff0000, v162\n"
    "v_lshlrev_b32 v134, 16, v163\n"
    "v_and_b32 v135, 0xffff0000, v163\n"
    "v_lshlrev_b32 v136, 16, v164\n"
    "v_and_b32 v137, 0xffff0000, v164\n"
    "v_lshlrev_b32 v138, 16, v165\n"
    "v_and_b32 v139, 0xffff0000, v165\n"
    "v_lshlrev_b32 v140, 16, v166\n"
    "v_and_b32 v141, 0xffff0000, v166\n"
    "v_lshlrev_b32 v142, 16, v167\n"
    "v_and_b32 v143, 0xffff0000, v167\n"
    "v_fma_f32 v128, v128, v96, v144\n"
    "v_fma_f32 v129, v129, v97, v145\n"
    "v_fma_f32 v130, v130, v98, v146\n"
    "v_fma_f32 v131, v131, v99, v147\n"
    "v_fma_f32 v132, v132, v100, v148\n"
    "v_fma_f32 v133, v133, v101, v149\n"
    "v_fma_f32 v134, v134, v102, v150\n"
    "v_fma_f32 v135, v135, v103, v151\n"
    "v_fma_f32 v136, v136, v104, v152\n"
    "v_fma_f32 v137, v137, v105, v153\n"
    "v_fma_f32 v138, v138, v106, v154\n"
    "v_fma_f32 v139, v139, v107, v155\n"
    "v_fma_f32 v140, v140, v108, v156\n"
    "v_fma_f32 v141, v141, v109, v157\n"
    "v_fma_f32 v142, v142, v110, v158\n"
    "v_fma_f32 v143, v143, v111, v159\n"
    "v_cvt_pk_bf16_f32 v160, v128, v129\n"
    "v_cvt_pk_bf16_f32 v161, v130, v131\n"
    "v_cvt_pk_bf16_f32 v162, v132, v133\n"
    "v_cvt_pk_bf16_f32 v163, v134, v135\n"
    "v_cvt_pk_bf16_f32 v164, v136, v137\n"
    "v_cvt_pk_bf16_f32 v165, v138, v139\n"
    "v_cvt_pk_bf16_f32 v166, v140, v141\n"
    "v_cvt_pk_bf16_f32 v167, v142, v143\n"
    "s_mov_b64 s[60:61], %9\n"
    "s_and_b32 s61, s61, 0xffff\n"
    "s_mov_b32 s62, 0x200000\n"
    "s_mov_b32 s63, 0x20000\n"
    "s_nop 1\n"
    "v_permlane32_swap_b32 v160, v162\n"
    "v_permlane32_swap_b32 v161, v163\n"
    "v_permlane32_swap_b32 v164, v166\n"
    "v_permlane32_swap_b32 v165, v167\n"
    "v_and_b32 v9, 31, v1\n"
    "v_lshlrev_b32 v9, 10, v9\n"
    "v_lshrrev_b32 v10, 5, v1\n"
    "v_lshl_add_u32 v9, v10, 4, v9\n"
    "s_lshl_b32 s65, s71, 16\n"
    "s_lshl_b32 s66, s75, 15\n"
    "s_add_u32 s65, s65, s66\n"
    "s_lshl_b32 s66, s72, 7\n"
    "s_add_u32 s65, s65, s66\n"
    "s_lshl_b32 s66, s74, 6\n"
    "s_add_u32 s65, s65, s66\n"
    "buffer_store_dwordx4 v[160:163], v9, s[60:63], s65 offen offset:0\n"
    "buffer_store_dwordx4 v[164:167], v9, s[60:63], s65 offen offset:32\n"
    "s_cmp_lg_u32 s98, 0\n"
    "s_cbranch_scc1 .Lep15x\n"
    "s_cmp_lg_u32 s64, 0\n"
    "s_cbranch_scc1 .Lep15x\n"
    "v_lshrrev_b32 v116, 5, v1\n"
    "v_lshlrev_b32 v116, 8, v116\n"
    "v_and_b32 v117, 31, v1\n"
    "v_lshl_add_u32 v116, v117, 2, v116\n"
    "v_add_u32 v116, 0x100, v116\n"
    "v_add_u32 v117, 0x200, v116\n"
    "v_add_u32 v118, 0x400, v116\n"
    "v_add_u32 v119, 0x600, v116\n"
    "s_mov_b32 s94, 0\n"
    ".Lep15:\n"
    "global_load_dword v120, v116, s[86:87] sc1\n"
    "global_load_dword v121, v117, s[86:87] sc1\n"
    "global_load_dword v122, v118, s[86:87] sc1\n"
    "global_load_dword v123, v119, s[86:87] sc1\n"
    "s_waitcnt vmcnt(0)\n"
    "v_cmp_eq_u32 vcc, s88, v120\n"
    "v_cmp_eq_u32 s[84:85], s88, v121\n"
    "s_nop 1\n"
    "s_and_b64 vcc, vcc, s[84:85]\n"
    "v_cmp_eq_u32 s[84:85], s88, v122\n"
    "s_nop 1\n"
    "s_and_b64 vcc, vcc, s[84:85]\n"
    "v_cmp_eq_u32 s[84:85], s88, v123\n"
    "s_nop 1\n"
    "s_and_b64 vcc, vcc, s[84:85]\n"
    "s_nop 3\n"
    "s_cmp_eq_u64 vcc, -1\n"
    "s_cbranch_scc1 .Lep15d\n"
    "s_add_u32 s94, s94, 1\n"
    "s_cmp_gt_u32 s94, 400000\n"
    "s_cbranch_scc1 .Lep15d\n"
    "s_sleep 1\n"
    "s_branch .Lep15\n"
    ".Lep15d:\n"
    "v_mov_b32 v120, 0\n"
    "v_mov_b32 v121, s88\n"
    "s_mov_b64 exec, 1\n"
    "global_store_dword v120, v121, s[86:87] sc1\n"
    "s_mov_b64 exec, -1\n"
    ".Lep15x:\n"
    "s_endpgm\n"
    ".Lfb_stub0:\n"
    "s_getpc_b64 s[92:93]\n"
    ".Lfb_pc0:\n"
    "s_add_u32 s92, s92, .Lfb_body0-.Lfb_pc0\n"
    "s_addc_u32 s93, s93, 0\n"
    "s_swappc_b64 s[94:95], s[92:93]\n"
    "s_branch .Lfb_ret0\n"
    ".Lfb_stub1:\n"
    "s_getpc_b64 s[92:93]\n"
    ".Lfb_pc1:\n"
    "s_add_u32 s92, s92, .Lfb_body1-.Lfb_pc1\n"
    "s_addc_u32 s93, s93, 0\n"
    "s_swappc_b64 s[94:95], s[92:93]\n"
    "s_branch .Lfb_ret1\n"
    ".Lfb_stub2:\n"
    "s_getpc_b64 s[92:93]\n"
    ".Lfb_pc2:\n"
    "s_add_u32 s92, s92, .Lfb_body0-.Lfb_pc2\n"
    "s_addc_u32 s93, s93, 0\n"
    "s_swappc_b64 s[94:95], s[92:93]\n"
    "s_branch .Lfb_ret2\n"
    ".Lfb_stub3:\n"
    "s_getpc_b64 s[92:93]\n"
    ".Lfb_pc3:\n"
    "s_add_u32 s92, s92, .Lfb_body1-.Lfb_pc3\n"
    "s_addc_u32 s93, s93, 0\n"
    "s_swappc_b64 s[94:95], s[92:93]\n"
    "s_branch .Lfb_ret3\n"
    ".Lfb_stub4:\n"
    "s_getpc_b64 s[92:93]\n"
    ".Lfb_pc4:\n"
    "s_add_u32 s92, s92, .Lfb_body0-.Lfb_pc4\n"
    "s_addc_u32 s93, s93, 0\n"
    "s_swappc_b64 s[94:95], s[92:93]\n"
    "s_branch .Lfb_ret4\n"
    ".Lfb_stub5:\n"
    "s_getpc_b64 s[92:93]\n"
    ".Lfb_pc5:\n"
    "s_add_u32 s92, s92, .Lfb_body1-.Lfb_pc5\n"
    "s_addc_u32 s93, s93, 0\n"
    "s_swappc_b64 s[94:95], s[92:93]\n"
    "s_branch .Lfb_ret5\n"
    ".Lfb_stub6:\n"
    "s_getpc_b64 s[92:93]\n"
    ".Lfb_pc6:\n"
    "s_add_u32 s92, s92, .Lfb_body0-.Lfb_pc6\n"
    "s_addc_u32 s93, s93, 0\n"
    "s_swappc_b64 s[94:95], s[92:93]\n"
    "s_branch .Lfb_ret6\n"
    ".Lfb_stub7:\n"
    "s_getpc_b64 s[92:93]\n"
    ".Lfb_pc7:\n"
    "s_add_u32 s92, s92, .Lfb_body1-.Lfb_pc7\n"
    "s_addc_u32 s93, s93, 0\n"
    "s_swappc_b64 s[94:95], s[92:93]\n"
    "s_branch .Lfb_ret7\n"
    ".Lfb_stub8:\n"
    "s_getpc_b64 s[92:93]\n"
    ".Lfb_pc8:\n"
    "s_add_u32 s92, s92, .Lfb_body0-.Lfb_pc8\n"
    "s_addc_u32 s93, s93, 0\n"
    "s_swappc_b64 s[94:95], s[92:93]\n"
    "s_branch .Lfb_ret8\n"
    ".Lfb_stub9:\n"
    "s_getpc_b64 s[92:93]\n"
    ".Lfb_pc9:\n"
    "s_add_u32 s92, s92, .Lfb_body1-.Lfb_pc9\n"
    "s_addc_u32 s93, s93, 0\n"
    "s_swappc_b64 s[94:95], s[92:93]\n"
    "s_branch .Lfb_ret9\n"
    ".Lfb_stub10:\n"
    "s_getpc_b64 s[92:93]\n"
    ".Lfb_pc10:\n"
    "s_add_u32 s92, s92, .Lfb_body0-.Lfb_pc10\n"
    "s_addc_u32 s93, s93, 0\n"
    "s_swappc_b64 s[94:95], s[92:93]\n"
    "s_branch .Lfb_ret10\n"
    ".Lfb_stub11:\n"
    "s_getpc_b64 s[92:93]\n"
    ".Lfb_pc11:\n"
    "s_add_u32 s92, s92, .Lfb_body1-.Lfb_pc11\n"
    "s_addc_u32 s93, s93, 0\n"
    "s_swappc_b64 s[94:95], s[92:93]\n"
    "s_branch .Lfb_ret11\n"
    ".Lfb_stub12:\n"
    "s_getpc_b64 s[92:93]\n"
    ".Lfb_pc12:\n"
    "s_add_u32 s92, s92, .Lfb_body0-.Lfb_pc12\n"
    "s_addc_u32 s93, s93, 0\n"
    "s_swappc_b64 s[94:95], s[92:93]\n"
    "s_branch .Lfb_ret12\n"
    ".Lfb_stub13:\n"
    "s_getpc_b64 s[92:93]\n"
    ".Lfb_pc13:\n"
    "s_add_u32 s92, s92, .Lfb_body1-.Lfb_pc13\n"
    "s_addc_u32 s93, s93, 0\n"
    "s_swappc_b64 s[94:95], s[92:93]\n"
    "s_branch .Lfb_ret13\n"
    ".Lfb_stub14:\n"
    "s_getpc_b64 s[92:93]\n"
    ".Lfb_pc14:\n"
    "s_add_u32 s92, s92, .Lfb_body0-.Lfb_pc14\n"
    "s_addc_u32 s93, s93, 0\n"
    "s_swappc_b64 s[94:95], s[92:93]\n"
    "s_branch .Lfb_ret14\n"
    ".Lfb_stub15:\n"
    "s_getpc_b64 s[92:93]\n"
    ".Lfb_pc15:\n"
    "s_add_u32 s92, s92, .Lfb_body1-.Lfb_pc15\n"
    "s_addc_u32 s93, s93, 0\n"
    "s_swappc_b64 s[94:95], s[92:93]\n"
    "s_branch .Lfb_ret15\n"
    ".Lfb_body0:\n"
    "s_nop 15\n"
    "s_nop 3\n"
    "v_max3_f32 v48, v128, v129, v130\n"
    "v_max3_f32 v49, v131, v132, v133\n"
    "v_max3_f32 v48, v48, v134, v135\n"
    "v_max3_f32 v49, v49, v136, v137\n"
    "v_max3_f32 v48, v48, v138, v139\n"
    "v_max3_f32 v49, v49, v140, v141\n"
    "v_max3_f32 v48, v48, v142, v143\n"
    "v_max_f32 v48, v48, v49\n"
    "v_mov_b32 v49, v48\n"
    "s_nop 1\n"
    "v_permlane32_swap_b32 v48, v49\n"
    "v_max_f32 v48, v48, v49\n"
    "v_ceil_f32 v50, v48\n"
    "v_add_f32 v50, 0x42cc0000, v50\n"
    "v_max_f32 v50, 0, v50\n"
    "v_cvt_i32_f32 v51, v50\n"
    "v_sub_u32 v51, 0, v51\n"
    "v_accvgpr_read_b32 v52, a0\n"
    "v_accvgpr_read_b32 v53, a1\n"
    "v_accvgpr_read_b32 v54, a2\n"
    "v_accvgpr_read_b32 v55, a3\n"
    "v_ldexp_f32 v52, v52, v51\n"
    "v_ldexp_f32 v53, v53, v51\n"
    "v_ldexp_f32 v54, v54, v51\n"
    "v_ldexp_f32 v55, v55, v51\n"
    "v_accvgpr_write_b32 a0, v52\n"
    "v_accvgpr_write_b32 a1, v53\n"
    "v_accvgpr_write_b32 a2, v54\n"
    "v_accvgpr_write_b32 a3, v55\n"
    "v_accvgpr_read_b32 v52, a4\n"
    "v_accvgpr_read_b32 v53, a5\n"
    "v_accvgpr_read_b32 v54, a6\n"
    "v_accvgpr_read_b32 v55, a7\n"
    "v_ldexp_f32 v52, v52, v51\n"
    "v_ldexp_f32 v53, v53, v51\n"
    "v_ldexp_f32 v54, v54, v51\n"
    "v_ldexp_f32 v55, v55, v51\n"
    "v_accvgpr_write_b32 a4, v52\n"
    "v_accvgpr_write_b32 a5, v53\n"
    "v_accvgpr_write_b32 a6, v54\n"
    "v_accvgpr_write_b32 a7, v55\n"
    "v_accvgpr_read_b32 v52, a8\n"
    "v_accvgpr_read_b32 v53, a9\n"
    "v_accvgpr_read_b32 v54, a10\n"
    "v_accvgpr_read_b32 v55, a11\n"
    "v_ldexp_f32 v52, v52, v51\n"
    "v_ldexp_f32 v53, v53, v51\n"
    "v_ldexp_f32 v54, v54, v51\n"
    "v_ldexp_f32 v55, v55, v51\n"
    "v_accvgpr_write_b32 a8, v52\n"
    "v_accvgpr_write_b32 a9, v53\n"
    "v_accvgpr_write_b32 a10, v54\n"
    "v_accvgpr_write_b32 a11, v55\n"
    "v_accvgpr_read_b32 v52, a12\n"
    "v_accvgpr_read_b32 v53, a13\n"
    "v_accvgpr_read_b32 v54, a14\n"
    "v_accvgpr_read_b32 v55, a15\n"
    "v_ldexp_f32 v52, v52, v51\n"
    "v_ldexp_f32 v53, v53, v51\n"
    "v_ldexp_f32 v54, v54, v51\n"
    "v_ldexp_f32 v55, v55, v51\n"
    "v_accvgpr_write_b32 a12, v52\n"
    "v_accvgpr_write_b32 a13, v53\n"
    "v_accvgpr_write_b32 a14, v54\n"
    "v_accvgpr_write_b32 a15, v55\n"
    "v_accvgpr_read_b32 v52, a32\n"
    "v_accvgpr_read_b32 v53, a33\n"
    "v_accvgpr_read_b32 v54, a34\n"
    "v_accvgpr_read_b32 v55, a35\n"
    "v_ldexp_f32 v52, v52, v51\n"
    "v_ldexp_f32 v53, v53, v51\n"
    "v_ldexp_f32 v54, v54, v51\n"
    "v_ldexp_f32 v55, v55, v51\n"
    "v_accvgpr_write_b32 a32, v52\n"
    "v_accvgpr_write_b32 a33, v53\n"
    "v_accvgpr_write_b32 a34, v54\n"
    "v_accvgpr_write_b32 a35, v55\n"
    "v_accvgpr_read_b32 v52, a36\n"
    "v_accvgpr_read_b32 v53, a37\n"
    "v_accvgpr_read_b32 v54, a38\n"
    "v_accvgpr_read_b32 v55, a39\n"
    "v_ldexp_f32 v52, v52, v51\n"
    "v_ldexp_f32 v53, v53, v51\n"
    "v_ldexp_f32 v54, v54, v51\n"
    "v_ldexp_f32 v55, v55, v51\n"
    "v_accvgpr_write_b32 a36, v52\n"
    "v_accvgpr_write_b32 a37, v53\n"
    "v_accvgpr_write_b32 a38, v54\n"
    "v_accvgpr_write_b32 a39, v55\n"
    "v_accvgpr_read_b32 v52, a40\n"
    "v_accvgpr_read_b32 v53, a41\n"
    "v_accvgpr_read_b32 v54, a42\n"
    "v_accvgpr_read_b32 v55, a43\n"
    "v_ldexp_f32 v52, v52, v51\n"
    "v_ldexp_f32 v53, v53, v51\n"
    "v_ldexp_f32 v54, v54, v51\n"
    "v_ldexp_f32 v55, v55, v51\n"
    "v_accvgpr_write_b32 a40, v52\n"
    "v_accvgpr_write_b32 a41, v53\n"
    "v_accvgpr_write_b32 a42, v54\n"
    "v_accvgpr_write_b32 a43, v55\n"
    "v_accvgpr_read_b32 v52, a44\n"
    "v_accvgpr_read_b32 v53, a45\n"
    "v_accvgpr_read_b32 v54, a46\n"
    "v_accvgpr_read_b32 v55, a47\n"
    "v_ldexp_f32 v52, v52, v51\n"
    "v_ldexp_f32 v53, v53, v51\n"
    "v_ldexp_f32 v54, v54, v51\n"
    "v_ldexp_f32 v55, v55, v51\n"
    "v_accvgpr_write_b32 a44, v52\n"
    "v_accvgpr_write_b32 a45, v53\n"
    "v_accvgpr_write_b32 a46, v54\n"
    "v_accvgpr_write_b32 a47, v55\n"
    "v_accvgpr_read_b32 v52, a64\n"
    "v_accvgpr_read_b32 v53, a65\n"
    "v_accvgpr_read_b32 v54, a66\n"
    "v_accvgpr_read_b32 v55, a67\n"
    "v_ldexp_f32 v52, v52, v51\n"
    "v_ldexp_f32 v53, v53, v51\n"
    "v_ldexp_f32 v54, v54, v51\n"
    "v_ldexp_f32 v55, v55, v51\n"
    "v_accvgpr_write_b32 a64, v52\n"
    "v_accvgpr_write_b32 a65, v53\n"
    "v_accvgpr_write_b32 a66, v54\n"
    "v_accvgpr_write_b32 a67, v55\n"
    "v_accvgpr_read_b32 v52, a68\n"
    "v_accvgpr_read_b32 v53, a69\n"
    "v_accvgpr_read_b32 v54, a70\n"
    "v_accvgpr_read_b32 v55, a71\n"
    "v_ldexp_f32 v52, v52, v51\n"
    "v_ldexp_f32 v53, v53, v51\n"
    "v_ldexp_f32 v54, v54, v51\n"
    "v_ldexp_f32 v55, v55, v51\n"
    "v_accvgpr_write_b32 a68, v52\n"
    "v_accvgpr_write_b32 a69, v53\n"
    "v_accvgpr_write_b32 a70, v54\n"
    "v_accvgpr_write_b32 a71, v55\n"
    "v_accvgpr_read_b32 v52, a72\n"
    "v_accvgpr_read_b32 v53, a73\n"
    "v_accvgpr_read_b32 v54, a74\n"
    "v_accvgpr_read_b32 v55, a75\n"
    "v_ldexp_f32 v52, v52, v51\n"
    "v_ldexp_f32 v53, v53, v51\n"
    "v_ldexp_f32 v54, v54, v51\n"
    "v_ldexp_f32 v55, v55, v51\n"
    "v_accvgpr_write_b32 a72, v52\n"
    "v_accvgpr_write_b32 a73, v53\n"
    "v_accvgpr_write_b32 a74, v54\n"
    "v_accvgpr_write_b32 a75, v55\n"
    "v_accvgpr_read_b32 v52, a76\n"
    "v_accvgpr_read_b32 v53, a77\n"
    "v_accvgpr_read_b32 v54, a78\n"
    "v_accvgpr_read_b32 v55, a79\n"
    "v_ldexp_f32 v52, v52, v51\n"
    "v_ldexp_f32 v53, v53, v51\n"
    "v_ldexp_f32 v54, v54, v51\n"
    "v_ldexp_f32 v55, v55, v51\n"
    "v_accvgpr_write_b32 a76, v52\n"
    "v_accvgpr_write_b32 a77, v53\n"
    "v_accvgpr_write_b32 a78, v54\n"
    "v_accvgpr_write_b32 a79, v55\n"
    "v_accvgpr_read_b32 v52, a96\n"
    "v_accvgpr_read_b32 v53, a97\n"
    "v_accvgpr_read_b32 v54, a98\n"
    "v_accvgpr_read_b32 v55, a99\n"
    "v_ldexp_f32 v52, v52, v51\n"
    "v_ldexp_f32 v53, v53, v51\n"
    "v_ldexp_f32 v54, v54, v51\n"
    "v_ldexp_f32 v55, v55, v51\n"
    "v_accvgpr_write_b32 a96, v52\n"
    "v_accvgpr_write_b32 a97, v53\n"
    "v_accvgpr_write_b32 a98, v54\n"
    "v_accvgpr_write_b32 a99, v55\n"
    "v_accvgpr_read_b32 v52, a100\n"
    "v_accvgpr_read_b32 v53, a101\n"
    "v_accvgpr_read_b32 v54, a102\n"
    "v_accvgpr_read_b32 v55, a103\n"
    "v_ldexp_f32 v52, v52, v51\n"
    "v_ldexp_f32 v53, v53, v51\n"
    "v_ldexp_f32 v54, v54, v51\n"
    "v_ldexp_f32 v55, v55, v51\n"
    "v_accvgpr_write_b32 a100, v52\n"
    "v_accvgpr_write_b32 a101, v53\n"
    "v_accvgpr_write_b32 a102, v54\n"
    "v_accvgpr_write_b32 a103, v55\n"
    "v_accvgpr_read_b32 v52, a104\n"
    "v_accvgpr_read_b32 v53, a105\n"
    "v_accvgpr_read_b32 v54, a106\n"
    "v_accvgpr_read_b32 v55, a107\n"
    "v_ldexp_f32 v52, v52, v51\n"
    "v_ldexp_f32 v53, v53, v51\n"
    "v_ldexp_f32 v54, v54, v51\n"
    "v_ldexp_f32 v55, v55, v51\n"
    "v_accvgpr_write_b32 a104, v52\n"
    "v_accvgpr_write_b32 a105, v53\n"
    "v_accvgpr_write_b32 a106, v54\n"
    "v_accvgpr_write_b32 a107, v55\n"
    "v_accvgpr_read_b32 v52, a108\n"
    "v_accvgpr_read_b32 v53, a109\n"
    "v_accvgpr_read_b32 v54, a110\n"
    "v_accvgpr_read_b32 v55, a111\n"
    "v_ldexp_f32 v52, v52, v51\n"
    "v_ldexp_f32 v53, v53, v51\n"
    "v_ldexp_f32 v54, v54, v51\n"
    "v_ldexp_f32 v55, v55, v51\n"
    "v_accvgpr_write_b32 a108, v52\n"
    "v_accvgpr_write_b32 a109, v53\n"
    "v_accvgpr_write_b32 a110, v54\n"
    "v_accvgpr_write_b32 a111, v55\n"
    "v_ldexp_f32 v16, v16, v51\n"
    "v_sub_f32 v128, v128, v50\n"
    "v_sub_f32 v160, v160, v50\n"
    "v_sub_f32 v224, v224, v50\n"
    "v_sub_f32 v129, v129, v50\n"
    "v_sub_f32 v161, v161, v50\n"
    "v_sub_f32 v225, v225, v50\n"
    "v_sub_f32 v130, v130, v50\n"
    "v_sub_f32 v162, v162, v50\n"
    "v_sub_f32 v226, v226, v50\n"
    "v_sub_f32 v131, v131, v50\n"
    "v_sub_f32 v163, v163, v50\n"
    "v_sub_f32 v227, v227, v50\n"
    "v_sub_f32 v132, v132, v50\n"
    "v_sub_f32 v164, v164, v50\n"
    "v_sub_f32 v228, v228, v50\n"
    "v_sub_f32 v133, v133, v50\n"
    "v_sub_f32 v165, v165, v50\n"
    "v_sub_f32 v229, v229, v50\n"
    "v_sub_f32 v134, v134, v50\n"
    "v_sub_f32 v166, v166, v50\n"
    "v_sub_f32 v230, v230, v50\n"
    "v_sub_f32 v135, v135, v50\n"
    "v_sub_f32 v167, v167, v50\n"
    "v_sub_f32 v231, v231, v50\n"
    "v_sub_f32 v136, v136, v50\n"
    "v_sub_f32 v168, v168, v50\n"
    "v_sub_f32 v232, v232, v50\n"
    "v_sub_f32 v137, v137, v50\n"
    "v_sub_f32 v169, v169, v50\n"
    "v_sub_f32 v233, v233, v50\n"
    "v_sub_f32 v138, v138, v50\n"
    "v_sub_f32 v170, v170, v50\n"
    "v_sub_f32 v234, v234, v50\n"
    "v_sub_f32 v139, v139, v50\n"
    "v_sub_f32 v171, v171, v50\n"
    "v_sub_f32 v235, v235, v50\n"
    "v_sub_f32 v140, v140, v50\n"
    "v_sub_f32 v172, v172, v50\n"
    "v_sub_f32 v236, v236, v50\n"
    "v_sub_f32 v141, v141, v50\n"
    "v_sub_f32 v173, v173, v50\n"
    "v_sub_f32 v237, v237, v50\n"
    "v_sub_f32 v142, v142, v50\n"
    "v_sub_f32 v174, v174, v50\n"
    "v_sub_f32 v238, v238, v50\n"
    "v_sub_f32 v143, v143, v50\n"
    "v_sub_f32 v175, v175, v50\n"
    "v_sub_f32 v239, v239, v50\n"
    "v_exp_f32 v32, v128\n"
    "v_exp_f32 v33, v129\n"
    "v_exp_f32 v34, v130\n"
    "v_exp_f32 v35, v131\n"
    "v_exp_f32 v36, v132\n"
    "v_exp_f32 v37, v133\n"
    "v_exp_f32 v38, v134\n"
    "v_exp_f32 v39, v135\n"
    "v_exp_f32 v40, v136\n"
    "v_exp_f32 v41, v137\n"
    "v_exp_f32 v42, v138\n"
    "v_exp_f32 v43, v139\n"
    "v_exp_f32 v44, v140\n"
    "v_exp_f32 v45, v141\n"
    "v_exp_f32 v46, v142\n"
    "v_exp_f32 v47, v143\n"
    "s_nop 0\n"
    "v_add_f32 v18, v32, v33\n"
    "v_add_f32 v18, v18, v34\n"
    "v_add_f32 v18, v18, v35\n"
    "v_add_f32 v18, v18, v36\n"
    "v_add_f32 v18, v18, v37\n"
    "v_add_f32 v18, v18, v38\n"
    "v_add_f32 v18, v18, v39\n"
    "v_add_f32 v18, v18, v40\n"
    "v_add_f32 v18, v18, v41\n"
    "v_add_f32 v18, v18, v42\n"
    "v_add_f32 v18, v18, v43\n"
    "v_add_f32 v18, v18, v44\n"
    "v_add_f32 v18, v18, v45\n"
    "v_add_f32 v18, v18, v46\n"
    "v_add_f32 v18, v18, v47\n"
    "v_cvt_pk_bf16_f32 v192, v32, v33\n"
    "v_cvt_pk_bf16_f32 v193, v34, v35\n"
    "v_cvt_pk_bf16_f32 v194, v36, v37\n"
    "v_cvt_pk_bf16_f32 v195, v38, v39\n"
    "v_cvt_pk_bf16_f32 v196, v40, v41\n"
    "v_cvt_pk_bf16_f32 v197, v42, v43\n"
    "v_cvt_pk_bf16_f32 v198, v44, v45\n"
    "v_cvt_pk_bf16_f32 v199, v46, v47\n"
    "v_max3_f32 v48, v144, v145, v146\n"
    "v_max3_f32 v49, v147, v148, v149\n"
    "v_max3_f32 v48, v48, v150, v151\n"
    "v_max3_f32 v49, v49, v152, v153\n"
    "v_max3_f32 v48, v48, v154, v155\n"
    "v_max3_f32 v49, v49, v156, v157\n"
    "v_max3_f32 v48, v48, v158, v159\n"
    "v_max_f32 v48, v48, v49\n"
    "v_mov_b32 v49, v48\n"
    "s_nop 1\n"
    "v_permlane32_swap_b32 v48, v49\n"
    "v_max_f32 v48, v48, v49\n"
    "v_ceil_f32 v50, v48\n"
    "v_add_f32 v50, 0x42cc0000, v50\n"
    "v_max_f32 v50, 0, v50\n"
    "v_cvt_i32_f32 v51, v50\n"
    "v_sub_u32 v51, 0, v51\n"
    "v_accvgpr_read_b32 v52, a16\n"
    "v_accvgpr_read_b32 v53, a17\n"
    "v_accvgpr_read_b32 v54, a18\n"
    "v_accvgpr_read_b32 v55, a19\n"
    "v_ldexp_f32 v52, v52, v51\n"
    "v_ldexp_f32 v53, v53, v51\n"
    "v_ldexp_f32 v54, v54, v51\n"
    "v_ldexp_f32 v55, v55, v51\n"
    "v_accvgpr_write_b32 a16, v52\n"
    "v_accvgpr_write_b32 a17, v53\n"
    "v_accvgpr_write_b32 a18, v54\n"
    "v_accvgpr_write_b32 a19, v55\n"
    "v_accvgpr_read_b32 v52, a20\n"
    "v_accvgpr_read_b32 v53, a21\n"
    "v_accvgpr_read_b32 v54, a22\n"
    "v_accvgpr_read_b32 v55, a23\n"
    "v_ldexp_f32 v52, v52, v51\n"
    "v_ldexp_f32 v53, v53, v51\n"
    "v_ldexp_f32 v54, v54, v51\n"
    "v_ldexp_f32 v55, v55, v51\n"
    "v_accvgpr_write_b32 a20, v52\n"
    "v_accvgpr_write_b32 a21, v53\n"
    "v_accvgpr_write_b32 a22, v54\n"
    "v_accvgpr_write_b32 a23, v55\n"
    "v_accvgpr_read_b32 v52, a24\n"
    "v_accvgpr_read_b32 v53, a25\n"
    "v_accvgpr_read_b32 v54, a26\n"
    "v_accvgpr_read_b32 v55, a27\n"
    "v_ldexp_f32 v52, v52, v51\n"
    "v_ldexp_f32 v53, v53, v51\n"
    "v_ldexp_f32 v54, v54, v51\n"
    "v_ldexp_f32 v55, v55, v51\n"
    "v_accvgpr_write_b32 a24, v52\n"
    "v_accvgpr_write_b32 a25, v53\n"
    "v_accvgpr_write_b32 a26, v54\n"
    "v_accvgpr_write_b32 a27, v55\n"
    "v_accvgpr_read_b32 v52, a28\n"
    "v_accvgpr_read_b32 v53, a29\n"
    "v_accvgpr_read_b32 v54, a30\n"
    "v_accvgpr_read_b32 v55, a31\n"
    "v_ldexp_f32 v52, v52, v51\n"
    "v_ldexp_f32 v53, v53, v51\n"
    "v_ldexp_f32 v54, v54, v51\n"
    "v_ldexp_f32 v55, v55, v51\n"
    "v_accvgpr_write_b32 a28, v52\n"
    "v_accvgpr_write_b32 a29, v53\n"
    "v_accvgpr_write_b32 a30, v54\n"
    "v_accvgpr_write_b32 a31, v55\n"
    "v_accvgpr_read_b32 v52, a48\n"
    "v_accvgpr_read_b32 v53, a49\n"
    "v_accvgpr_read_b32 v54, a50\n"
    "v_accvgpr_read_b32 v55, a51\n"
    "v_ldexp_f32 v52, v52, v51\n"
    "v_ldexp_f32 v53, v53, v51\n"
    "v_ldexp_f32 v54, v54, v51\n"
    "v_ldexp_f32 v55, v55, v51\n"
    "v_accvgpr_write_b32 a48, v52\n"
    "v_accvgpr_write_b32 a49, v53\n"
    "v_accvgpr_write_b32 a50, v54\n"
    "v_accvgpr_write_b32 a51, v55\n"
    "v_accvgpr_read_b32 v52, a52\n"
    "v_accvgpr_read_b32 v53, a53\n"
    "v_accvgpr_read_b32 v54, a54\n"
    "v_accvgpr_read_b32 v55, a55\n"
    "v_ldexp_f32 v52, v52, v51\n"
    "v_ldexp_f32 v53, v53, v51\n"
    "v_ldexp_f32 v54, v54, v51\n"
    "v_ldexp_f32 v55, v55, v51\n"
    "v_accvgpr_write_b32 a52, v52\n"
    "v_accvgpr_write_b32 a53, v53\n"
    "v_accvgpr_write_b32 a54, v54\n"
    "v_accvgpr_write_b32 a55, v55\n"
    "v_accvgpr_read_b32 v52, a56\n"
    "v_accvgpr_read_b32 v53, a57\n"
    "v_accvgpr_read_b32 v54, a58\n"
    "v_accvgpr_read_b32 v55, a59\n"
    "v_ldexp_f32 v52, v52, v51\n"
    "v_ldexp_f32 v53, v53, v51\n"
    "v_ldexp_f32 v54, v54, v51\n"
    "v_ldexp_f32 v55, v55, v51\n"
    "v_accvgpr_write_b32 a56, v52\n"
    "v_accvgpr_write_b32 a57, v53\n"
    "v_accvgpr_write_b32 a58, v54\n"
    "v_accvgpr_write_b32 a59, v55\n"
    "v_accvgpr_read_b32 v52, a60\n"
    "v_accvgpr_read_b32 v53, a61\n"
    "v_accvgpr_read_b32 v54, a62\n"
    "v_accvgpr_read_b32 v55, a63\n"
    "v_ldexp_f32 v52, v52, v51\n"
    "v_ldexp_f32 v53, v53, v51\n"
    "v_ldexp_f32 v54, v54, v51\n"
    "v_ldexp_f32 v55, v55, v51\n"
    "v_accvgpr_write_b32 a60, v52\n"
    "v_accvgpr_write_b32 a61, v53\n"
    "v_accvgpr_write_b32 a62, v54\n"
    "v_accvgpr_write_b32 a63, v55\n"
    "v_accvgpr_read_b32 v52, a80\n"
    "v_accvgpr_read_b32 v53, a81\n"
    "v_accvgpr_read_b32 v54, a82\n"
    "v_accvgpr_read_b32 v55, a83\n"
    "v_ldexp_f32 v52, v52, v51\n"
    "v_ldexp_f32 v53, v53, v51\n"
    "v_ldexp_f32 v54, v54, v51\n"
    "v_ldexp_f32 v55, v55, v51\n"
    "v_accvgpr_write_b32 a80, v52\n"
    "v_accvgpr_write_b32 a81, v53\n"
    "v_accvgpr_write_b32 a82, v54\n"
    "v_accvgpr_write_b32 a83, v55\n"
    "v_accvgpr_read_b32 v52, a84\n"
    "v_accvgpr_read_b32 v53, a85\n"
    "v_accvgpr_read_b32 v54, a86\n"
    "v_accvgpr_read_b32 v55, a87\n"
    "v_ldexp_f32 v52, v52, v51\n"
    "v_ldexp_f32 v53, v53, v51\n"
    "v_ldexp_f32 v54, v54, v51\n"
    "v_ldexp_f32 v55, v55, v51\n"
    "v_accvgpr_write_b32 a84, v52\n"
    "v_accvgpr_write_b32 a85, v53\n"
    "v_accvgpr_write_b32 a86, v54\n"
    "v_accvgpr_write_b32 a87, v55\n"
    "v_accvgpr_read_b32 v52, a88\n"
    "v_accvgpr_read_b32 v53, a89\n"
    "v_accvgpr_read_b32 v54, a90\n"
    "v_accvgpr_read_b32 v55, a91\n"
    "v_ldexp_f32 v52, v52, v51\n"
    "v_ldexp_f32 v53, v53, v51\n"
    "v_ldexp_f32 v54, v54, v51\n"
    "v_ldexp_f32 v55, v55, v51\n"
    "v_accvgpr_write_b32 a88, v52\n"
    "v_accvgpr_write_b32 a89, v53\n"
    "v_accvgpr_write_b32 a90, v54\n"
    "v_accvgpr_write_b32 a91, v55\n"
    "v_accvgpr_read_b32 v52, a92\n"
    "v_accvgpr_read_b32 v53, a93\n"
    "v_accvgpr_read_b32 v54, a94\n"
    "v_accvgpr_read_b32 v55, a95\n"
    "v_ldexp_f32 v52, v52, v51\n"
    "v_ldexp_f32 v53, v53, v51\n"
    "v_ldexp_f32 v54, v54, v51\n"
    "v_ldexp_f32 v55, v55, v51\n"
    "v_accvgpr_write_b32 a92, v52\n"
    "v_accvgpr_write_b32 a93, v53\n"
    "v_accvgpr_write_b32 a94, v54\n"
    "v_accvgpr_write_b32 a95, v55\n"
    "v_accvgpr_read_b32 v52, a112\n"
    "v_accvgpr_read_b32 v53, a113\n"
    "v_accvgpr_read_b32 v54, a114\n"
    "v_accvgpr_read_b32 v55, a115\n"
    "v_ldexp_f32 v52, v52, v51\n"
    "v_ldexp_f32 v53, v53, v51\n"
    "v_ldexp_f32 v54, v54, v51\n"
    "v_ldexp_f32 v55, v55, v51\n"
    "v_accvgpr_write_b32 a112, v52\n"
    "v_accvgpr_write_b32 a113, v53\n"
    "v_accvgpr_write_b32 a114, v54\n"
    "v_accvgpr_write_b32 a115, v55\n"
    "v_accvgpr_read_b32 v52, a116\n"
    "v_accvgpr_read_b32 v53, a117\n"
    "v_accvgpr_read_b32 v54, a118\n"
    "v_accvgpr_read_b32 v55, a119\n"
    "v_ldexp_f32 v52, v52, v51\n"
    "v_ldexp_f32 v53, v53, v51\n"
    "v_ldexp_f32 v54, v54, v51\n"
    "v_ldexp_f32 v55, v55, v51\n"
    "v_accvgpr_write_b32 a116, v52\n"
    "v_accvgpr_write_b32 a117, v53\n"
    "v_accvgpr_write_b32 a118, v54\n"
    "v_accvgpr_write_b32 a119, v55\n"
    "v_accvgpr_read_b32 v52, a120\n"
    "v_accvgpr_read_b32 v53, a121\n"
    "v_accvgpr_read_b32 v54, a122\n"
    "v_accvgpr_read_b32 v55, a123\n"
    "v_ldexp_f32 v52, v52, v51\n"
    "v_ldexp_f32 v53, v53, v51\n"
    "v_ldexp_f32 v54, v54, v51\n"
    "v_ldexp_f32 v55, v55, v51\n"
    "v_accvgpr_write_b32 a120, v52\n"
    "v_accvgpr_write_b32 a121, v53\n"
    "v_accvgpr_write_b32 a122, v54\n"
    "v_accvgpr_write_b32 a123, v55\n"
    "v_accvgpr_read_b32 v52, a124\n"
    "v_accvgpr_read_b32 v53, a125\n"
    "v_accvgpr_read_b32 v54, a126\n"
    "v_accvgpr_read_b32 v55, a127\n"
    "v_ldexp_f32 v52, v52, v51\n"
    "v_ldexp_f32 v53, v53, v51\n"
    "v_ldexp_f32 v54, v54, v51\n"
    "v_ldexp_f32 v55, v55, v51\n"
    "v_accvgpr_write_b32 a124, v52\n"
    "v_accvgpr_write_b32 a125, v53\n"
    "v_accvgpr_write_b32 a126, v54\n"
    "v_accvgpr_write_b32 a127, v55\n"
    "v_ldexp_f32 v17, v17, v51\n"
    "v_sub_f32 v144, v144, v50\n"
    "v_sub_f32 v176, v176, v50\n"
    "v_sub_f32 v240, v240, v50\n"
    "v_sub_f32 v145, v145, v50\n"
    "v_sub_f32 v177, v177, v50\n"
    "v_sub_f32 v241, v241, v50\n"
    "v_sub_f32 v146, v146, v50\n"
    "v_sub_f32 v178, v178, v50\n"
    "v_sub_f32 v242, v242, v50\n"
    "v_sub_f32 v147, v147, v50\n"
    "v_sub_f32 v179, v179, v50\n"
    "v_sub_f32 v243, v243, v50\n"
    "v_sub_f32 v148, v148, v50\n"
    "v_sub_f32 v180, v180, v50\n"
    "v_sub_f32 v244, v244, v50\n"
    "v_sub_f32 v149, v149, v50\n"
    "v_sub_f32 v181, v181, v50\n"
    "v_sub_f32 v245, v245, v50\n"
    "v_sub_f32 v150, v150, v50\n"
    "v_sub_f32 v182, v182, v50\n"
    "v_sub_f32 v246, v246, v50\n"
    "v_sub_f32 v151, v151, v50\n"
    "v_sub_f32 v183, v183, v50\n"
    "v_sub_f32 v247, v247, v50\n"
    "v_sub_f32 v152, v152, v50\n"
    "v_sub_f32 v184, v184, v50\n"
    "v_sub_f32 v248, v248, v50\n"
    "v_sub_f32 v153, v153, v50\n"
    "v_sub_f32 v185, v185, v50\n"
    "v_sub_f32 v249, v249, v50\n"
    "v_sub_f32 v154, v154, v50\n"
    "v_sub_f32 v186, v186, v50\n"
    "v_sub_f32 v250, v250, v50\n"
    "v_sub_f32 v155, v155, v50\n"
    "v_sub_f32 v187, v187, v50\n"
    "v_sub_f32 v251, v251, v50\n"
    "v_sub_f32 v156, v156, v50\n"
    "v_sub_f32 v188, v188, v50\n"
    "v_sub_f32 v252, v252, v50\n"
    "v_sub_f32 v157, v157, v50\n"
    "v_sub_f32 v189, v189, v50\n"
    "v_sub_f32 v253, v253, v50\n"
    "v_sub_f32 v158, v158, v50\n"
    "v_sub_f32 v190, v190, v50\n"
    "v_sub_f32 v254, v254, v50\n"
    "v_sub_f32 v159, v159, v50\n"
    "v_sub_f32 v191, v191, v50\n"
    "v_sub_f32 v255, v255, v50\n"
    "v_exp_f32 v32, v144\n"
    "v_exp_f32 v33, v145\n"
    "v_exp_f32 v34, v146\n"
    "v_exp_f32 v35, v147\n"
    "v_exp_f32 v36, v148\n"
    "v_exp_f32 v37, v149\n"
    "v_exp_f32 v38, v150\n"
    "v_exp_f32 v39, v151\n"
    "v_exp_f32 v40, v152\n"
    "v_exp_f32 v41, v153\n"
    "v_exp_f32 v42, v154\n"
    "v_exp_f32 v43, v155\n"
    "v_exp_f32 v44, v156\n"
    "v_exp_f32 v45, v157\n"
    "v_exp_f32 v46, v158\n"
    "v_exp_f32 v47, v159\n"
    "s_nop 0\n"
    "v_add_f32 v19, v32, v33\n"
    "v_add_f32 v19, v19, v34\n"
    "v_add_f32 v19, v19, v35\n"
    "v_add_f32 v19, v19, v36\n"
    "v_add_f32 v19, v19, v37\n"
    "v_add_f32 v19, v19, v38\n"
    "v_add_f32 v19, v19, v39\n"
    "v_add_f32 v19, v19, v40\n"
    "v_add_f32 v19, v19, v41\n"
    "v_add_f32 v19, v19, v42\n"
    "v_add_f32 v19, v19, v43\n"
    "v_add_f32 v19, v19, v44\n"
    "v_add_f32 v19, v19, v45\n"
    "v_add_f32 v19, v19, v46\n"
    "v_add_f32 v19, v19, v47\n"
    "v_cvt_pk_bf16_f32 v200, v32, v33\n"
    "v_cvt_pk_bf16_f32 v201, v34, v35\n"
    "v_cvt_pk_bf16_f32 v202, v36, v37\n"
    "v_cvt_pk_bf16_f32 v203, v38, v39\n"
    "v_cvt_pk_bf16_f32 v204, v40, v41\n"
    "v_cvt_pk_bf16_f32 v205, v42, v43\n"
    "v_cvt_pk_bf16_f32 v206, v44, v45\n"
    "v_cvt_pk_bf16_f32 v207, v46, v47\n"
    "s_nop 3\n"
    "s_setpc_b64 s[94:95]\n"
    ".Lfb_body1:\n"
    "s_nop 15\n"
    "s_nop 3\n"
    "v_max3_f32 v48, v160, v161, v162\n"
    "v_max3_f32 v49, v163, v164, v165\n"
    "v_max3_f32 v48, v48, v166, v167\n"
    "v_max3_f32 v49, v49, v168, v169\n"
    "v_max3_f32 v48, v48, v170, v171\n"
    "v_max3_f32 v49, v49, v172, v173\n"
    "v_max3_f32 v48, v48, v174, v175\n"
    "v_max_f32 v48, v48, v49\n"
    "v_mov_b32 v49, v48\n"
    "s_nop 1\n"
    "v_permlane32_swap_b32 v48, v49\n"
    "v_max_f32 v48, v48, v49\n"
    "v_ceil_f32 v50, v48\n"
    "v_add_f32 v50, 0x42cc0000, v50\n"
    "v_max_f32 v50, 0, v50\n"
    "v_cvt_i32_f32 v51, v50\n"
    "v_sub_u32 v51, 0, v51\n"
    "v_accvgpr_read_b32 v52, a0\n"
    "v_accvgpr_read_b32 v53, a1\n"
    "v_accvgpr_read_b32 v54, a2\n"
    "v_accvgpr_read_b32 v55, a3\n"
    "v_ldexp_f32 v52, v52, v51\n"
    "v_ldexp_f32 v53, v53, v51\n"
    "v_ldexp_f32 v54, v54, v51\n"
    "v_ldexp_f32 v55, v55, v51\n"
    "v_accvgpr_write_b32 a0, v52\n"
    "v_accvgpr_write_b32 a1, v53\n"
    "v_accvgpr_write_b32 a2, v54\n"
    "v_accvgpr_write_b32 a3, v55\n"
    "v_accvgpr_read_b32 v52, a4\n"
    "v_accvgpr_read_b32 v53, a5\n"
    "v_accvgpr_read_b32 v54, a6\n"
    "v_accvgpr_read_b32 v55, a7\n"
    "v_ldexp_f32 v52, v52, v51\n"
    "v_ldexp_f32 v53, v53, v51\n"
    "v_ldexp_f32 v54, v54, v51\n"
    "v_ldexp_f32 v55, v55, v51\n"
    "v_accvgpr_write_b32 a4, v52\n"
    "v_accvgpr_write_b32 a5, v53\n"
    "v_accvgpr_write_b32 a6, v54\n"
    "v_accvgpr_write_b32 a7, v55\n"
    "v_accvgpr_read_b32 v52, a8\n"
    "v_accvgpr_read_b32 v53, a9\n"
    "v_accvgpr_read_b32 v54, a10\n"
    "v_accvgpr_read_b32 v55, a11\n"
    "v_ldexp_f32 v52, v52, v51\n"
    "v_ldexp_f32 v53, v53, v51\n"
    "v_ldexp_f32 v54, v54, v51\n"
    "v_ldexp_f32 v55, v55, v51\n"
    "v_accvgpr_write_b32 a8, v52\n"
    "v_accvgpr_write_b32 a9, v53\n"
    "v_accvgpr_write_b32 a10, v54\n"
    "v_accvgpr_write_b32 a11, v55\n"
    "v_accvgpr_read_b32 v52, a12\n"
    "v_accvgpr_read_b32 v53, a13\n"
    "v_accvgpr_read_b32 v54, a14\n"
    "v_accvgpr_read_b32 v55, a15\n"
    "v_ldexp_f32 v52, v52, v51\n"
    "v_ldexp_f32 v53, v53, v51\n"
    "v_ldexp_f32 v54, v54, v51\n"
    "v_ldexp_f32 v55, v55, v51\n"
    "v_accvgpr_write_b32 a12, v52\n"
    "v_accvgpr_write_b32 a13, v53\n"
    "v_accvgpr_write_b32 a14, v54\n"
    "v_accvgpr_write_b32 a15, v55\n"
    "v_accvgpr_read_b32 v52, a32\n"
    "v_accvgpr_read_b32 v53, a33\n"
    "v_accvgpr_read_b32 v54, a34\n"
    "v_accvgpr_read_b32 v55, a35\n"
    "v_ldexp_f32 v52, v52, v51\n"
    "v_ldexp_f32 v53, v53, v51\n"
    "v_ldexp_f32 v54, v54, v51\n"
    "v_ldexp_f32 v55, v55, v51\n"
    "v_accvgpr_write_b32 a32, v52\n"
    "v_accvgpr_write_b32 a33, v53\n"
    "v_accvgpr_write_b32 a34, v54\n"
    "v_accvgpr_write_b32 a35, v55\n"
    "v_accvgpr_read_b32 v52, a36\n"
    "v_accvgpr_read_b32 v53, a37\n"
    "v_accvgpr_read_b32 v54, a38\n"
    "v_accvgpr_read_b32 v55, a39\n"
    "v_ldexp_f32 v52, v52, v51\n"
    "v_ldexp_f32 v53, v53, v51\n"
    "v_ldexp_f32 v54, v54, v51\n"
    "v_ldexp_f32 v55, v55, v51\n"
    "v_accvgpr_write_b32 a36, v52\n"
    "v_accvgpr_write_b32 a37, v53\n"
    "v_accvgpr_write_b32 a38, v54\n"
    "v_accvgpr_write_b32 a39, v55\n"
    "v_accvgpr_read_b32 v52, a40\n"
    "v_accvgpr_read_b32 v53, a41\n"
    "v_accvgpr_read_b32 v54, a42\n"
    "v_accvgpr_read_b32 v55, a43\n"
    "v_ldexp_f32 v52, v52, v51\n"
    "v_ldexp_f32 v53, v53, v51\n"
    "v_ldexp_f32 v54, v54, v51\n"
    "v_ldexp_f32 v55, v55, v51\n"
    "v_accvgpr_write_b32 a40, v52\n"
    "v_accvgpr_write_b32 a41, v53\n"
    "v_accvgpr_write_b32 a42, v54\n"
    "v_accvgpr_write_b32 a43, v55\n"
    "v_accvgpr_read_b32 v52, a44\n"
    "v_accvgpr_read_b32 v53, a45\n"
    "v_accvgpr_read_b32 v54, a46\n"
    "v_accvgpr_read_b32 v55, a47\n"
    "v_ldexp_f32 v52, v52, v51\n"
    "v_ldexp_f32 v53, v53, v51\n"
    "v_ldexp_f32 v54, v54, v51\n"
    "v_ldexp_f32 v55, v55, v51\n"
    "v_accvgpr_write_b32 a44, v52\n"
    "v_accvgpr_write_b32 a45, v53\n"
    "v_accvgpr_write_b32 a46, v54\n"
    "v_accvgpr_write_b32 a47, v55\n"
    "v_accvgpr_read_b32 v52, a64\n"
    "v_accvgpr_read_b32 v53, a65\n"
    "v_accvgpr_read_b32 v54, a66\n"
    "v_accvgpr_read_b32 v55, a67\n"
    "v_ldexp_f32 v52, v52, v51\n"
    "v_ldexp_f32 v53, v53, v51\n"
    "v_ldexp_f32 v54, v54, v51\n"
    "v_ldexp_f32 v55, v55, v51\n"
    "v_accvgpr_write_b32 a64, v52\n"
    "v_accvgpr_write_b32 a65, v53\n"
    "v_accvgpr_write_b32 a66, v54\n"
    "v_accvgpr_write_b32 a67, v55\n"
    "v_accvgpr_read_b32 v52, a68\n"
    "v_accvgpr_read_b32 v53, a69\n"
    "v_accvgpr_read_b32 v54, a70\n"
    "v_accvgpr_read_b32 v55, a71\n"
    "v_ldexp_f32 v52, v52, v51\n"
    "v_ldexp_f32 v53, v53, v51\n"
    "v_ldexp_f32 v54, v54, v51\n"
    "v_ldexp_f32 v55, v55, v51\n"
    "v_accvgpr_write_b32 a68, v52\n"
    "v_accvgpr_write_b32 a69, v53\n"
    "v_accvgpr_write_b32 a70, v54\n"
    "v_accvgpr_write_b32 a71, v55\n"
    "v_accvgpr_read_b32 v52, a72\n"
    "v_accvgpr_read_b32 v53, a73\n"
    "v_accvgpr_read_b32 v54, a74\n"
    "v_accvgpr_read_b32 v55, a75\n"
    "v_ldexp_f32 v52, v52, v51\n"
    "v_ldexp_f32 v53, v53, v51\n"
    "v_ldexp_f32 v54, v54, v51\n"
    "v_ldexp_f32 v55, v55, v51\n"
    "v_accvgpr_write_b32 a72, v52\n"
    "v_accvgpr_write_b32 a73, v53\n"
    "v_accvgpr_write_b32 a74, v54\n"
    "v_accvgpr_write_b32 a75, v55\n"
    "v_accvgpr_read_b32 v52, a76\n"
    "v_accvgpr_read_b32 v53, a77\n"
    "v_accvgpr_read_b32 v54, a78\n"
    "v_accvgpr_read_b32 v55, a79\n"
    "v_ldexp_f32 v52, v52, v51\n"
    "v_ldexp_f32 v53, v53, v51\n"
    "v_ldexp_f32 v54, v54, v51\n"
    "v_ldexp_f32 v55, v55, v51\n"
    "v_accvgpr_write_b32 a76, v52\n"
    "v_accvgpr_write_b32 a77, v53\n"
    "v_accvgpr_write_b32 a78, v54\n"
    "v_accvgpr_write_b32 a79, v55\n"
    "v_accvgpr_read_b32 v52, a96\n"
    "v_accvgpr_read_b32 v53, a97\n"
    "v_accvgpr_read_b32 v54, a98\n"
    "v_accvgpr_read_b32 v55, a99\n"
    "v_ldexp_f32 v52, v52, v51\n"
    "v_ldexp_f32 v53, v53, v51\n"
    "v_ldexp_f32 v54, v54, v51\n"
    "v_ldexp_f32 v55, v55, v51\n"
    "v_accvgpr_write_b32 a96, v52\n"
    "v_accvgpr_write_b32 a97, v53\n"
    "v_accvgpr_write_b32 a98, v54\n"
    "v_accvgpr_write_b32 a99, v55\n"
    "v_accvgpr_read_b32 v52, a100\n"
    "v_accvgpr_read_b32 v53, a101\n"
    "v_accvgpr_read_b32 v54, a102\n"
    "v_accvgpr_read_b32 v55, a103\n"
    "v_ldexp_f32 v52, v52, v51\n"
    "v_ldexp_f32 v53, v53, v51\n"
    "v_ldexp_f32 v54, v54, v51\n"
    "v_ldexp_f32 v55, v55, v51\n"
    "v_accvgpr_write_b32 a100, v52\n"
    "v_accvgpr_write_b32 a101, v53\n"
    "v_accvgpr_write_b32 a102, v54\n"
    "v_accvgpr_write_b32 a103, v55\n"
    "v_accvgpr_read_b32 v52, a104\n"
    "v_accvgpr_read_b32 v53, a105\n"
    "v_accvgpr_read_b32 v54, a106\n"
    "v_accvgpr_read_b32 v55, a107\n"
    "v_ldexp_f32 v52, v52, v51\n"
    "v_ldexp_f32 v53, v53, v51\n"
    "v_ldexp_f32 v54, v54, v51\n"
    "v_ldexp_f32 v55, v55, v51\n"
    "v_accvgpr_write_b32 a104, v52\n"
    "v_accvgpr_write_b32 a105, v53\n"
    "v_accvgpr_write_b32 a106, v54\n"
    "v_accvgpr_write_b32 a107, v55\n"
    "v_accvgpr_read_b32 v52, a108\n"
    "v_accvgpr_read_b32 v53, a109\n"
    "v_accvgpr_read_b32 v54, a110\n"
    "v_accvgpr_read_b32 v55, a111\n"
    "v_ldexp_f32 v52, v52, v51\n"
    "v_ldexp_f32 v53, v53, v51\n"
    "v_ldexp_f32 v54, v54, v51\n"
    "v_ldexp_f32 v55, v55, v51\n"
    "v_accvgpr_write_b32 a108, v52\n"
    "v_accvgpr_write_b32 a109, v53\n"
    "v_accvgpr_write_b32 a110, v54\n"
    "v_accvgpr_write_b32 a111, v55\n"
    "v_ldexp_f32 v16, v16, v51\n"
    "v_sub_f32 v160, v160, v50\n"
    "v_sub_f32 v128, v128, v50\n"
    "v_sub_f32 v224, v224, v50\n"
    "v_sub_f32 v161, v161, v50\n"
    "v_sub_f32 v129, v129, v50\n"
    "v_sub_f32 v225, v225, v50\n"
    "v_sub_f32 v162, v162, v50\n"
    "v_sub_f32 v130, v130, v50\n"
    "v_sub_f32 v226, v226, v50\n"
    "v_sub_f32 v163, v163, v50\n"
    "v_sub_f32 v131, v131, v50\n"
    "v_sub_f32 v227, v227, v50\n"
    "v_sub_f32 v164, v164, v50\n"
    "v_sub_f32 v132, v132, v50\n"
    "v_sub_f32 v228, v228, v50\n"
    "v_sub_f32 v165, v165, v50\n"
    "v_sub_f32 v133, v133, v50\n"
    "v_sub_f32 v229, v229, v50\n"
    "v_sub_f32 v166, v166, v50\n"
    "v_sub_f32 v134, v134, v50\n"
    "v_sub_f32 v230, v230, v50\n"
    "v_sub_f32 v167, v167, v50\n"
    "v_sub_f32 v135, v135, v50\n"
    "v_sub_f32 v231, v231, v50\n"
    "v_sub_f32 v168, v168, v50\n"
    "v_sub_f32 v136, v136, v50\n"
    "v_sub_f32 v232, v232, v50\n"
    "v_sub_f32 v169, v169, v50\n"
    "v_sub_f32 v137, v137, v50\n"
    "v_sub_f32 v233, v233, v50\n"
    "v_sub_f32 v170, v170, v50\n"
    "v_sub_f32 v138, v138, v50\n"
    "v_sub_f32 v234, v234, v50\n"
    "v_sub_f32 v171, v171, v50\n"
    "v_sub_f32 v139, v139, v50\n"
    "v_sub_f32 v235, v235, v50\n"
    "v_sub_f32 v172, v172, v50\n"
    "v_sub_f32 v140, v140, v50\n"
    "v_sub_f32 v236, v236, v50\n"
    "v_sub_f32 v173, v173, v50\n"
    "v_sub_f32 v141, v141, v50\n"
    "v_sub_f32 v237, v237, v50\n"
    "v_sub_f32 v174, v174, v50\n"
    "v_sub_f32 v142, v142, v50\n"
    "v_sub_f32 v238, v238, v50\n"
    "v_sub_f32 v175, v175, v50\n"
    "v_sub_f32 v143, v143, v50\n"
    "v_sub_f32 v239, v239, v50\n"
    "v_exp_f32 v32, v160\n"
    "v_exp_f32 v33, v161\n"
    "v_exp_f32 v34, v162\n"
    "v_exp_f32 v35, v163\n"
    "v_exp_f32 v36, v164\n"
    "v_exp_f32 v37, v165\n"
    "v_exp_f32 v38, v166\n"
    "v_exp_f32 v39, v167\n"
    "v_exp_f32 v40, v168\n"
    "v_exp_f32 v41, v169\n"
    "v_exp_f32 v42, v170\n"
    "v_exp_f32 v43, v171\n"
    "v_exp_f32 v44, v172\n"
    "v_exp_f32 v45, v173\n"
    "v_exp_f32 v46, v174\n"
    "v_exp_f32 v47, v175\n"
    "s_nop 0\n"
    "v_add_f32 v18, v32, v33\n"
    "v_add_f32 v18, v18, v34\n"
    "v_add_f32 v18, v18, v35\n"
    "v_add_f32 v18, v18, v36\n"
    "v_add_f32 v18, v18, v37\n"
    "v_add_f32 v18, v18, v38\n"
    "v_add_f32 v18, v18, v39\n"
    "v_add_f32 v18, v18, v40\n"
    "v_add_f32 v18, v18, v41\n"
    "v_add_f32 v18, v18, v42\n"
    "v_add_f32 v18, v18, v43\n"
    "v_add_f32 v18, v18, v44\n"
    "v_add_f32 v18, v18, v45\n"
    "v_add_f32 v18, v18, v46\n"
    "v_add_f32 v18, v18, v47\n"
    "v_cvt_pk_bf16_f32 v208, v32, v33\n"
    "v_cvt_pk_bf16_f32 v209, v34, v35\n"
    "v_cvt_pk_bf16_f32 v210, v36, v37\n"
    "v_cvt_pk_bf16_f32 v211, v38, v39\n"
    "v_cvt_pk_bf16_f32 v212, v40, v41\n"
    "v_cvt_pk_bf16_f32 v213, v42, v43\n"
    "v_cvt_pk_bf16_f32 v214, v44, v45\n"
    "v_cvt_pk_bf16_f32 v215, v46, v47\n"
    "v_max3_f32 v48, v176, v177, v178\n"
    "v_max3_f32 v49, v179, v180, v181\n"
    "v_max3_f32 v48, v48, v182, v183\n"
    "v_max3_f32 v49, v49, v184, v185\n"
    "v_max3_f32 v48, v48, v186, v187\n"
    "v_max3_f32 v49, v49, v188, v189\n"
    "v_max3_f32 v48, v48, v190, v191\n"
    "v_max_f32 v48, v48, v49\n"
    "v_mov_b32 v49, v48\n"
    "s_nop 1\n"
    "v_permlane32_swap_b32 v48, v49\n"
    "v_max_f32 v48, v48, v49\n"
    "v_ceil_f32 v50, v48\n"
    "v_add_f32 v50, 0x42cc0000, v50\n"
    "v_max_f32 v50, 0, v50\n"
    "v_cvt_i32_f32 v51, v50\n"
    "v_sub_u32 v51, 0, v51\n"
    "v_accvgpr_read_b32 v52, a16\n"
    "v_accvgpr_read_b32 v53, a17\n"
    "v_accvgpr_read_b32 v54, a18\n"
    "v_accvgpr_read_b32 v55, a19\n"
    "v_ldexp_f32 v52, v52, v51\n"
    "v_ldexp_f32 v53, v53, v51\n"
    "v_ldexp_f32 v54, v54, v51\n"
    "v_ldexp_f32 v55, v55, v51\n"
    "v_accvgpr_write_b32 a16, v52\n"
    "v_accvgpr_write_b32 a17, v53\n"
    "v_accvgpr_write_b32 a18, v54\n"
    "v_accvgpr_write_b32 a19, v55\n"
    "v_accvgpr_read_b32 v52, a20\n"
    "v_accvgpr_read_b32 v53, a21\n"
    "v_accvgpr_read_b32 v54, a22\n"
    "v_accvgpr_read_b32 v55, a23\n"
    "v_ldexp_f32 v52, v52, v51\n"
    "v_ldexp_f32 v53, v53, v51\n"
    "v_ldexp_f32 v54, v54, v51\n"
    "v_ldexp_f32 v55, v55, v51\n"
    "v_accvgpr_write_b32 a20, v52\n"
    "v_accvgpr_write_b32 a21, v53\n"
    "v_accvgpr_write_b32 a22, v54\n"
    "v_accvgpr_write_b32 a23, v55\n"
    "v_accvgpr_read_b32 v52, a24\n"
    "v_accvgpr_read_b32 v53, a25\n"
    "v_accvgpr_read_b32 v54, a26\n"
    "v_accvgpr_read_b32 v55, a27\n"
    "v_ldexp_f32 v52, v52, v51\n"
    "v_ldexp_f32 v53, v53, v51\n"
    "v_ldexp_f32 v54, v54, v51\n"
    "v_ldexp_f32 v55, v55, v51\n"
    "v_accvgpr_write_b32 a24, v52\n"
    "v_accvgpr_write_b32 a25, v53\n"
    "v_accvgpr_write_b32 a26, v54\n"
    "v_accvgpr_write_b32 a27, v55\n"
    "v_accvgpr_read_b32 v52, a28\n"
    "v_accvgpr_read_b32 v53, a29\n"
    "v_accvgpr_read_b32 v54, a30\n"
    "v_accvgpr_read_b32 v55, a31\n"
    "v_ldexp_f32 v52, v52, v51\n"
    "v_ldexp_f32 v53, v53, v51\n"
    "v_ldexp_f32 v54, v54, v51\n"
    "v_ldexp_f32 v55, v55, v51\n"
    "v_accvgpr_write_b32 a28, v52\n"
    "v_accvgpr_write_b32 a29, v53\n"
    "v_accvgpr_write_b32 a30, v54\n"
    "v_accvgpr_write_b32 a31, v55\n"
    "v_accvgpr_read_b32 v52, a48\n"
    "v_accvgpr_read_b32 v53, a49\n"
    "v_accvgpr_read_b32 v54, a50\n"
    "v_accvgpr_read_b32 v55, a51\n"
    "v_ldexp_f32 v52, v52, v51\n"
    "v_ldexp_f32 v53, v53, v51\n"
    "v_ldexp_f32 v54, v54, v51\n"
    "v_ldexp_f32 v55, v55, v51\n"
    "v_accvgpr_write_b32 a48, v52\n"
    "v_accvgpr_write_b32 a49, v53\n"
    "v_accvgpr_write_b32 a50, v54\n"
    "v_accvgpr_write_b32 a51, v55\n"
    "v_accvgpr_read_b32 v52, a52\n"
    "v_accvgpr_read_b32 v53, a53\n"
    "v_accvgpr_read_b32 v54, a54\n"
    "v_accvgpr_read_b32 v55, a55\n"
    "v_ldexp_f32 v52, v52, v51\n"
    "v_ldexp_f32 v53, v53, v51\n"
    "v_ldexp_f32 v54, v54, v51\n"
    "v_ldexp_f32 v55, v55, v51\n"
    "v_accvgpr_write_b32 a52, v52\n"
    "v_accvgpr_write_b32 a53, v53\n"
    "v_accvgpr_write_b32 a54, v54\n"
    "v_accvgpr_write_b32 a55, v55\n"
    "v_accvgpr_read_b32 v52, a56\n"
    "v_accvgpr_read_b32 v53, a57\n"
    "v_accvgpr_read_b32 v54, a58\n"
    "v_accvgpr_read_b32 v55, a59\n"
    "v_ldexp_f32 v52, v52, v51\n"
    "v_ldexp_f32 v53, v53, v51\n"
    "v_ldexp_f32 v54, v54, v51\n"
    "v_ldexp_f32 v55, v55, v51\n"
    "v_accvgpr_write_b32 a56, v52\n"
    "v_accvgpr_write_b32 a57, v53\n"
    "v_accvgpr_write_b32 a58, v54\n"
    "v_accvgpr_write_b32 a59, v55\n"
    "v_accvgpr_read_b32 v52, a60\n"
    "v_accvgpr_read_b32 v53, a61\n"
    "v_accvgpr_read_b32 v54, a62\n"
    "v_accvgpr_read_b32 v55, a63\n"
    "v_ldexp_f32 v52, v52, v51\n"
    "v_ldexp_f32 v53, v53, v51\n"
    "v_ldexp_f32 v54, v54, v51\n"
    "v_ldexp_f32 v55, v55, v51\n"
    "v_accvgpr_write_b32 a60, v52\n"
    "v_accvgpr_write_b32 a61, v53\n"
    "v_accvgpr_write_b32 a62, v54\n"
    "v_accvgpr_write_b32 a63, v55\n"
    "v_accvgpr_read_b32 v52, a80\n"
    "v_accvgpr_read_b32 v53, a81\n"
    "v_accvgpr_read_b32 v54, a82\n"
    "v_accvgpr_read_b32 v55, a83\n"
    "v_ldexp_f32 v52, v52, v51\n"
    "v_ldexp_f32 v53, v53, v51\n"
    "v_ldexp_f32 v54, v54, v51\n"
    "v_ldexp_f32 v55, v55, v51\n"
    "v_accvgpr_write_b32 a80, v52\n"
    "v_accvgpr_write_b32 a81, v53\n"
    "v_accvgpr_write_b32 a82, v54\n"
    "v_accvgpr_write_b32 a83, v55\n"
    "v_accvgpr_read_b32 v52, a84\n"
    "v_accvgpr_read_b32 v53, a85\n"
    "v_accvgpr_read_b32 v54, a86\n"
    "v_accvgpr_read_b32 v55, a87\n"
    "v_ldexp_f32 v52, v52, v51\n"
    "v_ldexp_f32 v53, v53, v51\n"
    "v_ldexp_f32 v54, v54, v51\n"
    "v_ldexp_f32 v55, v55, v51\n"
    "v_accvgpr_write_b32 a84, v52\n"
    "v_accvgpr_write_b32 a85, v53\n"
    "v_accvgpr_write_b32 a86, v54\n"
    "v_accvgpr_write_b32 a87, v55\n"
    "v_accvgpr_read_b32 v52, a88\n"
    "v_accvgpr_read_b32 v53, a89\n"
    "v_accvgpr_read_b32 v54, a90\n"
    "v_accvgpr_read_b32 v55, a91\n"
    "v_ldexp_f32 v52, v52, v51\n"
    "v_ldexp_f32 v53, v53, v51\n"
    "v_ldexp_f32 v54, v54, v51\n"
    "v_ldexp_f32 v55, v55, v51\n"
    "v_accvgpr_write_b32 a88, v52\n"
    "v_accvgpr_write_b32 a89, v53\n"
    "v_accvgpr_write_b32 a90, v54\n"
    "v_accvgpr_write_b32 a91, v55\n"
    "v_accvgpr_read_b32 v52, a92\n"
    "v_accvgpr_read_b32 v53, a93\n"
    "v_accvgpr_read_b32 v54, a94\n"
    "v_accvgpr_read_b32 v55, a95\n"
    "v_ldexp_f32 v52, v52, v51\n"
    "v_ldexp_f32 v53, v53, v51\n"
    "v_ldexp_f32 v54, v54, v51\n"
    "v_ldexp_f32 v55, v55, v51\n"
    "v_accvgpr_write_b32 a92, v52\n"
    "v_accvgpr_write_b32 a93, v53\n"
    "v_accvgpr_write_b32 a94, v54\n"
    "v_accvgpr_write_b32 a95, v55\n"
    "v_accvgpr_read_b32 v52, a112\n"
    "v_accvgpr_read_b32 v53, a113\n"
    "v_accvgpr_read_b32 v54, a114\n"
    "v_accvgpr_read_b32 v55, a115\n"
    "v_ldexp_f32 v52, v52, v51\n"
    "v_ldexp_f32 v53, v53, v51\n"
    "v_ldexp_f32 v54, v54, v51\n"
    "v_ldexp_f32 v55, v55, v51\n"
    "v_accvgpr_write_b32 a112, v52\n"
    "v_accvgpr_write_b32 a113, v53\n"
    "v_accvgpr_write_b32 a114, v54\n"
    "v_accvgpr_write_b32 a115, v55\n"
    "v_accvgpr_read_b32 v52, a116\n"
    "v_accvgpr_read_b32 v53, a117\n"
    "v_accvgpr_read_b32 v54, a118\n"
    "v_accvgpr_read_b32 v55, a119\n"
    "v_ldexp_f32 v52, v52, v51\n"
    "v_ldexp_f32 v53, v53, v51\n"
    "v_ldexp_f32 v54, v54, v51\n"
    "v_ldexp_f32 v55, v55, v51\n"
    "v_accvgpr_write_b32 a116, v52\n"
    "v_accvgpr_write_b32 a117, v53\n"
    "v_accvgpr_write_b32 a118, v54\n"
    "v_accvgpr_write_b32 a119, v55\n"
    "v_accvgpr_read_b32 v52, a120\n"
    "v_accvgpr_read_b32 v53, a121\n"
    "v_accvgpr_read_b32 v54, a122\n"
    "v_accvgpr_read_b32 v55, a123\n"
    "v_ldexp_f32 v52, v52, v51\n"
    "v_ldexp_f32 v53, v53, v51\n"
    "v_ldexp_f32 v54, v54, v51\n"
    "v_ldexp_f32 v55, v55, v51\n"
    "v_accvgpr_write_b32 a120, v52\n"
    "v_accvgpr_write_b32 a121, v53\n"
    "v_accvgpr_write_b32 a122, v54\n"
    "v_accvgpr_write_b32 a123, v55\n"
    "v_accvgpr_read_b32 v52, a124\n"
    "v_accvgpr_read_b32 v53, a125\n"
    "v_accvgpr_read_b32 v54, a126\n"
    "v_accvgpr_read_b32 v55, a127\n"
    "v_ldexp_f32 v52, v52, v51\n"
    "v_ldexp_f32 v53, v53, v51\n"
    "v_ldexp_f32 v54, v54, v51\n"
    "v_ldexp_f32 v55, v55, v51\n"
    "v_accvgpr_write_b32 a124, v52\n"
    "v_accvgpr_write_b32 a125, v53\n"
    "v_accvgpr_write_b32 a126, v54\n"
    "v_accvgpr_write_b32 a127, v55\n"
    "v_ldexp_f32 v17, v17, v51\n"
    "v_sub_f32 v176, v176, v50\n"
    "v_sub_f32 v144, v144, v50\n"
    "v_sub_f32 v240, v240, v50\n"
    "v_sub_f32 v177, v177, v50\n"
    "v_sub_f32 v145, v145, v50\n"
    "v_sub_f32 v241, v241, v50\n"
    "v_sub_f32 v178, v178, v50\n"
    "v_sub_f32 v146, v146, v50\n"
    "v_sub_f32 v242, v242, v50\n"
    "v_sub_f32 v179, v179, v50\n"
    "v_sub_f32 v147, v147, v50\n"
    "v_sub_f32 v243, v243, v50\n"
    "v_sub_f32 v180, v180, v50\n"
    "v_sub_f32 v148, v148, v50\n"
    "v_sub_f32 v244, v244, v50\n"
    "v_sub_f32 v181, v181, v50\n"
    "v_sub_f32 v149, v149, v50\n"
    "v_sub_f32 v245, v245, v50\n"
    "v_sub_f32 v182, v182, v50\n"
    "v_sub_f32 v150, v150, v50\n"
    "v_sub_f32 v246, v246, v50\n"
    "v_sub_f32 v183, v183, v50\n"
    "v_sub_f32 v151, v151, v50\n"
    "v_sub_f32 v247, v247, v50\n"
    "v_sub_f32 v184, v184, v50\n"
    "v_sub_f32 v152, v152, v50\n"
    "v_sub_f32 v248, v248, v50\n"
    "v_sub_f32 v185, v185, v50\n"
    "v_sub_f32 v153, v153, v50\n"
    "v_sub_f32 v249, v249, v50\n"
    "v_sub_f32 v186, v186, v50\n"
    "v_sub_f32 v154, v154, v50\n"
    "v_sub_f32 v250, v250, v50\n"
    "v_sub_f32 v187, v187, v50\n"
    "v_sub_f32 v155, v155, v50\n"
    "v_sub_f32 v251, v251, v50\n"
    "v_sub_f32 v188, v188, v50\n"
    "v_sub_f32 v156, v156, v50\n"
    "v_sub_f32 v252, v252, v50\n"
    "v_sub_f32 v189, v189, v50\n"
    "v_sub_f32 v157, v157, v50\n"
    "v_sub_f32 v253, v253, v50\n"
    "v_sub_f32 v190, v190, v50\n"
    "v_sub_f32 v158, v158, v50\n"
    "v_sub_f32 v254, v254, v50\n"
    "v_sub_f32 v191, v191, v50\n"
    "v_sub_f32 v159, v159, v50\n"
    "v_sub_f32 v255, v255, v50\n"
    "v_exp_f32 v32, v176\n"
    "v_exp_f32 v33, v177\n"
    "v_exp_f32 v34, v178\n"
    "v_exp_f32 v35, v179\n"
    "v_exp_f32 v36, v180\n"
    "v_exp_f32 v37, v181\n"
    "v_exp_f32 v38, v182\n"
    "v_exp_f32 v39, v183\n"
    "v_exp_f32 v40, v184\n"
    "v_exp_f32 v41, v185\n"
    "v_exp_f32 v42, v186\n"
    "v_exp_f32 v43, v187\n"
    "v_exp_f32 v44, v188\n"
    "v_exp_f32 v45, v189\n"
    "v_exp_f32 v46, v190\n"
    "v_exp_f32 v47, v191\n"
    "s_nop 0\n"
    "v_add_f32 v19, v32, v33\n"
    "v_add_f32 v19, v19, v34\n"
    "v_add_f32 v19, v19, v35\n"
    "v_add_f32 v19, v19, v36\n"
    "v_add_f32 v19, v19, v37\n"
    "v_add_f32 v19, v19, v38\n"
    "v_add_f32 v19, v19, v39\n"
    "v_add_f32 v19, v19, v40\n"
    "v_add_f32 v19, v19, v41\n"
    "v_add_f32 v19, v19, v42\n"
    "v_add_f32 v19, v19, v43\n"
    "v_add_f32 v19, v19, v44\n"
    "v_add_f32 v19, v19, v45\n"
    "v_add_f32 v19, v19, v46\n"
    "v_add_f32 v19, v19, v47\n"
    "v_cvt_pk_bf16_f32 v216, v32, v33\n"
    "v_cvt_pk_bf16_f32 v217, v34, v35\n"
    "v_cvt_pk_bf16_f32 v218, v36, v37\n"
    "v_cvt_pk_bf16_f32 v219, v38, v39\n"
    "v_cvt_pk_bf16_f32 v220, v40, v41\n"
    "v_cvt_pk_bf16_f32 v221, v42, v43\n"
    "v_cvt_pk_bf16_f32 v222, v44, v45\n"
    "v_cvt_pk_bf16_f32 v223, v46, v47\n"
    "s_nop 3\n"
    "s_setpc_b64 s[94:95]\n"
    ".Lci6:\n"
    "s_mov_b32 s94, 0\n"
    ".Lci6p:\n"
    "global_load_dword v60, v56, s[86:87] sc1\n"
    "global_load_dword v61, v57, s[86:87] sc1\n"
    "global_load_dword v62, v58, s[86:87] sc1\n"
    "global_load_dword v63, v59, s[86:87] sc1\n"
    "s_waitcnt vmcnt(0)\n"
    "v_cmp_eq_u32 vcc, s88, v60\n"
    "v_cmp_eq_u32 s[84:85], s88, v61\n"
    "s_nop 1\n"
    "s_and_b64 vcc, vcc, s[84:85]\n"
    "v_cmp_eq_u32 s[84:85], s88, v62\n"
    "s_nop 1\n"
    "s_and_b64 vcc, vcc, s[84:85]\n"
    "v_cmp_eq_u32 s[84:85], s88, v63\n"
    "s_nop 1\n"
    "s_and_b64 vcc, vcc, s[84:85]\n"
    "s_nop 3\n"
    "s_cmp_eq_u64 vcc, -1\n"
    "s_cbranch_scc1 .Lci6pd\n"
    "s_add_u32 s94, s94, 1\n"
    "s_cmp_gt_u32 s94, 400000\n"
    "s_cbranch_scc1 .Lci6pd\n"
    "s_sleep 1\n"
    "s_branch .Lci6p\n"
    ".Lci6pd:\n"
    "s_branch .Lci6r\n"
    ".Lmega_code_end:\n"
    :: "s"(ws), "s"(wq), "s"(kc), "s"(vc), "s"(wo), "s"(w1), "s"(w3), "s"(w2), "s"(x), "s"(out), "s"(bid), "v"(tid), "s"(cvec), "s"(wmod)
    : "memory", "vcc", "scc", "m0", "v1", "v2", "v3", "v4", "v5", "v6", "v7", "v8", "v9", "v10", "v11", "v12", "v13", "v14", "v15", "v16", "v17", "v18", "v19", "v20", "v21", "v22", "v23", "v24", "v25", "v26", "v27", "v28", "v29", "v30", "v31", "v32", "v33", "v34", "v35", "v36", "v37", "v38", "v39", "v40", "v41", "v42", "v43", "v44", "v45", "v46", "v47", "v48", "v49", "v50", "v51", "v52", "v53", "v54", "v55", "v56", "v57", "v58", "v59", "v60", "v61", "v62", "v63", "v64", "v65", "v66", "v67", "v68", "v69", "v70", "v71", "v72", "v73", "v74", "v75", "v76", "v77", "v78", "v79", "v80", "v81", "v82", "v83", "v84", "v85", "v86", "v87", "v88", "v89", "v90", "v91", "v92", "v93", "v94", "v95", "v96", "v97", "v98", "v99", "v100", "v101", "v102", "v103", "v104", "v105", "v106", "v107", "v108", "v109", "v110", "v111", "v112", "v113", "v114", "v115", "v116", "v117", "v118", "v119", "v120", "v121", "v122", "v123", "v124", "v125", "v126", "v127", "v128", "v129", "v130", "v131", "v132", "v133", "v134", "v135", "v136", "v137", "v138", "v139", "v140", "v141", "v142", "v143", "v144", "v145", "v146", "v147", "v148", "v149", "v150", "v151", "v152", "v153", "v154", "v155", "v156", "v157", "v158", "v159", "v160", "v161", "v162", "v163", "v164", "v165", "v166", "v167", "v168", "v169", "v170", "v171", "v172", "v173", "v174", "v175", "v176", "v177", "v178", "v179", "v180", "v181", "v182", "v183", "v184", "v185", "v186", "v187", "v188", "v189", "v190", "v191", "v192", "v193", "v194", "v195", "v196", "v197", "v198", "v199", "v200", "v201", "v202", "v203", "v204", "v205", "v206", "v207", "v208", "v209", "v210", "v211", "v212", "v213", "v214", "v215", "v216", "v217", "v218", "v219", "v220", "v221", "v222", "v223", "v224", "v225", "v226", "v227", "v228", "v229", "v230", "v231", "v232", "v233", "v234", "v235", "v236", "v237", "v238", "v239", "v240", "v241", "v242", "v243", "v244", "v245", "v246", "v247", "v248", "v249", "v250", "v251", "v252", "v253", "v254", "v255", "a0", "a1", "a2", "a3", "a4", "a5", "a6", "a7", "a8", "a9", "a10", "a11", "a12", "a13", "a14", "a15", "a16", "a17", "a18", "a19", "a20", "a21", "a22", "a23", "a24", "a25", "a26", "a27", "a28", "a29", "a30", "a31", "a32", "a33", "a34", "a35", "a36", "a37", "a38", "a39", "a40", "a41", "a42", "a43", "a44", "a45", "a46", "a47", "a48", "a49", "a50", "a51", "a52", "a53", "a54", "a55", "a56", "a57", "a58", "a59", "a60", "a61", "a62", "a63", "a64", "a65", "a66", "a67", "a68", "a69", "a70", "a71", "a72", "a73", "a74", "a75", "a76", "a77", "a78", "a79", "a80", "a81", "a82", "a83", "a84", "a85", "a86", "a87", "a88", "a89", "a90", "a91", "a92", "a93", "a94", "a95", "a96", "a97", "a98", "a99", "a100", "a101", "a102", "a103", "a104", "a105", "a106", "a107", "a108", "a109", "a110", "a111", "a112", "a113", "a114", "a115", "a116", "a117", "a118", "a119", "a120", "a121", "a122", "a123", "a124", "a125", "a126", "a127", "a128", "a129", "a130", "a131", "a132", "a133", "a134", "a135", "a136", "a137", "a138", "a139", "a140", "a141", "a142", "a143", "a144", "a145", "a146", "a147", "a148", "a149", "a150", "a151", "a152", "a153", "a154", "a155", "a156", "a157", "a158", "a159", "a160", "a161", "a162", "a163", "a164", "a165", "a166", "a167", "a168", "a169", "a170", "a171", "a172", "a173", "a174", "a175", "a176", "a177", "a178", "a179", "a180", "a181", "a182", "a183", "a184", "a185", "a186", "a187", "a188", "a189", "a190", "a191", "a192", "a193", "a194", "a195", "a196", "a197", "a198", "a199", "a200", "a201", "a202", "a203", "a204", "a205", "a206", "a207", "a208", "a209", "a210", "a211", "a212", "a213", "a214", "a215", "a216", "a217", "a218", "a219", "a220", "a221", "a222", "a223", "a224", "a225", "a226", "a227", "a228", "a229", "a230", "a231", "a232", "a233", "a234", "a235", "a236", "a237", "a238", "a239", "a240", "a241", "a242", "a243", "a244", "a245", "a246", "a247", "a248", "a249", "a250", "a251", "a252", "a253", "a254", "a255", "s40", "s41", "s42", "s43", "s44", "s45", "s46", "s47", "s48", "s49", "s50", "s51", "s52", "s53", "s54", "s55", "s56", "s57", "s58", "s59", "s60", "s61", "s62", "s63", "s64", "s65", "s66", "s67", "s68", "s69", "s70", "s71", "s72", "s73", "s74", "s75", "s76", "s77", "s78", "s79", "s80", "s81", "s82", "s83", "s84", "s85", "s86", "s87", "s88", "s89", "s90", "s91", "s92", "s93", "s94", "s95", "s96", "s97", "s98", "s99");
}

extern "C" void kernel_launch(void* const* d_in, const int* in_sizes, int n_in, void* d_out, int out_size, void* d_ws, size_t ws_size, hipStream_t stream) {
  (void)hipFuncSetAttribute((const void*)k_mega, hipFuncAttributeMaxDynamicSharedMemorySize, 163840);
  k_mega<<<256, 256, 163840, stream>>>(d_ws, d_in[3], d_in[1], d_in[2], d_in[4], d_in[6], d_in[7], d_in[8], d_in[0], d_out, d_in[9], d_in[10]);
}
```
